```python
import torch

def _softmax(x, axis=-1):
    return torch.softmax(x, dim=axis)
def _gelu(x):
    return torch.nn.functional.gelu(x, approximate="tanh")
def _silu(x):
    return torch.nn.functional.silu(x)
def _sigmoid(x):
    return torch.sigmoid(x)
def _layernorm(x, axis=-1, eps=1e-5):
    m = x.mean(dim=axis, keepdim=True)
    v = x.var(dim=axis, keepdim=True, unbiased=False)
    return (x - m) / torch.sqrt(v + eps)
def _scatter_add(tgt, idx, src, axis=0):
    return tgt.clone().index_add_(axis, idx, src)
def _segment_sum(src, seg, n):
    out = torch.zeros((n,) + src.shape[1:], dtype=src.dtype)
    return out.index_add_(0, seg, src)
def _pad(x, pw):
    flat = tuple(v for pair in reversed(pw) for v in pair)
    return torch.nn.functional.pad(x, flat)

kernel_name = 'recurrent_scan__sliding_block_cached__dense_activation_squared_relu__prompt'

def setup_inputs():

    _w = (lambda a: (lambda t: t.to(torch.bfloat16) if t.is_floating_point() else t)(torch.as_tensor(a)))
    def _A_setup():
        B, Sq, D = 1, 2048, 512
        Hq, Hkv, Sk, Dh = 32, 32, 2048, 128
        assert Hq % Hkv == 0, (Hq, Hkv)
        return {
            "x": _w((lambda *s: torch.randn(*s, dtype=torch.bfloat16) * 0.05)(B, Sq, D)),
            "k_cache": _w((lambda *s: torch.randn(*s, dtype=torch.bfloat16) * 0.8)(B, Hkv, Sk, Dh)),
            "v_cache": _w((lambda *s: torch.randn(*s, dtype=torch.bfloat16) * 0.2)(B, Hkv, Sk, Dh)),
            "wq": _w((lambda *s: torch.randn(*s, dtype=torch.bfloat16) * 0.8)(D, Hq * Dh) * (D ** -0.5)),
            "wo": _w((lambda *s: torch.randn(*s, dtype=torch.bfloat16) * 0.2)(Hq * Dh, D) * ((Hq * Dh) ** -0.5)),
        }
    _A_in = _A_setup()

    def _B_setup():
        B, S, D, F = 1, 2048, 512, 1376
        return {
            "x": _w((lambda *s: torch.randn(*s, dtype=torch.bfloat16) * 0.05)(B, S, D)),
            "w1": _w((lambda *s: torch.randn(*s, dtype=torch.bfloat16) * 0.1)(D, F)),
            "b1": _w((lambda *s: torch.randn(*s, dtype=torch.bfloat16) * 0.8)(F)),
            "w2": _w((lambda *s: torch.randn(*s, dtype=torch.bfloat16) * 0.1)(F, D)),
            "b2": _w((lambda *s: torch.randn(*s, dtype=torch.bfloat16) * 6.4)(D)),
        }
    _B_in = _B_setup()

    return {
        "A_x": _A_in["x"],
        "A_k_cache": _A_in["k_cache"],
        "A_v_cache": _A_in["v_cache"],
        "A_wq": _A_in["wq"],
        "A_wo": _A_in["wo"],
        "B_x": _B_in["x"],
        "B_w1": _B_in["w1"],
        "B_b1": _B_in["b1"],
        "B_w2": _B_in["w2"],
        "B_b2": _B_in["b2"],
    }

def reference(A_x, A_k_cache, A_v_cache, A_wq, A_wo, B_x, B_w1, B_b1, B_w2, B_b2):
    def _A_ref(x, k_cache, v_cache, wq, wo):
        B, Sq, D = x.shape
        Hq, Dh = 32, 128
        Hkv, Sk = k_cache.shape[1], k_cache.shape[2]
        W = min(4096, max(256, Sk // 4))
        rep = Hq // Hkv
        q = (lambda x, *d: x.permute(*d))((x @ wq).reshape(B, Sq, Hq, Dh), 0, 2, 1, 3)
        k = (lambda x, n, axis: torch.repeat_interleave(x, n, dim=axis))(k_cache[:, :, Sk - W:, :], rep, axis=1)
        v = (lambda x, n, axis: torch.repeat_interleave(x, n, dim=axis))(v_cache[:, :, Sk - W:, :], rep, axis=1)
        scores = torch.einsum("bhqd,bhkd->bhqk", q, k) * (Dh ** -0.5)
        p = _softmax(scores, axis=-1)
        o = torch.einsum("bhqk,bhkd->bhqd", p, v)
        return (lambda x, *d: x.permute(*d))(o, 0, 2, 1, 3).reshape(B, Sq, Hq * Dh) @ wo

    def _B_ref(x, w1, b1, w2, b2):
        h = torch.einsum("bsd,df->bsf", x, w1) + b1
        zero = torch.zeros_like(h)
        r = torch.where(h > zero, h, zero)
        h = r * r
        out = torch.einsum("bsf,fd->bsd", h, w2) + b2
        return out

    x = A_x
    xn = _layernorm(x, axis=-1)
    s = _A_ref(xn, A_k_cache, A_v_cache, A_wq, A_wo)
    g = _silu(_B_ref(xn, B_w1, B_b1, B_w2, B_b2))
    return x + s * g
```

```cpp
#include <hip/hip_runtime.h>
#include <hip/hip_bf16.h>
typedef float f32x16 __attribute__((ext_vector_type(16)));
typedef __bf16 bf16x8 __attribute__((ext_vector_type(8)));
typedef unsigned short u16;
__device__ inline float bf2f(u16 v){ return __uint_as_float(((unsigned)v)<<16); }
__device__ inline u16 f2bf(float f){ __bf16 b=(__bf16)f; return __builtin_bit_cast(u16,b); }

__global__ void ln_k(const u16* x, u16* xn){
  int row=blockIdx.x*4+(threadIdx.x>>6), l=threadIdx.x&63;
  float v[8]; float s=0;
  for(int j=0;j<8;j++){ v[j]=bf2f(x[row*512+l*8+j]); s+=v[j]; }
  for(int o=32;o;o>>=1) s+=__shfl_xor(s,o);
  float m=s/512; float q=0;
  for(int j=0;j<8;j++){ float d=v[j]-m; q+=d*d; }
  for(int o=32;o;o>>=1) q+=__shfl_xor(q,o);
  float r=rsqrtf(q/512+1e-5f);
  for(int j=0;j<8;j++) xn[row*512+l*8+j]=f2bf((v[j]-m)*r);
}
__global__ void gemm_k(const u16* A,int lda,const u16* B,int ldb,int K,int mode,const u16* bias,u16* C,int ldc,float* F,const u16* X){
  int l=threadIdx.x, r=l&31,h=l>>5; int m0=blockIdx.y*32,n0=blockIdx.x*32;
  f32x16 acc={0};
  for(int k=0;k<K;k+=16){
    bf16x8 a=*(const bf16x8*)(A+(size_t)(m0+r)*lda+k+8*h); bf16x8 b;
    for(int j=0;j<8;j++) b[j]=__builtin_bit_cast(__bf16,B[(size_t)(k+8*h+j)*ldb+n0+r]);
    acc=__builtin_amdgcn_mfma_f32_32x32x16_bf16(a,b,acc,0,0,0);
  }
  for(int i=0;i<16;i++){ int row=m0+(i&3)+8*(i>>2)+4*h, col=n0+r; float c=acc[i];
    if(mode==0) C[(size_t)row*ldc+col]=f2bf(c);
    else if(mode==1){ c+=bf2f(bias[col]); c=c>0?c:0; C[(size_t)row*ldc+col]=f2bf(c*c);}
    else if(mode==2){ F[(size_t)row*ldc+col]=c+bf2f(bias[col]); }
    else { float f=F[(size_t)row*ldc+col]; float g=f/(1.f+__expf(-f)); C[(size_t)row*ldc+col]=f2bf(bf2f(X[(size_t)row*ldc+col])+c*g);} }
}
__global__ __launch_bounds__(64) void attn_k(const u16* Q,const u16* Kc,const u16* Vc,u16* O){
  int l=threadIdx.x,r=l&31,h=l>>5; int m0=blockIdx.x*32, hd=blockIdx.y;
  const u16* Kp=Kc+((size_t)hd*2048+1536)*128; const u16* Vp=Vc+((size_t)hd*2048+1536)*128;
  bf16x8 q[8]; _Pragma("unroll") for(int s=0;s<8;s++) q[s]=*(const bf16x8*)(Q+(size_t)(m0+r)*4096+hd*128+s*16+8*h);
  f32x16 o[4]; _Pragma("unroll") for(int d=0;d<4;d++) o[d]=f32x16{0};
  float mx=-1e30f, ls=0; const float c=0.12751743082f;
  for(int t=0;t<16;t++){
    f32x16 s={0};
    _Pragma("unroll") for(int k=0;k<8;k++){ bf16x8 a=*(const bf16x8*)(Kp+(size_t)(t*32+r)*128+k*16+8*h); s=__builtin_amdgcn_mfma_f32_32x32x16_bf16(a,q[k],s,0,0,0);}
    float tm=-1e30f; _Pragma("unroll") for(int i=0;i<16;i++) tm=fmaxf(tm,s[i]*c); tm=fmaxf(tm,__shfl_xor(tm,32));
    float nm=fmaxf(mx,tm), f=exp2f(mx-nm); mx=nm; ls*=f; _Pragma("unroll") for(int d=0;d<4;d++) _Pragma("unroll") for(int i=0;i<16;i++) o[d][i]*=f;
    float p[16]; _Pragma("unroll") for(int i=0;i<16;i++){ p[i]=exp2f(s[i]*c-mx); ls+=p[i]; }
    _Pragma("unroll") for(int ks=0;ks<2;ks++){ bf16x8 pb; _Pragma("unroll") for(int j=0;j<8;j++) pb[j]=(__bf16)p[8*ks+j];
      _Pragma("unroll") for(int d=0;d<4;d++){ bf16x8 a; _Pragma("unroll") for(int j=0;j<8;j++){ int key=t*32+16*ks+8*(j>>2)+4*h+(j&3); a[j]=__builtin_bit_cast(__bf16,Vp[(size_t)key*128+d*32+r]); }
        o[d]=__builtin_amdgcn_mfma_f32_32x32x16_bf16(a,pb,o[d],0,0,0);} }
  }
  ls+=__shfl_xor(ls,32); float inv=1.f/ls;
  _Pragma("unroll") for(int d=0;d<4;d++) _Pragma("unroll") for(int i=0;i<16;i++){ int dd=d*32+(i&3)+8*(i>>2)+4*h; O[(size_t)(m0+r)*4096+hd*128+dd]=f2bf(o[d][i]*inv);}
}
extern "C" void kernel_launch(void* const* d_in, const int* in_sizes, int n_in, void* d_out, int out_size, void* d_ws, size_t ws_size, hipStream_t stream){
  const u16 *x=(const u16*)d_in[0],*kc=(const u16*)d_in[1],*vc=(const u16*)d_in[2],*wq=(const u16*)d_in[3],*wo=(const u16*)d_in[4],*w1=(const u16*)d_in[6],*b1=(const u16*)d_in[7],*w2=(const u16*)d_in[8],*b2=(const u16*)d_in[9];
  char* ws=(char*)d_ws; u16* xn=(u16*)ws; u16* Q=(u16*)(ws+(4<<20)); u16* O=(u16*)(ws+(24<<20)); u16* H=(u16*)(ws+(44<<20)); float* F=(float*)(ws+(52<<20));
  ln_k<<<512,256,0,stream>>>(x,xn);
  gemm_k<<<dim3(128,64),64,0,stream>>>(xn,512,wq,4096,512,0,nullptr,Q,4096,nullptr,nullptr);
  gemm_k<<<dim3(43,64),64,0,stream>>>(xn,512,w1,1376,512,1,b1,H,1376,nullptr,nullptr);
  attn_k<<<dim3(64,32),64,0,stream>>>(Q,kc,vc,O);
  gemm_k<<<dim3(16,64),64,0,stream>>>(H,1376,w2,512,1376,2,b2,nullptr,512,F,nullptr);
  gemm_k<<<dim3(16,64),64,0,stream>>>(O,4096,wo,512,4096,3,nullptr,(u16*)d_out,512,F,x);
}
```

```cpp
#include <hip/hip_runtime.h>
#include <hip/hip_bf16.h>
__global__ __launch_bounds__(256,1) void k_fused(void* ws,const void* x,const void* wq,const void* w1,const void* b1,const void* kc,const void* vc,const void* wo,const void* w2,const void* b2,void* out){
  __shared__ __attribute__((aligned(16))) char lds[163840];
  asm volatile(
"s_load_dwordx16 s[4:19], %0, 0x0\n"
"s_load_dwordx4 s[20:23], %0, 0x40\n"
"s_load_dwordx2 s[24:25], %0, 0x50\n"
"v_and_b32 v64, 63, %2\n"
"v_lshrrev_b32 v0, 6, %2\n"
"s_nop 0\n"
"v_readfirstlane_b32 s38, v0\n"
"s_getpc_b64 s[64:65]\n"
"PC0_%=:\n"
"s_mov_b32 s66, END_%=-PC0_%=\n"
"s_mov_b32 s67, 0x20000\n"
"s_lshr_b32 s98, %1, 3\n"
"s_lshl_b32 s98, s98, 2\n"
"s_add_u32 s98, s98, s38\n"
"s_mul_i32 s98, s98, 640\n"
"v_lshlrev_b32 v95, 7, v64\n"
"s_mov_b64 exec, 31\n"
"buffer_load_dword v96, v95, s[64:67], s98 offen\n"
"s_mov_b64 exec, -1\n"
"v_bfe_u32 v65, v64, 2, 3\n"
"v_and_b32 v66, 3, v64\n"
"v_lshrrev_b32 v67, 5, v64\n"
"v_lshrrev_b32 v68, 4, v64\n"
"v_and_b32 v69, 15, v64\n"
"v_lshrrev_b32 v70, 2, v65\n"
"v_lshlrev_b32 v251, 4, v64\n"
"v_and_b32 v72, 31, v64\n"
"v_lshlrev_b32 v73, 4, v72\n"
"v_lshl_add_u32 v73, v67, 10, v73\n"
"s_and_b32 s28, %1, 7\n"
"s_bfe_u32 s98, %1, 0x20003\n"
"s_lshl_b32 s26, s28, 2\n"
"s_add_u32 s26, s26, s98\n"
"s_lshr_b32 s27, %1, 5\n"
"s_lshl_b32 s98, s27, 5\n"
"s_add_u32 s98, s98, s26\n"
"s_lshl_b32 s98, s98, 2\n"
"s_add_u32 s98, s98, s38\n"
"s_lshl_b32 s29, s98, 11\n"
"s_waitcnt lgkmcnt(0)\n"
"s_mov_b32 s68, s6\n"
"s_mov_b32 s69, s7\n"
"s_mov_b32 s70, 2097152\n"
"s_mov_b32 s71, 0x20000\n"
"buffer_load_dwordx4 v[0:3], v73, s[68:71], s29 offen\n"
"buffer_load_dwordx4 v[4:7], v73, s[68:71], s29 offen offset:512\n"
"s_mov_b32 s40, s4\n"
"s_mov_b32 s41, s5\n"
"s_mov_b32 s42, 67108864\n"
"s_mov_b32 s43, 0x20000\n"
"s_lshl_b32 s99, s27, 16\n"
"s_add_u32 s99, s99, 58720256\n"
"s_lshl_b32 s98, s26, 3\n"
"s_add_u32 s31, s99, s98\n"
"s_lshl_b32 s98, s26, 8\n"
"s_add_u32 s30, s99, s98\n"
"s_lshr_b32 s98, s30, 9\n"
"s_and_b32 s98, s98, 896\n"
"s_add_u32 s39, s98, 59768832\n"
"v_mov_b32 v74, 1\n"
"v_mov_b32 v75, 0\n"
"s_cmp_lg_u32 s38, 0\n"
"s_cbranch_scc1 TK1_%=\n"
"s_mov_b64 exec, 1\n"
"buffer_atomic_add v74, v75, s[40:43], s39 offen sc0\n"
"s_mov_b64 exec, -1\n"
"TK1_%=:\n"
"s_lshr_b32 s34, s38, 1\n"
"s_and_b32 s35, s38, 1\n"
"s_lshl_b32 s98, s34, 5\n"
"s_lshl_b32 s99, s35, 2\n"
"s_add_u32 s36, s98, s99\n"
"s_lshl_b32 s98, s36, 13\n"
"s_lshl_b32 s99, s26, 8\n"
"s_add_u32 s98, s98, s99\n"
"s_sub_u32 s99, 4194304, s98\n"
"s_add_u32 s44, s8, s98\n"
"s_addc_u32 s45, s9, 0\n"
"s_mov_b32 s46, s99\n"
"s_mov_b32 s47, 0x20000\n"
"v_and_b32 v76, 3, v65\n"
"v_lshl_add_u32 v76, v70, 3, v76\n"
"v_lshlrev_b32 v77, 4, v66\n"
"v_lshl_add_u32 v77, v67, 6, v77\n"
"v_lshl_add_u32 v247, v76, 13, v77\n"
"s_lshl_b32 s91, s38, 12\n"
"s_mov_b32 s84, 0\n"
"s_mov_b32 s85, 131072\n"
"s_mul_i32 s98, s36, 2752\n"
"s_sub_u32 s99, 1409024, s98\n"
"s_add_u32 s48, s10, s98\n"
"s_addc_u32 s49, s11, 0\n"
"s_mov_b32 s50, s99\n"
"s_mov_b32 s51, 0x20000\n"
"s_mov_b32 s76, s12\n"
"s_mov_b32 s77, s13\n"
"s_mov_b32 s78, 2752\n"
"s_mov_b32 s79, 0x20000\n"
"s_mov_b32 s6, s26\n"
"s_mov_b32 s7, s27\n"
"s_mov_b32 s12, s28\n"
"s_and_b32 s8, s6, 1\n"
"s_mov_b32 s10, s34\n"
"s_mov_b32 s11, s35\n"
"s_and_b32 s13, s6, 3\n"
"s_mul_i32 s98, s6, 3\n"
"s_lshr_b32 s98, s98, 1\n"
"s_add_u32 s34, s98, s8\n"
"s_add_u32 s35, s98, 1\n"
"s_sub_u32 s35, s35, s8\n"
"s_lshl_b32 s98, s34, 6\n"
"s_cmp_lt_u32 s34, 43\n"
"s_cselect_b32 s98, s98, 0x40000000\n"
"s_lshl_b32 s99, s35, 6\n"
"s_cmp_lt_u32 s35, 43\n"
"s_cselect_b32 s99, s99, 0x40000000\n"
"v_mov_b32 v78, s98\n"
"v_mov_b32 v79, s99\n"
"v_cmp_eq_u32 vcc, 0, v67\n"
"v_lshlrev_b32 v81, 1, v70\n"
"v_xor_b32 v81, v81, v66\n"
"v_cndmask_b32 v80, v79, v78, vcc\n"
"v_lshl_add_u32 v80, v81, 4, v80\n"
"s_movk_i32 s98, 2752\n"
"v_mad_u32_u24 v248, v76, s98, v80\n"
"s_lshl_b32 s92, s38, 11\n"
"s_add_u32 s92, s92, 65536\n"
"s_mov_b32 s86, 0\n"
"s_mov_b32 s87, 44032\n"
"v_lshrrev_b32 v82, 1, v64\n"
"v_and_b32 v83, 1, v64\n"
"v_cmp_eq_u32 vcc, 0, v83\n"
"v_lshlrev_b32 v83, 7, v83\n"
"v_lshl_add_u32 v94, v82, 13, v83\n"
"v_cndmask_b32 v83, v79, v78, vcc\n"
"s_movk_i32 s99, 2752\n"
"v_mad_u32_u24 v93, v82, s99, v83\n"
"s_lshl_b32 s98, s7, 6\n"
"s_mul_i32 s99, s11, 28\n"
"s_add_u32 s98, s98, s99\n"
"s_lshl_b32 s95, s98, 13\n"
"s_sub_u32 s98, s98, 32\n"
"s_max_i32 s98, s98, 0\n"
"s_mul_i32 s97, s98, 2752\n"
"s_cmp_eq_u32 s38, 0\n"
"s_cbranch_scc1 TW0_%=\n"
"s_cmp_eq_u32 s38, 1\n"
"s_cbranch_scc1 TW0_%=\n"
"buffer_load_dword v85, v93, s[48:51], s97 offen\n"
"s_branch TW1_%=\n"
"TW0_%=:\n"
"buffer_load_dword v85, v94, s[44:47], s95 offen\n"
"TW1_%=:\n"
"v_lshl_add_u32 v78, v68, 3, v78\n"
"v_lshl_add_u32 v79, v68, 3, v79\n"
"buffer_load_dwordx2 v[144:145], v78, s[76:79], 0 offen\n"
"buffer_load_dwordx2 v[146:147], v78, s[76:79], 0 offen offset:32\n"
"buffer_load_dwordx2 v[148:149], v79, s[76:79], 0 offen\n"
"buffer_load_dwordx2 v[150:151], v79, s[76:79], 0 offen offset:32\n"
"v_lshlrev_b32 v89, 7, v72\n"
"v_lshl_add_u32 v89, v67, 4, v89\n"
"s_lshl_b32 s98, s38, 5\n"
"s_add_u32 s98, s98, 98304\n"
"v_add_u32 v89, s98, v89\n"
"s_lshl_b32 s98, s38, 11\n"
"s_add_u32 s98, s98, 98304\n"
"v_add_u32 v90, s98, v251\n"
"v_lshlrev_b32 v91, 3, v72\n"
"v_mov_b32 v92, 106496\n"
"s_lshl_b32 s98, s27, 4\n"
"s_lshr_b32 s99, s26, 1\n"
"s_add_u32 s98, s98, s99\n"
"s_lshl_b32 s98, s98, 2\n"
"s_add_u32 s98, s98, s38\n"
"s_lshl_b32 s98, s98, 12\n"
"s_and_b32 s99, s26, 1\n"
"s_lshl_b32 s99, s99, 7\n"
"s_add_u32 s29, s98, s99\n"
"v_and_b32 v83, 7, v64\n"
"v_lshrrev_b32 v84, 3, v64\n"
"v_lshlrev_b32 v84, 8, v84\n"
"v_lshl_add_u32 v88, v83, 4, v84\n"
"s_waitcnt vmcnt(6)\n"
"v_lshlrev_b32 v8, 16, v0\n"
"v_and_b32 v9, 0xffff0000, v0\n"
"v_lshlrev_b32 v10, 16, v1\n"
"v_and_b32 v11, 0xffff0000, v1\n"
"v_lshlrev_b32 v12, 16, v2\n"
"v_and_b32 v13, 0xffff0000, v2\n"
"v_lshlrev_b32 v14, 16, v3\n"
"v_and_b32 v15, 0xffff0000, v3\n"
"s_waitcnt vmcnt(5)\n"
"v_lshlrev_b32 v16, 16, v4\n"
"v_and_b32 v17, 0xffff0000, v4\n"
"v_lshlrev_b32 v18, 16, v5\n"
"v_and_b32 v19, 0xffff0000, v5\n"
"v_lshlrev_b32 v20, 16, v6\n"
"v_and_b32 v21, 0xffff0000, v6\n"
"v_lshlrev_b32 v22, 16, v7\n"
"v_and_b32 v23, 0xffff0000, v7\n"
"v_add_f32 v40, v8, v12\n"
"v_add_f32 v41, v9, v13\n"
"v_add_f32 v42, v10, v14\n"
"v_add_f32 v43, v11, v15\n"
"v_add_f32 v40, v40, v16\n"
"v_add_f32 v41, v41, v17\n"
"v_add_f32 v42, v42, v18\n"
"v_add_f32 v43, v43, v19\n"
"v_add_f32 v40, v40, v20\n"
"v_add_f32 v41, v41, v21\n"
"v_add_f32 v42, v42, v22\n"
"v_add_f32 v43, v43, v23\n"
"v_add_f32 v40, v40, v41\n"
"v_add_f32 v42, v42, v43\n"
"v_add_f32 v40, v40, v42\n"
"s_nop 1\n"
"v_add_f32_dpp v40, v40, v40 quad_perm:[1,0,3,2] row_mask:0xf bank_mask:0xf\n"
"s_nop 1\n"
"v_add_f32_dpp v40, v40, v40 quad_perm:[2,3,0,1] row_mask:0xf bank_mask:0xf\n"
"s_nop 1\n"
"v_add_f32_dpp v40, v40, v40 row_half_mirror row_mask:0xf bank_mask:0xf\n"
"s_nop 1\n"
"v_add_f32_dpp v40, v40, v40 row_mirror row_mask:0xf bank_mask:0xf\n"
"v_mov_b32 v41, v40\n"
"s_nop 1\n"
"v_permlane16_swap_b32 v41, v40\n"
"v_add_f32 v40, v40, v41\n"
"v_mul_f32 v40, 0x3b000000, v40\n"
"v_sub_f32 v24, v8, v40\n"
"v_sub_f32 v25, v9, v40\n"
"v_sub_f32 v26, v10, v40\n"
"v_sub_f32 v27, v11, v40\n"
"v_sub_f32 v28, v12, v40\n"
"v_sub_f32 v29, v13, v40\n"
"v_sub_f32 v30, v14, v40\n"
"v_sub_f32 v31, v15, v40\n"
"v_sub_f32 v32, v16, v40\n"
"v_sub_f32 v33, v17, v40\n"
"v_sub_f32 v34, v18, v40\n"
"v_sub_f32 v35, v19, v40\n"
"v_sub_f32 v36, v20, v40\n"
"v_sub_f32 v37, v21, v40\n"
"v_sub_f32 v38, v22, v40\n"
"v_sub_f32 v39, v23, v40\n"
"v_mul_f32 v44, v24, v24\n"
"v_mul_f32 v45, v25, v25\n"
"v_mul_f32 v46, v26, v26\n"
"v_mul_f32 v47, v27, v27\n"
"v_fmac_f32 v44, v28, v28\n"
"v_fmac_f32 v45, v29, v29\n"
"v_fmac_f32 v46, v30, v30\n"
"v_fmac_f32 v47, v31, v31\n"
"v_fmac_f32 v44, v32, v32\n"
"v_fmac_f32 v45, v33, v33\n"
"v_fmac_f32 v46, v34, v34\n"
"v_fmac_f32 v47, v35, v35\n"
"v_fmac_f32 v44, v36, v36\n"
"v_fmac_f32 v45, v37, v37\n"
"v_fmac_f32 v46, v38, v38\n"
"v_fmac_f32 v47, v39, v39\n"
"v_add_f32 v44, v44, v45\n"
"v_add_f32 v46, v46, v47\n"
"v_add_f32 v44, v44, v46\n"
"s_nop 1\n"
"v_add_f32_dpp v44, v44, v44 quad_perm:[1,0,3,2] row_mask:0xf bank_mask:0xf\n"
"s_nop 1\n"
"v_add_f32_dpp v44, v44, v44 quad_perm:[2,3,0,1] row_mask:0xf bank_mask:0xf\n"
"s_nop 1\n"
"v_add_f32_dpp v44, v44, v44 row_half_mirror row_mask:0xf bank_mask:0xf\n"
"s_nop 1\n"
"v_add_f32_dpp v44, v44, v44 row_mirror row_mask:0xf bank_mask:0xf\n"
"v_mov_b32 v45, v44\n"
"s_nop 1\n"
"v_permlane16_swap_b32 v45, v44\n"
"v_add_f32 v44, v44, v45\n"
"v_mul_f32 v44, 0x3b000000, v44\n"
"v_cvt_pk_bf16_f32 v44, v44, v44\n"
"v_lshlrev_b32 v44, 16, v44\n"
"v_add_f32 v44, 0x3727c5ac, v44\n"
"v_cvt_pk_bf16_f32 v44, v44, v44\n"
"v_lshlrev_b32 v44, 16, v44\n"
"v_sqrt_f32 v44, v44\n"
"v_cvt_pk_bf16_f32 v40, v40, v40\n"
"v_lshlrev_b32 v40, 16, v40\n"
"v_cvt_pk_bf16_f32 v44, v44, v44\n"
"v_lshlrev_b32 v44, 16, v44\n"
"v_rcp_f32 v44, v44\n"
"s_nop 0\n"
"v_mul_f32 v44, 0x3e0293ee, v44\n"
"v_mul_f32 v40, v40, v44\n"
"v_fma_f32 v8, v8, v44, -v40\n"
"v_fma_f32 v9, v9, v44, -v40\n"
"v_fma_f32 v10, v10, v44, -v40\n"
"v_fma_f32 v11, v11, v44, -v40\n"
"v_fma_f32 v12, v12, v44, -v40\n"
"v_fma_f32 v13, v13, v44, -v40\n"
"v_fma_f32 v14, v14, v44, -v40\n"
"v_fma_f32 v15, v15, v44, -v40\n"
"v_fma_f32 v16, v16, v44, -v40\n"
"v_fma_f32 v17, v17, v44, -v40\n"
"v_fma_f32 v18, v18, v44, -v40\n"
"v_fma_f32 v19, v19, v44, -v40\n"
"v_fma_f32 v20, v20, v44, -v40\n"
"v_fma_f32 v21, v21, v44, -v40\n"
"v_fma_f32 v22, v22, v44, -v40\n"
"v_fma_f32 v23, v23, v44, -v40\n"
"v_cvt_pk_bf16_f32 v0, v8, v9\n"
"v_cvt_pk_bf16_f32 v1, v10, v11\n"
"v_cvt_pk_bf16_f32 v2, v12, v13\n"
"v_cvt_pk_bf16_f32 v3, v14, v15\n"
"v_cvt_pk_bf16_f32 v4, v16, v17\n"
"v_cvt_pk_bf16_f32 v5, v18, v19\n"
"v_cvt_pk_bf16_f32 v6, v20, v21\n"
"v_cvt_pk_bf16_f32 v7, v22, v23\n"
"ds_write_b128 v89, v[0:3] offset:0\n"
"ds_write_b128 v89, v[4:7] offset:4096\n"
"s_cmp_lg_u32 s38, 0\n"
"s_cbranch_scc1 TK2_%=\n"
"s_waitcnt vmcnt(5)\n"
"s_mov_b64 exec, 1\n"
"ds_write_b32 v92, v74\n"
"s_mov_b64 exec, -1\n"
"TK2_%=:\n"
"s_waitcnt lgkmcnt(0)\n"
"s_barrier\n"
"ds_read_b128 v[0:3], v90 offset:0\n"
"ds_read_b128 v[4:7], v90 offset:1024\n"
"ds_read_b32 v8, v92 offset:0\n"
"s_waitcnt lgkmcnt(2)\n"
"buffer_store_dwordx4 v[0:3], v88, s[40:43], s29 offen sc1\n"
"s_waitcnt lgkmcnt(1)\n"
"buffer_store_dwordx4 v[4:7], v88, s[40:43], s29 offen offset:2048 sc1\n"
"s_waitcnt lgkmcnt(0)\n"
"v_readfirstlane_b32 s39, v8\n"
"v_xor_b32 v9, 0x7f4a7c15, v8\n"
"v_xor_b32 v8, 0x9e3779b9, v8\n"
"v_lshlrev_b32 v10, 4, v251\n"
"s_add_u32 m0, s91, 0\n"
"s_nop 0\n"
"buffer_load_dwordx4 v247, s[44:47], s84 offen lds\n"
"s_add_u32 m0, s91, 896\n"
"s_nop 0\n"
"buffer_load_dwordx4 v247, s[44:47], s84 offen offset:128 lds\n"
"s_add_u32 s84, s84, 524288\n"
"s_add_u32 m0, s92, 0\n"
"s_nop 0\n"
"buffer_load_dwordx4 v248, s[48:51], s86 offen lds\n"
"s_add_u32 s86, s86, 176128\n"
"s_add_u32 m0, s91, 2048\n"
"s_nop 0\n"
"buffer_load_dwordx4 v247, s[44:47], s85 offen lds\n"
"s_add_u32 m0, s91, 2944\n"
"s_nop 0\n"
"buffer_load_dwordx4 v247, s[44:47], s85 offen offset:128 lds\n"
"s_add_u32 s85, s85, 524288\n"
"s_add_u32 m0, s92, 1024\n"
"s_nop 0\n"
"buffer_load_dwordx4 v248, s[48:51], s87 offen lds\n"
"s_add_u32 s87, s87, 176128\n"
"s_waitcnt vmcnt(6)\n"
"s_barrier\n"
"s_cmp_lg_u32 s38, 0\n"
"s_cbranch_scc1 SG0_%=\n"
"s_mov_b32 exec_hi, 0\n"
"s_mov_b32 exec_lo, 0xffffffff\n"
"buffer_store_dwordx2 v[8:9], v10, s[40:43], s31 offen sc1\n"
"s_mov_b64 exec, -1\n"
"SG0_%=:\n"
"s_add_u32 s31, s31, 8192\n"
"s_add_u32 m0, s91, 16384\n"
"s_nop 0\n"
"buffer_load_dwordx4 v247, s[44:47], s84 offen lds\n"
"s_add_u32 m0, s91, 17280\n"
"s_nop 0\n"
"buffer_load_dwordx4 v247, s[44:47], s84 offen offset:128 lds\n"
"s_add_u32 s84, s84, 524288\n"
"s_add_u32 m0, s92, 8192\n"
"s_nop 0\n"
"buffer_load_dwordx4 v248, s[48:51], s86 offen lds\n"
"s_add_u32 s86, s86, 176128\n"
"s_add_u32 m0, s91, 18432\n"
"s_nop 0\n"
"buffer_load_dwordx4 v247, s[44:47], s85 offen lds\n"
"s_add_u32 m0, s91, 19328\n"
"s_nop 0\n"
"buffer_load_dwordx4 v247, s[44:47], s85 offen offset:128 lds\n"
"s_add_u32 s85, s85, 524288\n"
"s_add_u32 m0, s92, 9216\n"
"s_nop 0\n"
"buffer_load_dwordx4 v248, s[48:51], s87 offen lds\n"
"s_add_u32 s87, s87, 176128\n"
"s_add_u32 m0, s91, 32768\n"
"s_nop 0\n"
"buffer_load_dwordx4 v247, s[44:47], s84 offen lds\n"
"s_add_u32 m0, s91, 33664\n"
"s_nop 0\n"
"buffer_load_dwordx4 v247, s[44:47], s84 offen offset:128 lds\n"
"s_add_u32 s84, s84, 524288\n"
"s_add_u32 m0, s92, 16384\n"
"s_nop 0\n"
"buffer_load_dwordx4 v248, s[48:51], s86 offen lds\n"
"s_add_u32 s86, s86, 176128\n"
"s_add_u32 m0, s91, 34816\n"
"s_nop 0\n"
"buffer_load_dwordx4 v247, s[44:47], s85 offen lds\n"
"s_add_u32 m0, s91, 35712\n"
"s_nop 0\n"
"buffer_load_dwordx4 v247, s[44:47], s85 offen offset:128 lds\n"
"s_add_u32 s85, s85, 524288\n"
"s_add_u32 m0, s92, 17408\n"
"s_nop 0\n"
"buffer_load_dwordx4 v248, s[48:51], s87 offen lds\n"
"s_add_u32 s87, s87, 176128\n"
"s_lshl_b32 s36, s6, 11\n"
"s_add_u32 s36, s36, 1536\n"
"s_lshl_b32 s98, s10, 5\n"
"s_lshl_b32 s99, s11, 3\n"
"s_add_u32 s37, s98, s99\n"
"s_sub_u32 s99, 512, s37\n"
"s_lshl_b32 s99, s99, 8\n"
"s_add_u32 s98, s37, s36\n"
"s_lshl_b32 s98, s98, 8\n"
"s_add_u32 s52, s14, s98\n"
"s_addc_u32 s53, s15, 0\n"
"s_mov_b32 s54, s99\n"
"s_mov_b32 s55, 0x20000\n"
"s_lshl_b32 s98, s38, 4\n"
"s_sub_u32 s99, 512, s98\n"
"s_lshl_b32 s99, s99, 8\n"
"s_add_u32 s98, s98, s36\n"
"s_lshl_b32 s98, s98, 8\n"
"s_add_u32 s56, s16, s98\n"
"s_addc_u32 s57, s17, 0\n"
"s_mov_b32 s58, s99\n"
"s_mov_b32 s59, 0x20000\n"
"s_lshr_b32 s37, s12, 1\n"
"s_lshl_b32 s98, s38, 13\n"
"s_lshl_b32 s99, s13, 8\n"
"s_add_u32 s36, s98, s99\n"
"s_sub_u32 s99, 1409024, s36\n"
"s_add_u32 s60, s20, s36\n"
"s_addc_u32 s61, s21, 0\n"
"s_mov_b32 s62, s99\n"
"s_mov_b32 s63, 0x20000\n"
"s_lshl_b32 s98, s37, 20\n"
"s_add_u32 s98, s98, s36\n"
"s_sub_u32 s99, 4194304, s98\n"
"s_add_u32 s64, s18, s98\n"
"s_addc_u32 s65, s19, 0\n"
"s_mov_b32 s66, s99\n"
"s_mov_b32 s67, 0x20000\n"
"s_mov_b32 s72, s24\n"
"s_mov_b32 s73, s25\n"
"s_mov_b32 s74, 2097152\n"
"s_mov_b32 s75, 0x20000\n"
"s_mov_b32 s16, s22\n"
"s_mov_b32 s17, s23\n"
"s_lshl_b32 s9, s7, 2\n"
"s_add_u32 s9, s9, s38\n"
"s_lshl_b32 s9, s9, 2\n"
"s_lshl_b32 s98, s8, 1\n"
"s_xor_b32 s99, s98, 0\n"
"s_add_u32 s99, s99, s9\n"
"s_lshl_b32 s80, s99, 14\n"
"s_lshl_b32 s20, s99, 5\n"
"s_add_u32 s20, s20, s6\n"
"s_lshl_b32 s20, s20, 12\n"
"s_add_u32 s20, s20, 8388608\n"
"s_mul_i32 s99, s99, 48\n"
"s_add_u32 s99, s99, 2048\n"
"s_add_u32 s24, s99, s34\n"
"s_lshl_b32 s24, s24, 10\n"
"s_add_u32 s28, s99, s35\n"
"s_lshl_b32 s28, s28, 10\n"
"s_xor_b32 s99, s98, 1\n"
"s_add_u32 s99, s99, s9\n"
"s_lshl_b32 s81, s99, 14\n"
"s_lshl_b32 s21, s99, 5\n"
"s_add_u32 s21, s21, s6\n"
"s_lshl_b32 s21, s21, 12\n"
"s_add_u32 s21, s21, 8388608\n"
"s_mul_i32 s99, s99, 48\n"
"s_add_u32 s99, s99, 2048\n"
"s_add_u32 s25, s99, s34\n"
"s_lshl_b32 s25, s25, 10\n"
"s_add_u32 s29, s99, s35\n"
"s_lshl_b32 s29, s29, 10\n"
"s_xor_b32 s99, s98, 2\n"
"s_add_u32 s99, s99, s9\n"
"s_lshl_b32 s82, s99, 14\n"
"s_lshl_b32 s22, s99, 5\n"
"s_add_u32 s22, s22, s6\n"
"s_lshl_b32 s22, s22, 12\n"
"s_add_u32 s22, s22, 8388608\n"
"s_mul_i32 s99, s99, 48\n"
"s_add_u32 s99, s99, 2048\n"
"s_add_u32 s26, s99, s34\n"
"s_lshl_b32 s26, s26, 10\n"
"s_xor_b32 s99, s98, 3\n"
"s_add_u32 s99, s99, s9\n"
"s_lshl_b32 s83, s99, 14\n"
"s_lshl_b32 s23, s99, 5\n"
"s_add_u32 s23, s23, s6\n"
"s_lshl_b32 s23, s23, 12\n"
"s_add_u32 s23, s23, 8388608\n"
"s_mul_i32 s99, s99, 48\n"
"s_add_u32 s99, s99, 2048\n"
"s_add_u32 s27, s99, s34\n"
"s_lshl_b32 s27, s27, 10\n"
"s_lshl_b32 s98, s10, 13\n"
"s_lshl_b32 s99, s11, 10\n"
"s_add_u32 s93, s98, s99\n"
"s_add_u32 s93, s93, 98304\n"
"s_lshl_b32 s94, s38, 12\n"
"s_add_u32 s94, s94, 131072\n"
"s_mov_b32 s88, 0\n"
"s_mov_b32 s89, 4096\n"
"s_mov_b32 s90, 0\n"
"s_mov_b32 s96, 0x41000000\n"
"s_lshl_b32 s15, s7, 14\n"
"v_lshlrev_b32 v252, 7, v64\n"
"v_cmp_gt_u32 vcc, 16, v64\n"
"v_mov_b32 v82, 0x40000000\n"
"s_nop 0\n"
"v_cndmask_b32 v252, v82, v252, vcc\n"
"v_lshlrev_b32 v82, 6, v65\n"
"v_lshl_add_u32 v83, v66, 4, v82\n"
"v_lshl_add_u32 v240, v67, 11, v83\n"
"v_lshrrev_b32 v83, 1, v66\n"
"v_lshlrev_b32 v84, 1, v70\n"
"v_xor_b32 v83, v83, v84\n"
"v_and_b32 v84, 1, v66\n"
"v_lshl_add_u32 v82, v84, 3, v82\n"
"v_xor_b32 v86, 0, v83\n"
"v_lshl_add_u32 v86, v86, 4, v82\n"
"v_lshl_add_u32 v87, v67, 10, v86\n"
"v_add_u32 v241, 65536, v87\n"
"v_lshl_add_u32 v87, v67, 11, v86\n"
"v_add_u32 v245, 131072, v87\n"
"v_xor_b32 v86, 2, v83\n"
"v_lshl_add_u32 v86, v86, 4, v82\n"
"v_lshl_add_u32 v87, v67, 10, v86\n"
"v_add_u32 v242, 65536, v87\n"
"v_lshl_add_u32 v87, v67, 11, v86\n"
"v_add_u32 v246, 131072, v87\n"
"v_lshrrev_b32 v82, 3, v69\n"
"v_lshlrev_b32 v82, 10, v82\n"
"v_and_b32 v83, 7, v69\n"
"v_lshl_add_u32 v82, v83, 7, v82\n"
"v_lshrrev_b32 v83, 1, v69\n"
"v_xor_b32 v83, v83, v68\n"
"v_xor_b32 v84, 0, v83\n"
"v_lshl_add_u32 v84, v84, 4, v82\n"
"v_add_u32 v243, 98304, v84\n"
"v_xor_b32 v84, 4, v83\n"
"v_lshl_add_u32 v84, v84, 4, v82\n"
"v_add_u32 v244, 98304, v84\n"
"s_lshl_b32 s98, s11, 2\n"
"v_add_u32 v82, s98, v68\n"
"v_and_b32 v83, 7, v64\n"
"v_xor_b32 v82, v82, v83\n"
"v_lshrrev_b32 v84, 3, v64\n"
"v_lshlrev_b32 v84, 8, v84\n"
"v_lshl_add_u32 v249, v82, 4, v84\n"
"v_lshlrev_b32 v82, 6, v67\n"
"v_lshl_add_u32 v82, v81, 4, v82\n"
"v_lshl_add_u32 v250, v65, 8, v82\n"
"v_mov_b32 v82, 0x3f803f80\n"
"v_accvgpr_write_b32 a224, v82\n"
"v_accvgpr_write_b32 a225, v82\n"
"v_accvgpr_write_b32 a226, v82\n"
"v_accvgpr_write_b32 a227, v82\n"
"v_lshlrev_b32 v128, 16, v144\n"
"v_and_b32 v129, 0xffff0000, v144\n"
"v_lshlrev_b32 v130, 16, v145\n"
"v_and_b32 v131, 0xffff0000, v145\n"
"v_lshlrev_b32 v132, 16, v146\n"
"v_and_b32 v133, 0xffff0000, v146\n"
"v_lshlrev_b32 v134, 16, v147\n"
"v_and_b32 v135, 0xffff0000, v147\n"
"v_lshlrev_b32 v136, 16, v148\n"
"v_and_b32 v137, 0xffff0000, v148\n"
"v_lshlrev_b32 v138, 16, v149\n"
"v_and_b32 v139, 0xffff0000, v149\n"
"v_lshlrev_b32 v140, 16, v150\n"
"v_and_b32 v141, 0xffff0000, v150\n"
"v_lshlrev_b32 v142, 16, v151\n"
"v_and_b32 v143, 0xffff0000, v151\n"
"v_mul_f32 v128, 0x3e0293ee, v128\n"
"v_mul_f32 v129, 0x3e0293ee, v129\n"
"v_mul_f32 v130, 0x3e0293ee, v130\n"
"v_mul_f32 v131, 0x3e0293ee, v131\n"
"v_mul_f32 v132, 0x3e0293ee, v132\n"
"v_mul_f32 v133, 0x3e0293ee, v133\n"
"v_mul_f32 v134, 0x3e0293ee, v134\n"
"v_mul_f32 v135, 0x3e0293ee, v135\n"
"v_mul_f32 v136, 0x3e0293ee, v136\n"
"v_mul_f32 v137, 0x3e0293ee, v137\n"
"v_mul_f32 v138, 0x3e0293ee, v138\n"
"v_mul_f32 v139, 0x3e0293ee, v139\n"
"v_mul_f32 v140, 0x3e0293ee, v140\n"
"v_mul_f32 v141, 0x3e0293ee, v141\n"
"v_mul_f32 v142, 0x3e0293ee, v142\n"
"v_mul_f32 v143, 0x3e0293ee, v143\n"
"s_xor_b32 s34, s39, 0x9e3779b9\n"
"s_xor_b32 s35, s39, 0x7f4a7c15\n"
"s_sub_u32 s36, s39, 31\n"
"s_mov_b32 s76, s16\n"
"s_mov_b32 s77, s17\n"
"s_mov_b32 s78, 1024\n"
"s_mov_b32 s79, 0x20000\n"
"s_and_b32 s18, s12, 1\n"
"s_lshl_b32 s98, s7, 1\n"
"s_add_u32 s98, s98, s18\n"
"s_lshl_b32 s19, s98, 2\n"
"s_add_u32 s16, s19, s38\n"
"s_lshl_b32 s16, s16, 1\n"
"s_lshr_b32 s17, s37, 1\n"
"s_add_u32 s19, s19, s13\n"
"s_lshl_b32 s19, s19, 19\n"
"s_lshl_b32 s98, s38, 13\n"
"s_add_u32 s19, s19, s98\n"
"s_add_u32 s9, s19, 25165824\n"
"s_lshl_b32 s98, s18, 2\n"
"s_add_u32 s11, s98, s13\n"
"s_lshl_b32 s11, s11, 3\n"
"s_lshl_b32 s12, s7, 2\n"
"s_add_u32 s12, s12, s13\n"
"s_add_u32 s98, s16, s17\n"
"s_mul_i32 s99, s98, 49152\n"
"s_add_u32 s6, s99, 2097152\n"
"s_lshl_b32 s99, s37, 15\n"
"s_add_u32 s19, s99, 8388608\n"
"s_lshl_b32 s99, s98, 17\n"
"s_add_u32 s7, s99, s19\n"
"s_xor_b32 s8, s99, 131072\n"
"s_add_u32 s8, s8, s19\n"
"s_lshl_b32 s98, s98, 14\n"
"s_lshl_b32 s99, s13, 8\n"
"s_add_u32 s98, s98, s99\n"
"s_and_b32 s99, s37, 1\n"
"s_lshl_b32 s99, s99, 7\n"
"s_add_u32 s10, s98, s99\n"
"s_mov_b32 s95, 0\n"
"s_waitcnt vmcnt(0)\n"
"s_mov_b32 s39, 0\n"
"SPx_%=:\n"
"buffer_load_dwordx2 v[8:9], v91, s[40:43], s30 offen sc1\n"
"s_waitcnt vmcnt(0)\n"
"v_xor_b32 v9, v8, v9\n"
"v_xor_b32 v8, 0x9e3779b9, v8\n"
"v_cmp_eq_u32 vcc, 0xe17d05ac, v9\n"
"v_subrev_u32 v8, s36, v8\n"
"v_cmp_gt_u32 s[98:99], 63, v8\n"
"s_nop 0\n"
"s_and_b64 vcc, vcc, s[98:99]\n"
"s_cmp_eq_u64 vcc, -1\n"
"s_cbranch_scc1 SDx_%=\n"
"s_sleep 1\n"
"s_add_u32 s39, s39, 1\n"
"s_cmp_lt_u32 s39, 0x20000\n"
"s_cbranch_scc1 SPx_%=\n"
"SDx_%=:\n"
"s_add_u32 s30, s30, 8192\n"
"buffer_load_dwordx4 a[0:3], v251, s[40:43], s80 offen sc1\n"
"buffer_load_dwordx4 a[4:7], v251, s[40:43], s80 offen offset:1024 sc1\n"
"buffer_load_dwordx4 a[8:11], v251, s[40:43], s81 offen sc1\n"
"buffer_load_dwordx4 a[12:15], v251, s[40:43], s81 offen offset:1024 sc1\n"
"buffer_load_dwordx4 a[16:19], v251, s[40:43], s82 offen sc1\n"
"buffer_load_dwordx4 a[20:23], v251, s[40:43], s82 offen offset:1024 sc1\n"
"buffer_load_dwordx4 a[24:27], v251, s[40:43], s83 offen sc1\n"
"buffer_load_dwordx4 a[28:31], v251, s[40:43], s83 offen offset:1024 sc1\n"
"s_barrier\n"
"ds_read_b64_tr_b16 a[128:129], v241 offset:0\n"
"ds_read_b64_tr_b16 a[130:131], v241 offset:2048\n"
"ds_read_b64_tr_b16 a[132:133], v241 offset:4096\n"
"ds_read_b64_tr_b16 a[134:135], v241 offset:6144\n"
"ds_read_b64_tr_b16 a[136:137], v242 offset:0\n"
"ds_read_b64_tr_b16 a[138:139], v242 offset:2048\n"
"ds_read_b64_tr_b16 a[140:141], v242 offset:4096\n"
"ds_read_b64_tr_b16 a[142:143], v242 offset:6144\n"
"s_waitcnt vmcnt(7) lgkmcnt(6)\n"
"v_mfma_f32_16x16x32_bf16 v[192:195], a[128:131], a[0:3], v[128:131]\n"
"s_waitcnt vmcnt(6) lgkmcnt(4)\n"
"ds_read_b64_tr_b16 a[144:145], v241 offset:512\n"
"v_mfma_f32_16x16x32_bf16 v[192:195], a[132:135], a[4:7], v[192:195]\n"
"s_waitcnt vmcnt(5)\n"
"v_mfma_f32_16x16x32_bf16 v[200:203], a[128:131], a[8:11], v[128:131]\n"
"s_waitcnt vmcnt(4)\n"
"ds_read_b64_tr_b16 a[146:147], v241 offset:2560\n"
"v_mfma_f32_16x16x32_bf16 v[200:203], a[132:135], a[12:15], v[200:203]\n"
"s_waitcnt vmcnt(3)\n"
"buffer_load_dwordx4 a[32:35], v251, s[40:43], s80 offen offset:2048 sc1\n"
"v_mfma_f32_16x16x32_bf16 v[208:211], a[128:131], a[16:19], v[128:131]\n"
"s_waitcnt vmcnt(3)\n"
"ds_read_b64_tr_b16 a[148:149], v241 offset:4608\n"
"v_mfma_f32_16x16x32_bf16 v[208:211], a[132:135], a[20:23], v[208:211]\n"
"s_waitcnt vmcnt(2)\n"
"buffer_load_dwordx4 a[36:39], v251, s[40:43], s80 offen offset:3072 sc1\n"
"v_mfma_f32_16x16x32_bf16 v[216:219], a[128:131], a[24:27], v[128:131]\n"
"s_waitcnt vmcnt(2)\n"
"s_add_u32 s80, s80, 4096\n"
"ds_read_b64_tr_b16 a[150:151], v241 offset:6656\n"
"v_mfma_f32_16x16x32_bf16 v[216:219], a[132:135], a[28:31], v[216:219]\n"
"s_waitcnt lgkmcnt(6)\n"
"buffer_load_dwordx4 a[40:43], v251, s[40:43], s81 offen offset:2048 sc1\n"
"v_mfma_f32_16x16x32_bf16 v[196:199], a[136:139], a[0:3], v[132:135]\n"
"s_waitcnt lgkmcnt(4)\n"
"ds_read_b64_tr_b16 a[128:129], v242 offset:512\n"
"v_mfma_f32_16x16x32_bf16 v[196:199], a[140:143], a[4:7], v[196:199]\n"
"buffer_load_dwordx4 a[44:47], v251, s[40:43], s81 offen offset:3072 sc1\n"
"v_mfma_f32_16x16x32_bf16 v[204:207], a[136:139], a[8:11], v[132:135]\n"
"s_add_u32 s81, s81, 4096\n"
"ds_read_b64_tr_b16 a[130:131], v242 offset:2560\n"
"v_mfma_f32_16x16x32_bf16 v[204:207], a[140:143], a[12:15], v[204:207]\n"
"buffer_load_dwordx4 a[48:51], v251, s[40:43], s82 offen offset:2048 sc1\n"
"v_mfma_f32_16x16x32_bf16 v[212:215], a[136:139], a[16:19], v[132:135]\n"
"ds_read_b64_tr_b16 a[132:133], v242 offset:4608\n"
"v_mfma_f32_16x16x32_bf16 v[212:215], a[140:143], a[20:23], v[212:215]\n"
"buffer_load_dwordx4 a[52:55], v251, s[40:43], s82 offen offset:3072 sc1\n"
"v_mfma_f32_16x16x32_bf16 v[220:223], a[136:139], a[24:27], v[132:135]\n"
"s_add_u32 s82, s82, 4096\n"
"ds_read_b64_tr_b16 a[134:135], v242 offset:6656\n"
"v_mfma_f32_16x16x32_bf16 v[220:223], a[140:143], a[28:31], v[220:223]\n"
"s_waitcnt lgkmcnt(6)\n"
"buffer_load_dwordx4 a[56:59], v251, s[40:43], s83 offen offset:2048 sc1\n"
"v_mfma_f32_16x16x32_bf16 v[224:227], a[144:147], a[0:3], v[136:139]\n"
"s_waitcnt lgkmcnt(4)\n"
"ds_read_b64_tr_b16 a[136:137], v240 offset:0\n"
"v_mfma_f32_16x16x32_bf16 v[224:227], a[148:151], a[4:7], v[224:227]\n"
"ds_read_b64_tr_b16 a[138:139], v240 offset:4096\n"
"buffer_load_dwordx4 a[60:63], v251, s[40:43], s83 offen offset:3072 sc1\n"
"v_mfma_f32_16x16x32_bf16 v[232:235], a[144:147], a[8:11], v[136:139]\n"
"s_add_u32 s83, s83, 4096\n"
"ds_read_b64_tr_b16 a[140:141], v240 offset:8192\n"
"v_mfma_f32_16x16x32_bf16 v[232:235], a[148:151], a[12:15], v[232:235]\n"
"s_waitcnt lgkmcnt(5)\n"
"ds_read_b64_tr_b16 a[142:143], v240 offset:12288\n"
"v_mfma_f32_16x16x32_bf16 v[228:231], a[128:131], a[0:3], v[140:143]\n"
"s_waitcnt lgkmcnt(4)\n"
"ds_read_b64_tr_b16 a[144:145], v240 offset:8\n"
"v_mfma_f32_16x16x32_bf16 v[228:231], a[132:135], a[4:7], v[228:231]\n"
"ds_read_b64_tr_b16 a[146:147], v240 offset:4104\n"
"v_mfma_f32_16x16x32_bf16 v[236:239], a[128:131], a[8:11], v[140:143]\n"
"ds_read_b64_tr_b16 a[148:149], v240 offset:8200\n"
"v_mfma_f32_16x16x32_bf16 v[236:239], a[132:135], a[12:15], v[236:239]\n"
"s_waitcnt lgkmcnt(5)\n"
"ds_read_b64_tr_b16 a[150:151], v240 offset:12296\n"
"v_mfma_f32_16x16x32_bf16 v[0:3], a[136:139], a[0:3], 0\n"
"s_waitcnt lgkmcnt(4)\n"
"ds_read_b64_tr_b16 a[128:129], v240 offset:512\n"
"v_mfma_f32_16x16x32_bf16 v[0:3], a[140:143], a[4:7], v[0:3]\n"
"v_mfma_f32_16x16x32_bf16 v[8:11], a[136:139], a[8:11], 0\n"
"ds_read_b64_tr_b16 a[130:131], v240 offset:4608\n"
"v_mfma_f32_16x16x32_bf16 v[8:11], a[140:143], a[12:15], v[8:11]\n"
"v_mfma_f32_16x16x32_bf16 v[16:19], a[136:139], a[16:19], 0\n"
"ds_read_b64_tr_b16 a[132:133], v240 offset:8704\n"
"v_mfma_f32_16x16x32_bf16 v[16:19], a[140:143], a[20:23], v[16:19]\n"
"v_mfma_f32_16x16x32_bf16 v[24:27], a[136:139], a[24:27], 0\n"
"ds_read_b64_tr_b16 a[134:135], v240 offset:12800\n"
"v_mfma_f32_16x16x32_bf16 v[24:27], a[140:143], a[28:31], v[24:27]\n"
"s_waitcnt lgkmcnt(6)\n"
"v_mfma_f32_16x16x32_bf16 v[4:7], a[144:147], a[0:3], 0\n"
"s_waitcnt lgkmcnt(4)\n"
"ds_read_b64_tr_b16 a[136:137], v240 offset:520\n"
"v_mfma_f32_16x16x32_bf16 v[4:7], a[148:151], a[4:7], v[4:7]\n"
"v_mfma_f32_16x16x32_bf16 v[12:15], a[144:147], a[8:11], 0\n"
"ds_read_b64_tr_b16 a[138:139], v240 offset:4616\n"
"v_mfma_f32_16x16x32_bf16 v[12:15], a[148:151], a[12:15], v[12:15]\n"
"v_mfma_f32_16x16x32_bf16 v[20:23], a[144:147], a[16:19], 0\n"
"ds_read_b64_tr_b16 a[140:141], v240 offset:8712\n"
"v_mfma_f32_16x16x32_bf16 v[20:23], a[148:151], a[20:23], v[20:23]\n"
"v_mfma_f32_16x16x32_bf16 v[28:31], a[144:147], a[24:27], 0\n"
"ds_read_b64_tr_b16 a[142:143], v240 offset:12808\n"
"v_mfma_f32_16x16x32_bf16 v[28:31], a[148:151], a[28:31], v[28:31]\n"
"s_waitcnt lgkmcnt(6)\n"
"v_mfma_f32_16x16x32_bf16 v[32:35], a[128:131], a[0:3], 0\n"
"s_waitcnt lgkmcnt(4)\n"
"ds_read_b64_tr_b16 a[144:145], v240 offset:1024\n"
"v_mfma_f32_16x16x32_bf16 v[32:35], a[132:135], a[4:7], v[32:35]\n"
"v_mfma_f32_16x16x32_bf16 v[40:43], a[128:131], a[8:11], 0\n"
"ds_read_b64_tr_b16 a[146:147], v240 offset:5120\n"
"v_mfma_f32_16x16x32_bf16 v[40:43], a[132:135], a[12:15], v[40:43]\n"
"v_mfma_f32_16x16x32_bf16 v[48:51], a[128:131], a[16:19], 0\n"
"ds_read_b64_tr_b16 a[148:149], v240 offset:9216\n"
"v_mfma_f32_16x16x32_bf16 v[48:51], a[132:135], a[20:23], v[48:51]\n"
"v_mfma_f32_16x16x32_bf16 v[56:59], a[128:131], a[24:27], 0\n"
"ds_read_b64_tr_b16 a[150:151], v240 offset:13312\n"
"v_mfma_f32_16x16x32_bf16 v[56:59], a[132:135], a[28:31], v[56:59]\n"
"s_waitcnt lgkmcnt(6)\n"
"v_mfma_f32_16x16x32_bf16 v[36:39], a[136:139], a[0:3], 0\n"
"s_waitcnt lgkmcnt(4)\n"
"ds_read_b64_tr_b16 a[128:129], v240 offset:1032\n"
"v_mfma_f32_16x16x32_bf16 v[36:39], a[140:143], a[4:7], v[36:39]\n"
"v_mfma_f32_16x16x32_bf16 v[44:47], a[136:139], a[8:11], 0\n"
"ds_read_b64_tr_b16 a[130:131], v240 offset:5128\n"
"v_mfma_f32_16x16x32_bf16 v[44:47], a[140:143], a[12:15], v[44:47]\n"
"v_mfma_f32_16x16x32_bf16 v[52:55], a[136:139], a[16:19], 0\n"
"ds_read_b64_tr_b16 a[132:133], v240 offset:9224\n"
"v_mfma_f32_16x16x32_bf16 v[52:55], a[140:143], a[20:23], v[52:55]\n"
"v_mfma_f32_16x16x32_bf16 v[60:63], a[136:139], a[24:27], 0\n"
"ds_read_b64_tr_b16 a[134:135], v240 offset:13320\n"
"v_mfma_f32_16x16x32_bf16 v[60:63], a[140:143], a[28:31], v[60:63]\n"
"s_waitcnt lgkmcnt(6)\n"
"v_mfma_f32_16x16x32_bf16 v[64:67], a[144:147], a[0:3], 0\n"
"s_waitcnt lgkmcnt(4)\n"
"ds_read_b64_tr_b16 a[136:137], v240 offset:1536\n"
"v_mfma_f32_16x16x32_bf16 v[64:67], a[148:151], a[4:7], v[64:67]\n"
"v_mfma_f32_16x16x32_bf16 v[72:75], a[144:147], a[8:11], 0\n"
"ds_read_b64_tr_b16 a[138:139], v240 offset:5632\n"
"v_mfma_f32_16x16x32_bf16 v[72:75], a[148:151], a[12:15], v[72:75]\n"
"v_mfma_f32_16x16x32_bf16 v[80:83], a[144:147], a[16:19], 0\n"
"ds_read_b64_tr_b16 a[140:141], v240 offset:9728\n"
"v_mfma_f32_16x16x32_bf16 v[80:83], a[148:151], a[20:23], v[80:83]\n"
"v_mfma_f32_16x16x32_bf16 v[88:91], a[144:147], a[24:27], 0\n"
"ds_read_b64_tr_b16 a[142:143], v240 offset:13824\n"
"v_mfma_f32_16x16x32_bf16 v[88:91], a[148:151], a[28:31], v[88:91]\n"
"s_waitcnt lgkmcnt(6)\n"
"v_mfma_f32_16x16x32_bf16 v[68:71], a[128:131], a[0:3], 0\n"
"s_waitcnt lgkmcnt(4)\n"
"ds_read_b64_tr_b16 a[144:145], v240 offset:1544\n"
"v_mfma_f32_16x16x32_bf16 v[68:71], a[132:135], a[4:7], v[68:71]\n"
"v_mfma_f32_16x16x32_bf16 v[76:79], a[128:131], a[8:11], 0\n"
"ds_read_b64_tr_b16 a[146:147], v240 offset:5640\n"
"v_mfma_f32_16x16x32_bf16 v[76:79], a[132:135], a[12:15], v[76:79]\n"
"v_mfma_f32_16x16x32_bf16 v[84:87], a[128:131], a[16:19], 0\n"
"ds_read_b64_tr_b16 a[148:149], v240 offset:9736\n"
"v_mfma_f32_16x16x32_bf16 v[84:87], a[132:135], a[20:23], v[84:87]\n"
"v_mfma_f32_16x16x32_bf16 v[92:95], a[128:131], a[24:27], 0\n"
"ds_read_b64_tr_b16 a[150:151], v240 offset:13832\n"
"v_mfma_f32_16x16x32_bf16 v[92:95], a[132:135], a[28:31], v[92:95]\n"
"s_waitcnt lgkmcnt(6)\n"
"v_mfma_f32_16x16x32_bf16 v[96:99], a[136:139], a[0:3], 0\n"
"s_barrier\n"
"s_waitcnt lgkmcnt(4)\n"
"v_mfma_f32_16x16x32_bf16 v[96:99], a[140:143], a[4:7], v[96:99]\n"
"ds_read_b64_tr_b16 a[128:129], v241 offset:8192\n"
"v_mfma_f32_16x16x32_bf16 v[104:107], a[136:139], a[8:11], 0\n"
"v_mfma_f32_16x16x32_bf16 v[104:107], a[140:143], a[12:15], v[104:107]\n"
"s_add_u32 m0, s91, 49152\n"
"ds_read_b64_tr_b16 a[130:131], v241 offset:10240\n"
"v_mfma_f32_16x16x32_bf16 v[112:115], a[136:139], a[16:19], 0\n"
"buffer_load_dwordx4 v247, s[44:47], s84 offen lds\n"
"v_mfma_f32_16x16x32_bf16 v[112:115], a[140:143], a[20:23], v[112:115]\n"
"ds_read_b64_tr_b16 a[132:133], v241 offset:12288\n"
"v_mfma_f32_16x16x32_bf16 v[120:123], a[136:139], a[24:27], 0\n"
"v_mfma_f32_16x16x32_bf16 v[120:123], a[140:143], a[28:31], v[120:123]\n"
"s_waitcnt lgkmcnt(5)\n"
"s_add_u32 m0, s91, 50048\n"
"ds_read_b64_tr_b16 a[134:135], v241 offset:14336\n"
"v_mfma_f32_16x16x32_bf16 v[100:103], a[144:147], a[0:3], 0\n"
"s_waitcnt lgkmcnt(4)\n"
"ds_read_b64_tr_b16 a[136:137], v242 offset:8192\n"
"buffer_load_dwordx4 v247, s[44:47], s84 offen offset:128 lds\n"
"v_mfma_f32_16x16x32_bf16 v[100:103], a[148:151], a[4:7], v[100:103]\n"
"v_mfma_f32_16x16x32_bf16 v[108:111], a[144:147], a[8:11], 0\n"
"s_add_u32 s84, s84, 524288\n"
"ds_read_b64_tr_b16 a[138:139], v242 offset:10240\n"
"v_mfma_f32_16x16x32_bf16 v[108:111], a[148:151], a[12:15], v[108:111]\n"
"s_add_u32 m0, s92, 24576\n"
"v_mfma_f32_16x16x32_bf16 v[116:119], a[144:147], a[16:19], 0\n"
"ds_read_b64_tr_b16 a[140:141], v242 offset:12288\n"
"buffer_load_dwordx4 v248, s[48:51], s86 offen lds\n"
"v_mfma_f32_16x16x32_bf16 v[116:119], a[148:151], a[20:23], v[116:119]\n"
"v_mfma_f32_16x16x32_bf16 v[124:127], a[144:147], a[24:27], 0\n"
"s_add_u32 s86, s86, 176128\n"
"ds_read_b64_tr_b16 a[142:143], v242 offset:14336\n"
"v_mfma_f32_16x16x32_bf16 v[124:127], a[148:151], a[28:31], v[124:127]\n"
"s_add_u32 m0, s91, 51200\n"
"s_waitcnt vmcnt(10) lgkmcnt(6)\n"
"v_mfma_f32_16x16x32_bf16 v[192:195], a[128:131], a[32:35], v[192:195]\n"
"s_waitcnt vmcnt(9) lgkmcnt(4)\n"
"buffer_load_dwordx4 v247, s[44:47], s85 offen lds\n"
"ds_read_b64_tr_b16 a[144:145], v241 offset:8704\n"
"v_mfma_f32_16x16x32_bf16 v[192:195], a[132:135], a[36:39], v[192:195]\n"
"s_waitcnt vmcnt(9)\n"
"v_mfma_f32_16x16x32_bf16 v[200:203], a[128:131], a[40:43], v[200:203]\n"
"s_waitcnt vmcnt(8)\n"
"ds_read_b64_tr_b16 a[146:147], v241 offset:10752\n"
"v_mfma_f32_16x16x32_bf16 v[200:203], a[132:135], a[44:47], v[200:203]\n"
"s_waitcnt vmcnt(7)\n"
"s_add_u32 m0, s91, 52096\n"
"v_mfma_f32_16x16x32_bf16 v[208:211], a[128:131], a[48:51], v[208:211]\n"
"s_waitcnt vmcnt(6)\n"
"buffer_load_dwordx4 v247, s[44:47], s85 offen offset:128 lds\n"
"ds_read_b64_tr_b16 a[148:149], v241 offset:12800\n"
"v_mfma_f32_16x16x32_bf16 v[208:211], a[132:135], a[52:55], v[208:211]\n"
"s_waitcnt vmcnt(6)\n"
"v_mfma_f32_16x16x32_bf16 v[216:219], a[128:131], a[56:59], v[216:219]\n"
"s_waitcnt vmcnt(5)\n"
"s_add_u32 s85, s85, 524288\n"
"ds_read_b64_tr_b16 a[150:151], v241 offset:14848\n"
"v_mfma_f32_16x16x32_bf16 v[216:219], a[132:135], a[60:63], v[216:219]\n"
"s_waitcnt lgkmcnt(6)\n"
"s_add_u32 m0, s92, 25600\n"
"v_mfma_f32_16x16x32_bf16 v[196:199], a[136:139], a[32:35], v[196:199]\n"
"s_waitcnt lgkmcnt(4)\n"
"buffer_load_dwordx4 v248, s[48:51], s87 offen lds\n"
"ds_read_b64_tr_b16 a[128:129], v242 offset:8704\n"
"v_mfma_f32_16x16x32_bf16 v[196:199], a[140:143], a[36:39], v[196:199]\n"
"s_add_u32 s87, s87, 176128\n"
"v_mfma_f32_16x16x32_bf16 v[204:207], a[136:139], a[40:43], v[204:207]\n"
"ds_read_b64_tr_b16 a[130:131], v242 offset:10752\n"
"v_mfma_f32_16x16x32_bf16 v[204:207], a[140:143], a[44:47], v[204:207]\n"
"v_mfma_f32_16x16x32_bf16 v[212:215], a[136:139], a[48:51], v[212:215]\n"
"ds_read_b64_tr_b16 a[132:133], v242 offset:12800\n"
"v_mfma_f32_16x16x32_bf16 v[212:215], a[140:143], a[52:55], v[212:215]\n"
"buffer_load_dwordx4 a[64:67], v251, s[40:43], s80 offen sc1\n"
"v_mfma_f32_16x16x32_bf16 v[220:223], a[136:139], a[56:59], v[220:223]\n"
"ds_read_b64_tr_b16 a[134:135], v242 offset:14848\n"
"v_mfma_f32_16x16x32_bf16 v[220:223], a[140:143], a[60:63], v[220:223]\n"
"s_waitcnt lgkmcnt(6)\n"
"v_mfma_f32_16x16x32_bf16 v[224:227], a[144:147], a[32:35], v[224:227]\n"
"s_waitcnt lgkmcnt(4)\n"
"ds_read_b64_tr_b16 a[136:137], v240 offset:16384\n"
"v_mfma_f32_16x16x32_bf16 v[224:227], a[148:151], a[36:39], v[224:227]\n"
"ds_read_b64_tr_b16 a[138:139], v240 offset:20480\n"
"buffer_load_dwordx4 a[68:71], v251, s[40:43], s80 offen offset:1024 sc1\n"
"v_mfma_f32_16x16x32_bf16 v[232:235], a[144:147], a[40:43], v[232:235]\n"
"ds_read_b64_tr_b16 a[140:141], v240 offset:24576\n"
"v_mfma_f32_16x16x32_bf16 v[232:235], a[148:151], a[44:47], v[232:235]\n"
"s_waitcnt lgkmcnt(5)\n"
"ds_read_b64_tr_b16 a[142:143], v240 offset:28672\n"
"v_mfma_f32_16x16x32_bf16 v[228:231], a[128:131], a[32:35], v[228:231]\n"
"s_waitcnt lgkmcnt(4)\n"
"ds_read_b64_tr_b16 a[144:145], v240 offset:16392\n"
"v_mfma_f32_16x16x32_bf16 v[228:231], a[132:135], a[36:39], v[228:231]\n"
"ds_read_b64_tr_b16 a[146:147], v240 offset:20488\n"
"buffer_load_dwordx4 a[72:75], v251, s[40:43], s81 offen sc1\n"
"v_mfma_f32_16x16x32_bf16 v[236:239], a[128:131], a[40:43], v[236:239]\n"
"ds_read_b64_tr_b16 a[148:149], v240 offset:24584\n"
"v_mfma_f32_16x16x32_bf16 v[236:239], a[132:135], a[44:47], v[236:239]\n"
"s_waitcnt lgkmcnt(5)\n"
"ds_read_b64_tr_b16 a[150:151], v240 offset:28680\n"
"v_mfma_f32_16x16x32_bf16 v[0:3], a[136:139], a[32:35], v[0:3]\n"
"s_waitcnt lgkmcnt(4)\n"
"ds_read_b64_tr_b16 a[128:129], v240 offset:16896\n"
"v_mfma_f32_16x16x32_bf16 v[0:3], a[140:143], a[36:39], v[0:3]\n"
"buffer_load_dwordx4 a[76:79], v251, s[40:43], s81 offen offset:1024 sc1\n"
"v_mfma_f32_16x16x32_bf16 v[8:11], a[136:139], a[40:43], v[8:11]\n"
"ds_read_b64_tr_b16 a[130:131], v240 offset:20992\n"
"v_mfma_f32_16x16x32_bf16 v[8:11], a[140:143], a[44:47], v[8:11]\n"
"v_mfma_f32_16x16x32_bf16 v[16:19], a[136:139], a[48:51], v[16:19]\n"
"ds_read_b64_tr_b16 a[132:133], v240 offset:25088\n"
"v_mfma_f32_16x16x32_bf16 v[16:19], a[140:143], a[52:55], v[16:19]\n"
"buffer_load_dwordx4 a[80:83], v251, s[40:43], s82 offen sc1\n"
"v_mfma_f32_16x16x32_bf16 v[24:27], a[136:139], a[56:59], v[24:27]\n"
"ds_read_b64_tr_b16 a[134:135], v240 offset:29184\n"
"v_mfma_f32_16x16x32_bf16 v[24:27], a[140:143], a[60:63], v[24:27]\n"
"s_waitcnt lgkmcnt(6)\n"
"v_mfma_f32_16x16x32_bf16 v[4:7], a[144:147], a[32:35], v[4:7]\n"
"s_waitcnt lgkmcnt(4)\n"
"ds_read_b64_tr_b16 a[136:137], v240 offset:16904\n"
"v_mfma_f32_16x16x32_bf16 v[4:7], a[148:151], a[36:39], v[4:7]\n"
"buffer_load_dwordx4 a[84:87], v251, s[40:43], s82 offen offset:1024 sc1\n"
"v_mfma_f32_16x16x32_bf16 v[12:15], a[144:147], a[40:43], v[12:15]\n"
"ds_read_b64_tr_b16 a[138:139], v240 offset:21000\n"
"v_mfma_f32_16x16x32_bf16 v[12:15], a[148:151], a[44:47], v[12:15]\n"
"v_mfma_f32_16x16x32_bf16 v[20:23], a[144:147], a[48:51], v[20:23]\n"
"ds_read_b64_tr_b16 a[140:141], v240 offset:25096\n"
"v_mfma_f32_16x16x32_bf16 v[20:23], a[148:151], a[52:55], v[20:23]\n"
"buffer_load_dwordx4 a[88:91], v251, s[40:43], s83 offen sc1\n"
"v_mfma_f32_16x16x32_bf16 v[28:31], a[144:147], a[56:59], v[28:31]\n"
"ds_read_b64_tr_b16 a[142:143], v240 offset:29192\n"
"v_mfma_f32_16x16x32_bf16 v[28:31], a[148:151], a[60:63], v[28:31]\n"
"s_waitcnt lgkmcnt(6)\n"
"v_mfma_f32_16x16x32_bf16 v[32:35], a[128:131], a[32:35], v[32:35]\n"
"s_waitcnt lgkmcnt(4)\n"
"ds_read_b64_tr_b16 a[144:145], v240 offset:17408\n"
"v_mfma_f32_16x16x32_bf16 v[32:35], a[132:135], a[36:39], v[32:35]\n"
"buffer_load_dwordx4 a[92:95], v251, s[40:43], s83 offen offset:1024 sc1\n"
"v_mfma_f32_16x16x32_bf16 v[40:43], a[128:131], a[40:43], v[40:43]\n"
"ds_read_b64_tr_b16 a[146:147], v240 offset:21504\n"
"v_mfma_f32_16x16x32_bf16 v[40:43], a[132:135], a[44:47], v[40:43]\n"
"v_mfma_f32_16x16x32_bf16 v[48:51], a[128:131], a[48:51], v[48:51]\n"
"ds_read_b64_tr_b16 a[148:149], v240 offset:25600\n"
"v_mfma_f32_16x16x32_bf16 v[48:51], a[132:135], a[52:55], v[48:51]\n"
"v_mfma_f32_16x16x32_bf16 v[56:59], a[128:131], a[56:59], v[56:59]\n"
"ds_read_b64_tr_b16 a[150:151], v240 offset:29696\n"
"v_mfma_f32_16x16x32_bf16 v[56:59], a[132:135], a[60:63], v[56:59]\n"
"s_waitcnt lgkmcnt(6)\n"
"v_mfma_f32_16x16x32_bf16 v[36:39], a[136:139], a[32:35], v[36:39]\n"
"s_waitcnt lgkmcnt(4)\n"
"ds_read_b64_tr_b16 a[128:129], v240 offset:17416\n"
"v_mfma_f32_16x16x32_bf16 v[36:39], a[140:143], a[36:39], v[36:39]\n"
"v_mfma_f32_16x16x32_bf16 v[44:47], a[136:139], a[40:43], v[44:47]\n"
"ds_read_b64_tr_b16 a[130:131], v240 offset:21512\n"
"v_mfma_f32_16x16x32_bf16 v[44:47], a[140:143], a[44:47], v[44:47]\n"
"v_mfma_f32_16x16x32_bf16 v[52:55], a[136:139], a[48:51], v[52:55]\n"
"ds_read_b64_tr_b16 a[132:133], v240 offset:25608\n"
"v_mfma_f32_16x16x32_bf16 v[52:55], a[140:143], a[52:55], v[52:55]\n"
"v_mfma_f32_16x16x32_bf16 v[60:63], a[136:139], a[56:59], v[60:63]\n"
"ds_read_b64_tr_b16 a[134:135], v240 offset:29704\n"
"v_mfma_f32_16x16x32_bf16 v[60:63], a[140:143], a[60:63], v[60:63]\n"
"s_waitcnt lgkmcnt(6)\n"
"v_mfma_f32_16x16x32_bf16 v[64:67], a[144:147], a[32:35], v[64:67]\n"
"s_waitcnt lgkmcnt(4)\n"
"ds_read_b64_tr_b16 a[136:137], v240 offset:17920\n"
"v_mfma_f32_16x16x32_bf16 v[64:67], a[148:151], a[36:39], v[64:67]\n"
"v_mfma_f32_16x16x32_bf16 v[72:75], a[144:147], a[40:43], v[72:75]\n"
"ds_read_b64_tr_b16 a[138:139], v240 offset:22016\n"
"v_mfma_f32_16x16x32_bf16 v[72:75], a[148:151], a[44:47], v[72:75]\n"
"v_mfma_f32_16x16x32_bf16 v[80:83], a[144:147], a[48:51], v[80:83]\n"
"ds_read_b64_tr_b16 a[140:141], v240 offset:26112\n"
"v_mfma_f32_16x16x32_bf16 v[80:83], a[148:151], a[52:55], v[80:83]\n"
"v_mfma_f32_16x16x32_bf16 v[88:91], a[144:147], a[56:59], v[88:91]\n"
"ds_read_b64_tr_b16 a[142:143], v240 offset:30208\n"
"v_mfma_f32_16x16x32_bf16 v[88:91], a[148:151], a[60:63], v[88:91]\n"
"s_waitcnt lgkmcnt(6)\n"
"v_mfma_f32_16x16x32_bf16 v[68:71], a[128:131], a[32:35], v[68:71]\n"
"s_waitcnt lgkmcnt(4)\n"
"ds_read_b64_tr_b16 a[144:145], v240 offset:17928\n"
"v_mfma_f32_16x16x32_bf16 v[68:71], a[132:135], a[36:39], v[68:71]\n"
"v_mfma_f32_16x16x32_bf16 v[76:79], a[128:131], a[40:43], v[76:79]\n"
"ds_read_b64_tr_b16 a[146:147], v240 offset:22024\n"
"v_mfma_f32_16x16x32_bf16 v[76:79], a[132:135], a[44:47], v[76:79]\n"
"v_mfma_f32_16x16x32_bf16 v[84:87], a[128:131], a[48:51], v[84:87]\n"
"ds_read_b64_tr_b16 a[148:149], v240 offset:26120\n"
"v_mfma_f32_16x16x32_bf16 v[84:87], a[132:135], a[52:55], v[84:87]\n"
"v_mfma_f32_16x16x32_bf16 v[92:95], a[128:131], a[56:59], v[92:95]\n"
"ds_read_b64_tr_b16 a[150:151], v240 offset:30216\n"
"v_mfma_f32_16x16x32_bf16 v[92:95], a[132:135], a[60:63], v[92:95]\n"
"s_waitcnt lgkmcnt(6)\n"
"v_mfma_f32_16x16x32_bf16 v[96:99], a[136:139], a[32:35], v[96:99]\n"
"s_barrier\n"
"s_waitcnt lgkmcnt(4)\n"
"v_mfma_f32_16x16x32_bf16 v[96:99], a[140:143], a[36:39], v[96:99]\n"
"ds_read_b64_tr_b16 a[128:129], v241 offset:16384\n"
"v_mfma_f32_16x16x32_bf16 v[104:107], a[136:139], a[40:43], v[104:107]\n"
"v_mfma_f32_16x16x32_bf16 v[104:107], a[140:143], a[44:47], v[104:107]\n"
"s_add_u32 m0, s91, 0\n"
"ds_read_b64_tr_b16 a[130:131], v241 offset:18432\n"
"v_mfma_f32_16x16x32_bf16 v[112:115], a[136:139], a[48:51], v[112:115]\n"
"buffer_load_dwordx4 v247, s[44:47], s84 offen lds\n"
"v_mfma_f32_16x16x32_bf16 v[112:115], a[140:143], a[52:55], v[112:115]\n"
"ds_read_b64_tr_b16 a[132:133], v241 offset:20480\n"
"v_mfma_f32_16x16x32_bf16 v[120:123], a[136:139], a[56:59], v[120:123]\n"
"v_mfma_f32_16x16x32_bf16 v[120:123], a[140:143], a[60:63], v[120:123]\n"
"s_waitcnt lgkmcnt(5)\n"
"s_add_u32 m0, s91, 896\n"
"ds_read_b64_tr_b16 a[134:135], v241 offset:22528\n"
"v_mfma_f32_16x16x32_bf16 v[100:103], a[144:147], a[32:35], v[100:103]\n"
"s_waitcnt lgkmcnt(4)\n"
"ds_read_b64_tr_b16 a[136:137], v242 offset:16384\n"
"buffer_load_dwordx4 v247, s[44:47], s84 offen offset:128 lds\n"
"v_mfma_f32_16x16x32_bf16 v[100:103], a[148:151], a[36:39], v[100:103]\n"
"v_mfma_f32_16x16x32_bf16 v[108:111], a[144:147], a[40:43], v[108:111]\n"
"s_add_u32 s84, s84, 524288\n"
"ds_read_b64_tr_b16 a[138:139], v242 offset:18432\n"
"v_mfma_f32_16x16x32_bf16 v[108:111], a[148:151], a[44:47], v[108:111]\n"
"s_add_u32 m0, s92, 0\n"
"v_mfma_f32_16x16x32_bf16 v[116:119], a[144:147], a[48:51], v[116:119]\n"
"ds_read_b64_tr_b16 a[140:141], v242 offset:20480\n"
"buffer_load_dwordx4 v248, s[48:51], s86 offen lds\n"
"v_mfma_f32_16x16x32_bf16 v[116:119], a[148:151], a[52:55], v[116:119]\n"
"v_mfma_f32_16x16x32_bf16 v[124:127], a[144:147], a[56:59], v[124:127]\n"
"s_add_u32 s86, s86, 176128\n"
"ds_read_b64_tr_b16 a[142:143], v242 offset:22528\n"
"v_mfma_f32_16x16x32_bf16 v[124:127], a[148:151], a[60:63], v[124:127]\n"
"s_add_u32 m0, s91, 2048\n"
"s_waitcnt vmcnt(10) lgkmcnt(6)\n"
"v_mfma_f32_16x16x32_bf16 v[192:195], a[128:131], a[64:67], v[192:195]\n"
"s_waitcnt vmcnt(9) lgkmcnt(4)\n"
"buffer_load_dwordx4 v247, s[44:47], s85 offen lds\n"
"ds_read_b64_tr_b16 a[144:145], v241 offset:16896\n"
"v_mfma_f32_16x16x32_bf16 v[192:195], a[132:135], a[68:71], v[192:195]\n"
"s_waitcnt vmcnt(9)\n"
"v_mfma_f32_16x16x32_bf16 v[200:203], a[128:131], a[72:75], v[200:203]\n"
"s_waitcnt vmcnt(8)\n"
"ds_read_b64_tr_b16 a[146:147], v241 offset:18944\n"
"v_mfma_f32_16x16x32_bf16 v[200:203], a[132:135], a[76:79], v[200:203]\n"
"s_waitcnt vmcnt(7)\n"
"s_add_u32 m0, s91, 2944\n"
"v_mfma_f32_16x16x32_bf16 v[208:211], a[128:131], a[80:83], v[208:211]\n"
"s_waitcnt vmcnt(6)\n"
"buffer_load_dwordx4 v247, s[44:47], s85 offen offset:128 lds\n"
"ds_read_b64_tr_b16 a[148:149], v241 offset:20992\n"
"v_mfma_f32_16x16x32_bf16 v[208:211], a[132:135], a[84:87], v[208:211]\n"
"s_waitcnt vmcnt(6)\n"
"v_mfma_f32_16x16x32_bf16 v[216:219], a[128:131], a[88:91], v[216:219]\n"
"s_waitcnt vmcnt(5)\n"
"s_add_u32 s85, s85, 524288\n"
"ds_read_b64_tr_b16 a[150:151], v241 offset:23040\n"
"v_mfma_f32_16x16x32_bf16 v[216:219], a[132:135], a[92:95], v[216:219]\n"
"s_waitcnt lgkmcnt(6)\n"
"s_add_u32 m0, s92, 1024\n"
"v_mfma_f32_16x16x32_bf16 v[196:199], a[136:139], a[64:67], v[196:199]\n"
"s_waitcnt lgkmcnt(4)\n"
"buffer_load_dwordx4 v248, s[48:51], s87 offen lds\n"
"ds_read_b64_tr_b16 a[128:129], v242 offset:16896\n"
"v_mfma_f32_16x16x32_bf16 v[196:199], a[140:143], a[68:71], v[196:199]\n"
"s_add_u32 s87, s87, 176128\n"
"v_mfma_f32_16x16x32_bf16 v[204:207], a[136:139], a[72:75], v[204:207]\n"
"ds_read_b64_tr_b16 a[130:131], v242 offset:18944\n"
"v_mfma_f32_16x16x32_bf16 v[204:207], a[140:143], a[76:79], v[204:207]\n"
"v_mfma_f32_16x16x32_bf16 v[212:215], a[136:139], a[80:83], v[212:215]\n"
"ds_read_b64_tr_b16 a[132:133], v242 offset:20992\n"
"v_mfma_f32_16x16x32_bf16 v[212:215], a[140:143], a[84:87], v[212:215]\n"
"buffer_load_dwordx4 a[96:99], v251, s[40:43], s80 offen offset:2048 sc1\n"
"v_mfma_f32_16x16x32_bf16 v[220:223], a[136:139], a[88:91], v[220:223]\n"
"ds_read_b64_tr_b16 a[134:135], v242 offset:23040\n"
"v_mfma_f32_16x16x32_bf16 v[220:223], a[140:143], a[92:95], v[220:223]\n"
"s_waitcnt lgkmcnt(6)\n"
"v_mfma_f32_16x16x32_bf16 v[224:227], a[144:147], a[64:67], v[224:227]\n"
"s_waitcnt lgkmcnt(4)\n"
"ds_read_b64_tr_b16 a[136:137], v240 offset:32768\n"
"v_mfma_f32_16x16x32_bf16 v[224:227], a[148:151], a[68:71], v[224:227]\n"
"ds_read_b64_tr_b16 a[138:139], v240 offset:36864\n"
"buffer_load_dwordx4 a[100:103], v251, s[40:43], s80 offen offset:3072 sc1\n"
"v_mfma_f32_16x16x32_bf16 v[232:235], a[144:147], a[72:75], v[232:235]\n"
"ds_read_b64_tr_b16 a[140:141], v240 offset:40960\n"
"v_mfma_f32_16x16x32_bf16 v[232:235], a[148:151], a[76:79], v[232:235]\n"
"s_waitcnt lgkmcnt(5)\n"
"ds_read_b64_tr_b16 a[142:143], v240 offset:45056\n"
"v_mfma_f32_16x16x32_bf16 v[228:231], a[128:131], a[64:67], v[228:231]\n"
"s_waitcnt lgkmcnt(4)\n"
"s_add_u32 s80, s80, 4096\n"
"ds_read_b64_tr_b16 a[144:145], v240 offset:32776\n"
"v_mfma_f32_16x16x32_bf16 v[228:231], a[132:135], a[68:71], v[228:231]\n"
"ds_read_b64_tr_b16 a[146:147], v240 offset:36872\n"
"buffer_load_dwordx4 a[104:107], v251, s[40:43], s81 offen offset:2048 sc1\n"
"v_mfma_f32_16x16x32_bf16 v[236:239], a[128:131], a[72:75], v[236:239]\n"
"ds_read_b64_tr_b16 a[148:149], v240 offset:40968\n"
"v_mfma_f32_16x16x32_bf16 v[236:239], a[132:135], a[76:79], v[236:239]\n"
"s_waitcnt lgkmcnt(5)\n"
"ds_read_b64_tr_b16 a[150:151], v240 offset:45064\n"
"v_mfma_f32_16x16x32_bf16 v[0:3], a[136:139], a[64:67], v[0:3]\n"
"s_waitcnt lgkmcnt(4)\n"
"ds_read_b64_tr_b16 a[128:129], v240 offset:33280\n"
"v_mfma_f32_16x16x32_bf16 v[0:3], a[140:143], a[68:71], v[0:3]\n"
"buffer_load_dwordx4 a[108:111], v251, s[40:43], s81 offen offset:3072 sc1\n"
"v_mfma_f32_16x16x32_bf16 v[8:11], a[136:139], a[72:75], v[8:11]\n"
"ds_read_b64_tr_b16 a[130:131], v240 offset:37376\n"
"v_mfma_f32_16x16x32_bf16 v[8:11], a[140:143], a[76:79], v[8:11]\n"
"v_mfma_f32_16x16x32_bf16 v[16:19], a[136:139], a[80:83], v[16:19]\n"
"s_add_u32 s81, s81, 4096\n"
"ds_read_b64_tr_b16 a[132:133], v240 offset:41472\n"
"v_mfma_f32_16x16x32_bf16 v[16:19], a[140:143], a[84:87], v[16:19]\n"
"buffer_load_dwordx4 a[112:115], v251, s[40:43], s82 offen offset:2048 sc1\n"
"v_mfma_f32_16x16x32_bf16 v[24:27], a[136:139], a[88:91], v[24:27]\n"
"ds_read_b64_tr_b16 a[134:135], v240 offset:45568\n"
"v_mfma_f32_16x16x32_bf16 v[24:27], a[140:143], a[92:95], v[24:27]\n"
"s_waitcnt lgkmcnt(6)\n"
"v_mfma_f32_16x16x32_bf16 v[4:7], a[144:147], a[64:67], v[4:7]\n"
"s_waitcnt lgkmcnt(4)\n"
"ds_read_b64_tr_b16 a[136:137], v240 offset:33288\n"
"v_mfma_f32_16x16x32_bf16 v[4:7], a[148:151], a[68:71], v[4:7]\n"
"buffer_load_dwordx4 a[116:119], v251, s[40:43], s82 offen offset:3072 sc1\n"
"v_mfma_f32_16x16x32_bf16 v[12:15], a[144:147], a[72:75], v[12:15]\n"
"ds_read_b64_tr_b16 a[138:139], v240 offset:37384\n"
"v_mfma_f32_16x16x32_bf16 v[12:15], a[148:151], a[76:79], v[12:15]\n"
"v_mfma_f32_16x16x32_bf16 v[20:23], a[144:147], a[80:83], v[20:23]\n"
"s_add_u32 s82, s82, 4096\n"
"ds_read_b64_tr_b16 a[140:141], v240 offset:41480\n"
"v_mfma_f32_16x16x32_bf16 v[20:23], a[148:151], a[84:87], v[20:23]\n"
"buffer_load_dwordx4 a[120:123], v251, s[40:43], s83 offen offset:2048 sc1\n"
"v_mfma_f32_16x16x32_bf16 v[28:31], a[144:147], a[88:91], v[28:31]\n"
"ds_read_b64_tr_b16 a[142:143], v240 offset:45576\n"
"v_mfma_f32_16x16x32_bf16 v[28:31], a[148:151], a[92:95], v[28:31]\n"
"s_waitcnt lgkmcnt(6)\n"
"v_mfma_f32_16x16x32_bf16 v[32:35], a[128:131], a[64:67], v[32:35]\n"
"s_waitcnt lgkmcnt(4)\n"
"ds_read_b64_tr_b16 a[144:145], v240 offset:33792\n"
"v_mfma_f32_16x16x32_bf16 v[32:35], a[132:135], a[68:71], v[32:35]\n"
"buffer_load_dwordx4 a[124:127], v251, s[40:43], s83 offen offset:3072 sc1\n"
"v_mfma_f32_16x16x32_bf16 v[40:43], a[128:131], a[72:75], v[40:43]\n"
"s_add_u32 s83, s83, 4096\n"
"ds_read_b64_tr_b16 a[146:147], v240 offset:37888\n"
"v_mfma_f32_16x16x32_bf16 v[40:43], a[132:135], a[76:79], v[40:43]\n"
"v_mfma_f32_16x16x32_bf16 v[48:51], a[128:131], a[80:83], v[48:51]\n"
"ds_read_b64_tr_b16 a[148:149], v240 offset:41984\n"
"v_mfma_f32_16x16x32_bf16 v[48:51], a[132:135], a[84:87], v[48:51]\n"
"v_mfma_f32_16x16x32_bf16 v[56:59], a[128:131], a[88:91], v[56:59]\n"
"ds_read_b64_tr_b16 a[150:151], v240 offset:46080\n"
"v_mfma_f32_16x16x32_bf16 v[56:59], a[132:135], a[92:95], v[56:59]\n"
"s_waitcnt lgkmcnt(6)\n"
"v_mfma_f32_16x16x32_bf16 v[36:39], a[136:139], a[64:67], v[36:39]\n"
"s_waitcnt lgkmcnt(4)\n"
"ds_read_b64_tr_b16 a[128:129], v240 offset:33800\n"
"v_mfma_f32_16x16x32_bf16 v[36:39], a[140:143], a[68:71], v[36:39]\n"
"v_mfma_f32_16x16x32_bf16 v[44:47], a[136:139], a[72:75], v[44:47]\n"
"ds_read_b64_tr_b16 a[130:131], v240 offset:37896\n"
"v_mfma_f32_16x16x32_bf16 v[44:47], a[140:143], a[76:79], v[44:47]\n"
"v_mfma_f32_16x16x32_bf16 v[52:55], a[136:139], a[80:83], v[52:55]\n"
"ds_read_b64_tr_b16 a[132:133], v240 offset:41992\n"
"v_mfma_f32_16x16x32_bf16 v[52:55], a[140:143], a[84:87], v[52:55]\n"
"v_mfma_f32_16x16x32_bf16 v[60:63], a[136:139], a[88:91], v[60:63]\n"
"ds_read_b64_tr_b16 a[134:135], v240 offset:46088\n"
"v_mfma_f32_16x16x32_bf16 v[60:63], a[140:143], a[92:95], v[60:63]\n"
"s_waitcnt lgkmcnt(6)\n"
"v_mfma_f32_16x16x32_bf16 v[64:67], a[144:147], a[64:67], v[64:67]\n"
"s_waitcnt lgkmcnt(4)\n"
"ds_read_b64_tr_b16 a[136:137], v240 offset:34304\n"
"v_mfma_f32_16x16x32_bf16 v[64:67], a[148:151], a[68:71], v[64:67]\n"
"v_mfma_f32_16x16x32_bf16 v[72:75], a[144:147], a[72:75], v[72:75]\n"
"ds_read_b64_tr_b16 a[138:139], v240 offset:38400\n"
"v_mfma_f32_16x16x32_bf16 v[72:75], a[148:151], a[76:79], v[72:75]\n"
"v_mfma_f32_16x16x32_bf16 v[80:83], a[144:147], a[80:83], v[80:83]\n"
"ds_read_b64_tr_b16 a[140:141], v240 offset:42496\n"
"v_mfma_f32_16x16x32_bf16 v[80:83], a[148:151], a[84:87], v[80:83]\n"
"v_mfma_f32_16x16x32_bf16 v[88:91], a[144:147], a[88:91], v[88:91]\n"
"ds_read_b64_tr_b16 a[142:143], v240 offset:46592\n"
"v_mfma_f32_16x16x32_bf16 v[88:91], a[148:151], a[92:95], v[88:91]\n"
"s_waitcnt lgkmcnt(6)\n"
"v_mfma_f32_16x16x32_bf16 v[68:71], a[128:131], a[64:67], v[68:71]\n"
"s_waitcnt lgkmcnt(4)\n"
"ds_read_b64_tr_b16 a[144:145], v240 offset:34312\n"
"v_mfma_f32_16x16x32_bf16 v[68:71], a[132:135], a[68:71], v[68:71]\n"
"v_mfma_f32_16x16x32_bf16 v[76:79], a[128:131], a[72:75], v[76:79]\n"
"ds_read_b64_tr_b16 a[146:147], v240 offset:38408\n"
"v_mfma_f32_16x16x32_bf16 v[76:79], a[132:135], a[76:79], v[76:79]\n"
"v_mfma_f32_16x16x32_bf16 v[84:87], a[128:131], a[80:83], v[84:87]\n"
"ds_read_b64_tr_b16 a[148:149], v240 offset:42504\n"
"v_mfma_f32_16x16x32_bf16 v[84:87], a[132:135], a[84:87], v[84:87]\n"
"v_mfma_f32_16x16x32_bf16 v[92:95], a[128:131], a[88:91], v[92:95]\n"
"ds_read_b64_tr_b16 a[150:151], v240 offset:46600\n"
"v_mfma_f32_16x16x32_bf16 v[92:95], a[132:135], a[92:95], v[92:95]\n"
"s_waitcnt lgkmcnt(6)\n"
"v_mfma_f32_16x16x32_bf16 v[96:99], a[136:139], a[64:67], v[96:99]\n"
"s_barrier\n"
"s_waitcnt lgkmcnt(4)\n"
"v_mfma_f32_16x16x32_bf16 v[96:99], a[140:143], a[68:71], v[96:99]\n"
"ds_read_b64_tr_b16 a[128:129], v241 offset:24576\n"
"v_mfma_f32_16x16x32_bf16 v[104:107], a[136:139], a[72:75], v[104:107]\n"
"v_mfma_f32_16x16x32_bf16 v[104:107], a[140:143], a[76:79], v[104:107]\n"
"s_add_u32 m0, s91, 16384\n"
"ds_read_b64_tr_b16 a[130:131], v241 offset:26624\n"
"v_mfma_f32_16x16x32_bf16 v[112:115], a[136:139], a[80:83], v[112:115]\n"
"buffer_load_dwordx4 v247, s[44:47], s84 offen lds\n"
"v_mfma_f32_16x16x32_bf16 v[112:115], a[140:143], a[84:87], v[112:115]\n"
"ds_read_b64_tr_b16 a[132:133], v241 offset:28672\n"
"v_mfma_f32_16x16x32_bf16 v[120:123], a[136:139], a[88:91], v[120:123]\n"
"v_mfma_f32_16x16x32_bf16 v[120:123], a[140:143], a[92:95], v[120:123]\n"
"s_waitcnt lgkmcnt(5)\n"
"s_add_u32 m0, s91, 17280\n"
"ds_read_b64_tr_b16 a[134:135], v241 offset:30720\n"
"v_mfma_f32_16x16x32_bf16 v[100:103], a[144:147], a[64:67], v[100:103]\n"
"s_waitcnt lgkmcnt(4)\n"
"ds_read_b64_tr_b16 a[136:137], v242 offset:24576\n"
"buffer_load_dwordx4 v247, s[44:47], s84 offen offset:128 lds\n"
"v_mfma_f32_16x16x32_bf16 v[100:103], a[148:151], a[68:71], v[100:103]\n"
"v_mfma_f32_16x16x32_bf16 v[108:111], a[144:147], a[72:75], v[108:111]\n"
"s_add_u32 s84, s84, 524288\n"
"ds_read_b64_tr_b16 a[138:139], v242 offset:26624\n"
"v_mfma_f32_16x16x32_bf16 v[108:111], a[148:151], a[76:79], v[108:111]\n"
"s_add_u32 m0, s92, 8192\n"
"v_mfma_f32_16x16x32_bf16 v[116:119], a[144:147], a[80:83], v[116:119]\n"
"ds_read_b64_tr_b16 a[140:141], v242 offset:28672\n"
"buffer_load_dwordx4 v248, s[48:51], s86 offen lds\n"
"v_mfma_f32_16x16x32_bf16 v[116:119], a[148:151], a[84:87], v[116:119]\n"
"v_mfma_f32_16x16x32_bf16 v[124:127], a[144:147], a[88:91], v[124:127]\n"
"s_add_u32 s86, s86, 176128\n"
"ds_read_b64_tr_b16 a[142:143], v242 offset:30720\n"
"v_mfma_f32_16x16x32_bf16 v[124:127], a[148:151], a[92:95], v[124:127]\n"
"s_add_u32 m0, s91, 18432\n"
"s_waitcnt vmcnt(10) lgkmcnt(6)\n"
"v_mfma_f32_16x16x32_bf16 v[192:195], a[128:131], a[96:99], v[192:195]\n"
"s_waitcnt vmcnt(9) lgkmcnt(4)\n"
"buffer_load_dwordx4 v247, s[44:47], s85 offen lds\n"
"ds_read_b64_tr_b16 a[144:145], v241 offset:25088\n"
"v_mfma_f32_16x16x32_bf16 v[192:195], a[132:135], a[100:103], v[192:195]\n"
"s_waitcnt vmcnt(9)\n"
"v_mfma_f32_16x16x32_bf16 v[200:203], a[128:131], a[104:107], v[200:203]\n"
"s_waitcnt vmcnt(8)\n"
"ds_read_b64_tr_b16 a[146:147], v241 offset:27136\n"
"v_mfma_f32_16x16x32_bf16 v[200:203], a[132:135], a[108:111], v[200:203]\n"
"s_waitcnt vmcnt(7)\n"
"s_add_u32 m0, s91, 19328\n"
"v_mfma_f32_16x16x32_bf16 v[208:211], a[128:131], a[112:115], v[208:211]\n"
"s_waitcnt vmcnt(6)\n"
"buffer_load_dwordx4 v247, s[44:47], s85 offen offset:128 lds\n"
"ds_read_b64_tr_b16 a[148:149], v241 offset:29184\n"
"v_mfma_f32_16x16x32_bf16 v[208:211], a[132:135], a[116:119], v[208:211]\n"
"s_waitcnt vmcnt(6)\n"
"v_mfma_f32_16x16x32_bf16 v[216:219], a[128:131], a[120:123], v[216:219]\n"
"s_waitcnt vmcnt(5)\n"
"s_add_u32 s85, s85, 524288\n"
"ds_read_b64_tr_b16 a[150:151], v241 offset:31232\n"
"v_mfma_f32_16x16x32_bf16 v[216:219], a[132:135], a[124:127], v[216:219]\n"
"s_waitcnt lgkmcnt(6)\n"
"s_add_u32 m0, s92, 9216\n"
"v_mfma_f32_16x16x32_bf16 v[196:199], a[136:139], a[96:99], v[196:199]\n"
"s_waitcnt lgkmcnt(4)\n"
"buffer_load_dwordx4 v248, s[48:51], s87 offen lds\n"
"ds_read_b64_tr_b16 a[128:129], v242 offset:25088\n"
"v_mfma_f32_16x16x32_bf16 v[196:199], a[140:143], a[100:103], v[196:199]\n"
"s_add_u32 s87, s87, 176128\n"
"v_mfma_f32_16x16x32_bf16 v[204:207], a[136:139], a[104:107], v[204:207]\n"
"ds_read_b64_tr_b16 a[130:131], v242 offset:27136\n"
"v_mfma_f32_16x16x32_bf16 v[204:207], a[140:143], a[108:111], v[204:207]\n"
"v_mfma_f32_16x16x32_bf16 v[212:215], a[136:139], a[112:115], v[212:215]\n"
"ds_read_b64_tr_b16 a[132:133], v242 offset:29184\n"
"v_mfma_f32_16x16x32_bf16 v[212:215], a[140:143], a[116:119], v[212:215]\n"
"buffer_load_dwordx4 a[0:3], v251, s[40:43], s80 offen sc1\n"
"v_mfma_f32_16x16x32_bf16 v[220:223], a[136:139], a[120:123], v[220:223]\n"
"ds_read_b64_tr_b16 a[134:135], v242 offset:31232\n"
"v_mfma_f32_16x16x32_bf16 v[220:223], a[140:143], a[124:127], v[220:223]\n"
"s_waitcnt lgkmcnt(6)\n"
"v_mfma_f32_16x16x32_bf16 v[224:227], a[144:147], a[96:99], v[224:227]\n"
"s_waitcnt lgkmcnt(4)\n"
"ds_read_b64_tr_b16 a[136:137], v240 offset:49152\n"
"v_mfma_f32_16x16x32_bf16 v[224:227], a[148:151], a[100:103], v[224:227]\n"
"ds_read_b64_tr_b16 a[138:139], v240 offset:53248\n"
"buffer_load_dwordx4 a[4:7], v251, s[40:43], s80 offen offset:1024 sc1\n"
"v_mfma_f32_16x16x32_bf16 v[232:235], a[144:147], a[104:107], v[232:235]\n"
"ds_read_b64_tr_b16 a[140:141], v240 offset:57344\n"
"v_mfma_f32_16x16x32_bf16 v[232:235], a[148:151], a[108:111], v[232:235]\n"
"s_waitcnt lgkmcnt(5)\n"
"ds_read_b64_tr_b16 a[142:143], v240 offset:61440\n"
"v_mfma_f32_16x16x32_bf16 v[228:231], a[128:131], a[96:99], v[228:231]\n"
"s_waitcnt lgkmcnt(4)\n"
"ds_read_b64_tr_b16 a[144:145], v240 offset:49160\n"
"v_mfma_f32_16x16x32_bf16 v[228:231], a[132:135], a[100:103], v[228:231]\n"
"ds_read_b64_tr_b16 a[146:147], v240 offset:53256\n"
"buffer_load_dwordx4 a[8:11], v251, s[40:43], s81 offen sc1\n"
"v_mfma_f32_16x16x32_bf16 v[236:239], a[128:131], a[104:107], v[236:239]\n"
"ds_read_b64_tr_b16 a[148:149], v240 offset:57352\n"
"v_mfma_f32_16x16x32_bf16 v[236:239], a[132:135], a[108:111], v[236:239]\n"
"s_waitcnt lgkmcnt(5)\n"
"ds_read_b64_tr_b16 a[150:151], v240 offset:61448\n"
"v_mfma_f32_16x16x32_bf16 v[0:3], a[136:139], a[96:99], v[0:3]\n"
"s_waitcnt lgkmcnt(4)\n"
"ds_read_b64_tr_b16 a[128:129], v240 offset:49664\n"
"v_mfma_f32_16x16x32_bf16 v[0:3], a[140:143], a[100:103], v[0:3]\n"
"buffer_load_dwordx4 a[12:15], v251, s[40:43], s81 offen offset:1024 sc1\n"
"v_mfma_f32_16x16x32_bf16 v[8:11], a[136:139], a[104:107], v[8:11]\n"
"ds_read_b64_tr_b16 a[130:131], v240 offset:53760\n"
"v_mfma_f32_16x16x32_bf16 v[8:11], a[140:143], a[108:111], v[8:11]\n"
"v_mfma_f32_16x16x32_bf16 v[16:19], a[136:139], a[112:115], v[16:19]\n"
"ds_read_b64_tr_b16 a[132:133], v240 offset:57856\n"
"v_mfma_f32_16x16x32_bf16 v[16:19], a[140:143], a[116:119], v[16:19]\n"
"buffer_load_dwordx4 a[16:19], v251, s[40:43], s82 offen sc1\n"
"v_mfma_f32_16x16x32_bf16 v[24:27], a[136:139], a[120:123], v[24:27]\n"
"ds_read_b64_tr_b16 a[134:135], v240 offset:61952\n"
"v_mfma_f32_16x16x32_bf16 v[24:27], a[140:143], a[124:127], v[24:27]\n"
"s_waitcnt lgkmcnt(6)\n"
"v_mfma_f32_16x16x32_bf16 v[4:7], a[144:147], a[96:99], v[4:7]\n"
"s_waitcnt lgkmcnt(4)\n"
"ds_read_b64_tr_b16 a[136:137], v240 offset:49672\n"
"v_mfma_f32_16x16x32_bf16 v[4:7], a[148:151], a[100:103], v[4:7]\n"
"buffer_load_dwordx4 a[20:23], v251, s[40:43], s82 offen offset:1024 sc1\n"
"v_mfma_f32_16x16x32_bf16 v[12:15], a[144:147], a[104:107], v[12:15]\n"
"ds_read_b64_tr_b16 a[138:139], v240 offset:53768\n"
"v_mfma_f32_16x16x32_bf16 v[12:15], a[148:151], a[108:111], v[12:15]\n"
"v_mfma_f32_16x16x32_bf16 v[20:23], a[144:147], a[112:115], v[20:23]\n"
"ds_read_b64_tr_b16 a[140:141], v240 offset:57864\n"
"v_mfma_f32_16x16x32_bf16 v[20:23], a[148:151], a[116:119], v[20:23]\n"
"buffer_load_dwordx4 a[24:27], v251, s[40:43], s83 offen sc1\n"
"v_mfma_f32_16x16x32_bf16 v[28:31], a[144:147], a[120:123], v[28:31]\n"
"ds_read_b64_tr_b16 a[142:143], v240 offset:61960\n"
"v_mfma_f32_16x16x32_bf16 v[28:31], a[148:151], a[124:127], v[28:31]\n"
"s_waitcnt lgkmcnt(6)\n"
"v_mfma_f32_16x16x32_bf16 v[32:35], a[128:131], a[96:99], v[32:35]\n"
"s_waitcnt lgkmcnt(4)\n"
"ds_read_b64_tr_b16 a[144:145], v240 offset:50176\n"
"v_mfma_f32_16x16x32_bf16 v[32:35], a[132:135], a[100:103], v[32:35]\n"
"buffer_load_dwordx4 a[28:31], v251, s[40:43], s83 offen offset:1024 sc1\n"
"v_mfma_f32_16x16x32_bf16 v[40:43], a[128:131], a[104:107], v[40:43]\n"
"ds_read_b64_tr_b16 a[146:147], v240 offset:54272\n"
"v_mfma_f32_16x16x32_bf16 v[40:43], a[132:135], a[108:111], v[40:43]\n"
"v_mfma_f32_16x16x32_bf16 v[48:51], a[128:131], a[112:115], v[48:51]\n"
"ds_read_b64_tr_b16 a[148:149], v240 offset:58368\n"
"v_mfma_f32_16x16x32_bf16 v[48:51], a[132:135], a[116:119], v[48:51]\n"
"v_mfma_f32_16x16x32_bf16 v[56:59], a[128:131], a[120:123], v[56:59]\n"
"ds_read_b64_tr_b16 a[150:151], v240 offset:62464\n"
"v_mfma_f32_16x16x32_bf16 v[56:59], a[132:135], a[124:127], v[56:59]\n"
"s_waitcnt lgkmcnt(6)\n"
"v_mfma_f32_16x16x32_bf16 v[36:39], a[136:139], a[96:99], v[36:39]\n"
"s_waitcnt lgkmcnt(4)\n"
"ds_read_b64_tr_b16 a[128:129], v240 offset:50184\n"
"v_mfma_f32_16x16x32_bf16 v[36:39], a[140:143], a[100:103], v[36:39]\n"
"v_mfma_f32_16x16x32_bf16 v[44:47], a[136:139], a[104:107], v[44:47]\n"
"ds_read_b64_tr_b16 a[130:131], v240 offset:54280\n"
"v_mfma_f32_16x16x32_bf16 v[44:47], a[140:143], a[108:111], v[44:47]\n"
"v_mfma_f32_16x16x32_bf16 v[52:55], a[136:139], a[112:115], v[52:55]\n"
"ds_read_b64_tr_b16 a[132:133], v240 offset:58376\n"
"v_mfma_f32_16x16x32_bf16 v[52:55], a[140:143], a[116:119], v[52:55]\n"
"v_mfma_f32_16x16x32_bf16 v[60:63], a[136:139], a[120:123], v[60:63]\n"
"ds_read_b64_tr_b16 a[134:135], v240 offset:62472\n"
"v_mfma_f32_16x16x32_bf16 v[60:63], a[140:143], a[124:127], v[60:63]\n"
"s_waitcnt lgkmcnt(6)\n"
"v_mfma_f32_16x16x32_bf16 v[64:67], a[144:147], a[96:99], v[64:67]\n"
"s_waitcnt lgkmcnt(4)\n"
"ds_read_b64_tr_b16 a[136:137], v240 offset:50688\n"
"v_mfma_f32_16x16x32_bf16 v[64:67], a[148:151], a[100:103], v[64:67]\n"
"v_mfma_f32_16x16x32_bf16 v[72:75], a[144:147], a[104:107], v[72:75]\n"
"ds_read_b64_tr_b16 a[138:139], v240 offset:54784\n"
"v_mfma_f32_16x16x32_bf16 v[72:75], a[148:151], a[108:111], v[72:75]\n"
"v_mfma_f32_16x16x32_bf16 v[80:83], a[144:147], a[112:115], v[80:83]\n"
"ds_read_b64_tr_b16 a[140:141], v240 offset:58880\n"
"v_mfma_f32_16x16x32_bf16 v[80:83], a[148:151], a[116:119], v[80:83]\n"
"v_mfma_f32_16x16x32_bf16 v[88:91], a[144:147], a[120:123], v[88:91]\n"
"ds_read_b64_tr_b16 a[142:143], v240 offset:62976\n"
"v_mfma_f32_16x16x32_bf16 v[88:91], a[148:151], a[124:127], v[88:91]\n"
"s_waitcnt lgkmcnt(6)\n"
"v_mfma_f32_16x16x32_bf16 v[68:71], a[128:131], a[96:99], v[68:71]\n"
"s_waitcnt lgkmcnt(4)\n"
"ds_read_b64_tr_b16 a[144:145], v240 offset:50696\n"
"v_mfma_f32_16x16x32_bf16 v[68:71], a[132:135], a[100:103], v[68:71]\n"
"v_mfma_f32_16x16x32_bf16 v[76:79], a[128:131], a[104:107], v[76:79]\n"
"ds_read_b64_tr_b16 a[146:147], v240 offset:54792\n"
"v_mfma_f32_16x16x32_bf16 v[76:79], a[132:135], a[108:111], v[76:79]\n"
"v_mfma_f32_16x16x32_bf16 v[84:87], a[128:131], a[112:115], v[84:87]\n"
"ds_read_b64_tr_b16 a[148:149], v240 offset:58888\n"
"v_mfma_f32_16x16x32_bf16 v[84:87], a[132:135], a[116:119], v[84:87]\n"
"v_mfma_f32_16x16x32_bf16 v[92:95], a[128:131], a[120:123], v[92:95]\n"
"ds_read_b64_tr_b16 a[150:151], v240 offset:62984\n"
"v_mfma_f32_16x16x32_bf16 v[92:95], a[132:135], a[124:127], v[92:95]\n"
"s_waitcnt lgkmcnt(6)\n"
"v_mfma_f32_16x16x32_bf16 v[96:99], a[136:139], a[96:99], v[96:99]\n"
"s_barrier\n"
"s_waitcnt lgkmcnt(4)\n"
"v_mfma_f32_16x16x32_bf16 v[96:99], a[140:143], a[100:103], v[96:99]\n"
"ds_read_b64_tr_b16 a[128:129], v241 offset:0\n"
"v_mfma_f32_16x16x32_bf16 v[104:107], a[136:139], a[104:107], v[104:107]\n"
"v_mfma_f32_16x16x32_bf16 v[104:107], a[140:143], a[108:111], v[104:107]\n"
"s_add_u32 m0, s91, 32768\n"
"ds_read_b64_tr_b16 a[130:131], v241 offset:2048\n"
"v_mfma_f32_16x16x32_bf16 v[112:115], a[136:139], a[112:115], v[112:115]\n"
"buffer_load_dwordx4 v247, s[44:47], s84 offen lds\n"
"v_mfma_f32_16x16x32_bf16 v[112:115], a[140:143], a[116:119], v[112:115]\n"
"ds_read_b64_tr_b16 a[132:133], v241 offset:4096\n"
"v_mfma_f32_16x16x32_bf16 v[120:123], a[136:139], a[120:123], v[120:123]\n"
"v_mfma_f32_16x16x32_bf16 v[120:123], a[140:143], a[124:127], v[120:123]\n"
"s_waitcnt lgkmcnt(5)\n"
"s_add_u32 m0, s91, 33664\n"
"ds_read_b64_tr_b16 a[134:135], v241 offset:6144\n"
"v_mfma_f32_16x16x32_bf16 v[100:103], a[144:147], a[96:99], v[100:103]\n"
"s_waitcnt lgkmcnt(4)\n"
"ds_read_b64_tr_b16 a[136:137], v242 offset:0\n"
"buffer_load_dwordx4 v247, s[44:47], s84 offen offset:128 lds\n"
"v_mfma_f32_16x16x32_bf16 v[100:103], a[148:151], a[100:103], v[100:103]\n"
"v_mfma_f32_16x16x32_bf16 v[108:111], a[144:147], a[104:107], v[108:111]\n"
"s_add_u32 s84, s84, 524288\n"
"ds_read_b64_tr_b16 a[138:139], v242 offset:2048\n"
"v_mfma_f32_16x16x32_bf16 v[108:111], a[148:151], a[108:111], v[108:111]\n"
"s_add_u32 m0, s92, 16384\n"
"v_mfma_f32_16x16x32_bf16 v[116:119], a[144:147], a[112:115], v[116:119]\n"
"ds_read_b64_tr_b16 a[140:141], v242 offset:4096\n"
"buffer_load_dwordx4 v248, s[48:51], s86 offen lds\n"
"v_mfma_f32_16x16x32_bf16 v[116:119], a[148:151], a[116:119], v[116:119]\n"
"v_mfma_f32_16x16x32_bf16 v[124:127], a[144:147], a[120:123], v[124:127]\n"
"s_add_u32 s86, s86, 176128\n"
"ds_read_b64_tr_b16 a[142:143], v242 offset:6144\n"
"v_mfma_f32_16x16x32_bf16 v[124:127], a[148:151], a[124:127], v[124:127]\n"
"s_add_u32 m0, s91, 34816\n"
"s_waitcnt vmcnt(10) lgkmcnt(6)\n"
"v_mfma_f32_16x16x32_bf16 v[192:195], a[128:131], a[0:3], v[192:195]\n"
"s_waitcnt vmcnt(9) lgkmcnt(4)\n"
"buffer_load_dwordx4 v247, s[44:47], s85 offen lds\n"
"ds_read_b64_tr_b16 a[144:145], v241 offset:512\n"
"v_mfma_f32_16x16x32_bf16 v[192:195], a[132:135], a[4:7], v[192:195]\n"
"s_waitcnt vmcnt(9)\n"
"v_mfma_f32_16x16x32_bf16 v[200:203], a[128:131], a[8:11], v[200:203]\n"
"s_waitcnt vmcnt(8)\n"
"ds_read_b64_tr_b16 a[146:147], v241 offset:2560\n"
"v_mfma_f32_16x16x32_bf16 v[200:203], a[132:135], a[12:15], v[200:203]\n"
"s_waitcnt vmcnt(7)\n"
"s_add_u32 m0, s91, 35712\n"
"v_mfma_f32_16x16x32_bf16 v[208:211], a[128:131], a[16:19], v[208:211]\n"
"s_waitcnt vmcnt(6)\n"
"buffer_load_dwordx4 v247, s[44:47], s85 offen offset:128 lds\n"
"ds_read_b64_tr_b16 a[148:149], v241 offset:4608\n"
"v_mfma_f32_16x16x32_bf16 v[208:211], a[132:135], a[20:23], v[208:211]\n"
"s_waitcnt vmcnt(6)\n"
"v_mfma_f32_16x16x32_bf16 v[216:219], a[128:131], a[24:27], v[216:219]\n"
"s_waitcnt vmcnt(5)\n"
"s_add_u32 s85, s85, 524288\n"
"ds_read_b64_tr_b16 a[150:151], v241 offset:6656\n"
"v_mfma_f32_16x16x32_bf16 v[216:219], a[132:135], a[28:31], v[216:219]\n"
"s_waitcnt lgkmcnt(6)\n"
"s_add_u32 m0, s92, 17408\n"
"v_mfma_f32_16x16x32_bf16 v[196:199], a[136:139], a[0:3], v[196:199]\n"
"s_waitcnt lgkmcnt(4)\n"
"buffer_load_dwordx4 v248, s[48:51], s87 offen lds\n"
"ds_read_b64_tr_b16 a[128:129], v242 offset:512\n"
"v_mfma_f32_16x16x32_bf16 v[196:199], a[140:143], a[4:7], v[196:199]\n"
"s_add_u32 s87, s87, 176128\n"
"v_mfma_f32_16x16x32_bf16 v[204:207], a[136:139], a[8:11], v[204:207]\n"
"ds_read_b64_tr_b16 a[130:131], v242 offset:2560\n"
"v_mfma_f32_16x16x32_bf16 v[204:207], a[140:143], a[12:15], v[204:207]\n"
"v_mfma_f32_16x16x32_bf16 v[212:215], a[136:139], a[16:19], v[212:215]\n"
"ds_read_b64_tr_b16 a[132:133], v242 offset:4608\n"
"v_mfma_f32_16x16x32_bf16 v[212:215], a[140:143], a[20:23], v[212:215]\n"
"buffer_load_dwordx4 a[32:35], v251, s[40:43], s80 offen offset:2048 sc1\n"
"v_mfma_f32_16x16x32_bf16 v[220:223], a[136:139], a[24:27], v[220:223]\n"
"ds_read_b64_tr_b16 a[134:135], v242 offset:6656\n"
"v_mfma_f32_16x16x32_bf16 v[220:223], a[140:143], a[28:31], v[220:223]\n"
"s_waitcnt lgkmcnt(6)\n"
"v_mfma_f32_16x16x32_bf16 v[224:227], a[144:147], a[0:3], v[224:227]\n"
"s_waitcnt lgkmcnt(4)\n"
"ds_read_b64_tr_b16 a[136:137], v240 offset:0\n"
"v_mfma_f32_16x16x32_bf16 v[224:227], a[148:151], a[4:7], v[224:227]\n"
"ds_read_b64_tr_b16 a[138:139], v240 offset:4096\n"
"buffer_load_dwordx4 a[36:39], v251, s[40:43], s80 offen offset:3072 sc1\n"
"v_mfma_f32_16x16x32_bf16 v[232:235], a[144:147], a[8:11], v[232:235]\n"
"ds_read_b64_tr_b16 a[140:141], v240 offset:8192\n"
"v_mfma_f32_16x16x32_bf16 v[232:235], a[148:151], a[12:15], v[232:235]\n"
"s_waitcnt lgkmcnt(5)\n"
"ds_read_b64_tr_b16 a[142:143], v240 offset:12288\n"
"v_mfma_f32_16x16x32_bf16 v[228:231], a[128:131], a[0:3], v[228:231]\n"
"s_waitcnt lgkmcnt(4)\n"
"s_add_u32 s80, s80, 4096\n"
"ds_read_b64_tr_b16 a[144:145], v240 offset:8\n"
"v_mfma_f32_16x16x32_bf16 v[228:231], a[132:135], a[4:7], v[228:231]\n"
"ds_read_b64_tr_b16 a[146:147], v240 offset:4104\n"
"buffer_load_dwordx4 a[40:43], v251, s[40:43], s81 offen offset:2048 sc1\n"
"v_mfma_f32_16x16x32_bf16 v[236:239], a[128:131], a[8:11], v[236:239]\n"
"ds_read_b64_tr_b16 a[148:149], v240 offset:8200\n"
"v_mfma_f32_16x16x32_bf16 v[236:239], a[132:135], a[12:15], v[236:239]\n"
"s_waitcnt lgkmcnt(5)\n"
"ds_read_b64_tr_b16 a[150:151], v240 offset:12296\n"
"v_mfma_f32_16x16x32_bf16 v[0:3], a[136:139], a[0:3], v[0:3]\n"
"s_waitcnt lgkmcnt(4)\n"
"ds_read_b64_tr_b16 a[128:129], v240 offset:512\n"
"v_mfma_f32_16x16x32_bf16 v[0:3], a[140:143], a[4:7], v[0:3]\n"
"buffer_load_dwordx4 a[44:47], v251, s[40:43], s81 offen offset:3072 sc1\n"
"v_mfma_f32_16x16x32_bf16 v[8:11], a[136:139], a[8:11], v[8:11]\n"
"ds_read_b64_tr_b16 a[130:131], v240 offset:4608\n"
"v_mfma_f32_16x16x32_bf16 v[8:11], a[140:143], a[12:15], v[8:11]\n"
"v_mfma_f32_16x16x32_bf16 v[16:19], a[136:139], a[16:19], v[16:19]\n"
"s_add_u32 s81, s81, 4096\n"
"ds_read_b64_tr_b16 a[132:133], v240 offset:8704\n"
"v_mfma_f32_16x16x32_bf16 v[16:19], a[140:143], a[20:23], v[16:19]\n"
"buffer_load_dwordx4 a[48:51], v251, s[40:43], s82 offen offset:2048 sc1\n"
"v_mfma_f32_16x16x32_bf16 v[24:27], a[136:139], a[24:27], v[24:27]\n"
"ds_read_b64_tr_b16 a[134:135], v240 offset:12800\n"
"v_mfma_f32_16x16x32_bf16 v[24:27], a[140:143], a[28:31], v[24:27]\n"
"s_waitcnt lgkmcnt(6)\n"
"v_mfma_f32_16x16x32_bf16 v[4:7], a[144:147], a[0:3], v[4:7]\n"
"s_waitcnt lgkmcnt(4)\n"
"ds_read_b64_tr_b16 a[136:137], v240 offset:520\n"
"v_mfma_f32_16x16x32_bf16 v[4:7], a[148:151], a[4:7], v[4:7]\n"
"buffer_load_dwordx4 a[52:55], v251, s[40:43], s82 offen offset:3072 sc1\n"
"v_mfma_f32_16x16x32_bf16 v[12:15], a[144:147], a[8:11], v[12:15]\n"
"ds_read_b64_tr_b16 a[138:139], v240 offset:4616\n"
"v_mfma_f32_16x16x32_bf16 v[12:15], a[148:151], a[12:15], v[12:15]\n"
"v_mfma_f32_16x16x32_bf16 v[20:23], a[144:147], a[16:19], v[20:23]\n"
"s_add_u32 s82, s82, 4096\n"
"ds_read_b64_tr_b16 a[140:141], v240 offset:8712\n"
"v_mfma_f32_16x16x32_bf16 v[20:23], a[148:151], a[20:23], v[20:23]\n"
"buffer_load_dwordx4 a[56:59], v251, s[40:43], s83 offen offset:2048 sc1\n"
"v_mfma_f32_16x16x32_bf16 v[28:31], a[144:147], a[24:27], v[28:31]\n"
"ds_read_b64_tr_b16 a[142:143], v240 offset:12808\n"
"v_mfma_f32_16x16x32_bf16 v[28:31], a[148:151], a[28:31], v[28:31]\n"
"s_waitcnt lgkmcnt(6)\n"
"v_mfma_f32_16x16x32_bf16 v[32:35], a[128:131], a[0:3], v[32:35]\n"
"s_waitcnt lgkmcnt(4)\n"
"ds_read_b64_tr_b16 a[144:145], v240 offset:1024\n"
"v_mfma_f32_16x16x32_bf16 v[32:35], a[132:135], a[4:7], v[32:35]\n"
"buffer_load_dwordx4 a[60:63], v251, s[40:43], s83 offen offset:3072 sc1\n"
"v_mfma_f32_16x16x32_bf16 v[40:43], a[128:131], a[8:11], v[40:43]\n"
"s_add_u32 s83, s83, 4096\n"
"ds_read_b64_tr_b16 a[146:147], v240 offset:5120\n"
"v_mfma_f32_16x16x32_bf16 v[40:43], a[132:135], a[12:15], v[40:43]\n"
"v_mfma_f32_16x16x32_bf16 v[48:51], a[128:131], a[16:19], v[48:51]\n"
"ds_read_b64_tr_b16 a[148:149], v240 offset:9216\n"
"v_mfma_f32_16x16x32_bf16 v[48:51], a[132:135], a[20:23], v[48:51]\n"
"v_mfma_f32_16x16x32_bf16 v[56:59], a[128:131], a[24:27], v[56:59]\n"
"ds_read_b64_tr_b16 a[150:151], v240 offset:13312\n"
"v_mfma_f32_16x16x32_bf16 v[56:59], a[132:135], a[28:31], v[56:59]\n"
"s_waitcnt lgkmcnt(6)\n"
"v_mfma_f32_16x16x32_bf16 v[36:39], a[136:139], a[0:3], v[36:39]\n"
"s_waitcnt lgkmcnt(4)\n"
"ds_read_b64_tr_b16 a[128:129], v240 offset:1032\n"
"v_mfma_f32_16x16x32_bf16 v[36:39], a[140:143], a[4:7], v[36:39]\n"
"v_mfma_f32_16x16x32_bf16 v[44:47], a[136:139], a[8:11], v[44:47]\n"
"ds_read_b64_tr_b16 a[130:131], v240 offset:5128\n"
"v_mfma_f32_16x16x32_bf16 v[44:47], a[140:143], a[12:15], v[44:47]\n"
"v_mfma_f32_16x16x32_bf16 v[52:55], a[136:139], a[16:19], v[52:55]\n"
"ds_read_b64_tr_b16 a[132:133], v240 offset:9224\n"
"v_mfma_f32_16x16x32_bf16 v[52:55], a[140:143], a[20:23], v[52:55]\n"
"v_mfma_f32_16x16x32_bf16 v[60:63], a[136:139], a[24:27], v[60:63]\n"
"ds_read_b64_tr_b16 a[134:135], v240 offset:13320\n"
"v_mfma_f32_16x16x32_bf16 v[60:63], a[140:143], a[28:31], v[60:63]\n"
"s_waitcnt lgkmcnt(6)\n"
"v_mfma_f32_16x16x32_bf16 v[64:67], a[144:147], a[0:3], v[64:67]\n"
"s_waitcnt lgkmcnt(4)\n"
"ds_read_b64_tr_b16 a[136:137], v240 offset:1536\n"
"v_mfma_f32_16x16x32_bf16 v[64:67], a[148:151], a[4:7], v[64:67]\n"
"v_mfma_f32_16x16x32_bf16 v[72:75], a[144:147], a[8:11], v[72:75]\n"
"ds_read_b64_tr_b16 a[138:139], v240 offset:5632\n"
"v_mfma_f32_16x16x32_bf16 v[72:75], a[148:151], a[12:15], v[72:75]\n"
"v_mfma_f32_16x16x32_bf16 v[80:83], a[144:147], a[16:19], v[80:83]\n"
"ds_read_b64_tr_b16 a[140:141], v240 offset:9728\n"
"v_mfma_f32_16x16x32_bf16 v[80:83], a[148:151], a[20:23], v[80:83]\n"
"v_mfma_f32_16x16x32_bf16 v[88:91], a[144:147], a[24:27], v[88:91]\n"
"ds_read_b64_tr_b16 a[142:143], v240 offset:13824\n"
"v_mfma_f32_16x16x32_bf16 v[88:91], a[148:151], a[28:31], v[88:91]\n"
"s_waitcnt lgkmcnt(6)\n"
"v_mfma_f32_16x16x32_bf16 v[68:71], a[128:131], a[0:3], v[68:71]\n"
"s_waitcnt lgkmcnt(4)\n"
"ds_read_b64_tr_b16 a[144:145], v240 offset:1544\n"
"v_mfma_f32_16x16x32_bf16 v[68:71], a[132:135], a[4:7], v[68:71]\n"
"v_mfma_f32_16x16x32_bf16 v[76:79], a[128:131], a[8:11], v[76:79]\n"
"ds_read_b64_tr_b16 a[146:147], v240 offset:5640\n"
"v_mfma_f32_16x16x32_bf16 v[76:79], a[132:135], a[12:15], v[76:79]\n"
"v_mfma_f32_16x16x32_bf16 v[84:87], a[128:131], a[16:19], v[84:87]\n"
"ds_read_b64_tr_b16 a[148:149], v240 offset:9736\n"
"v_mfma_f32_16x16x32_bf16 v[84:87], a[132:135], a[20:23], v[84:87]\n"
"v_mfma_f32_16x16x32_bf16 v[92:95], a[128:131], a[24:27], v[92:95]\n"
"ds_read_b64_tr_b16 a[150:151], v240 offset:13832\n"
"v_mfma_f32_16x16x32_bf16 v[92:95], a[132:135], a[28:31], v[92:95]\n"
"s_waitcnt lgkmcnt(6)\n"
"v_mfma_f32_16x16x32_bf16 v[96:99], a[136:139], a[0:3], v[96:99]\n"
"s_barrier\n"
"s_waitcnt lgkmcnt(4)\n"
"v_mfma_f32_16x16x32_bf16 v[96:99], a[140:143], a[4:7], v[96:99]\n"
"ds_read_b64_tr_b16 a[128:129], v241 offset:8192\n"
"v_mfma_f32_16x16x32_bf16 v[104:107], a[136:139], a[8:11], v[104:107]\n"
"v_mfma_f32_16x16x32_bf16 v[104:107], a[140:143], a[12:15], v[104:107]\n"
"s_add_u32 m0, s91, 49152\n"
"ds_read_b64_tr_b16 a[130:131], v241 offset:10240\n"
"v_mfma_f32_16x16x32_bf16 v[112:115], a[136:139], a[16:19], v[112:115]\n"
"buffer_load_dwordx4 v247, s[44:47], s84 offen lds\n"
"v_mfma_f32_16x16x32_bf16 v[112:115], a[140:143], a[20:23], v[112:115]\n"
"ds_read_b64_tr_b16 a[132:133], v241 offset:12288\n"
"v_mfma_f32_16x16x32_bf16 v[120:123], a[136:139], a[24:27], v[120:123]\n"
"v_mfma_f32_16x16x32_bf16 v[120:123], a[140:143], a[28:31], v[120:123]\n"
"s_waitcnt lgkmcnt(5)\n"
"s_add_u32 m0, s91, 50048\n"
"ds_read_b64_tr_b16 a[134:135], v241 offset:14336\n"
"v_mfma_f32_16x16x32_bf16 v[100:103], a[144:147], a[0:3], v[100:103]\n"
"s_waitcnt lgkmcnt(4)\n"
"ds_read_b64_tr_b16 a[136:137], v242 offset:8192\n"
"buffer_load_dwordx4 v247, s[44:47], s84 offen offset:128 lds\n"
"v_mfma_f32_16x16x32_bf16 v[100:103], a[148:151], a[4:7], v[100:103]\n"
"v_mfma_f32_16x16x32_bf16 v[108:111], a[144:147], a[8:11], v[108:111]\n"
"s_add_u32 s84, s84, 524288\n"
"ds_read_b64_tr_b16 a[138:139], v242 offset:10240\n"
"v_mfma_f32_16x16x32_bf16 v[108:111], a[148:151], a[12:15], v[108:111]\n"
"s_add_u32 m0, s92, 24576\n"
"v_mfma_f32_16x16x32_bf16 v[116:119], a[144:147], a[16:19], v[116:119]\n"
"ds_read_b64_tr_b16 a[140:141], v242 offset:12288\n"
"buffer_load_dwordx4 v248, s[48:51], s86 offen lds\n"
"v_mfma_f32_16x16x32_bf16 v[116:119], a[148:151], a[20:23], v[116:119]\n"
"v_mfma_f32_16x16x32_bf16 v[124:127], a[144:147], a[24:27], v[124:127]\n"
"s_add_u32 s86, s86, 176128\n"
"ds_read_b64_tr_b16 a[142:143], v242 offset:14336\n"
"v_mfma_f32_16x16x32_bf16 v[124:127], a[148:151], a[28:31], v[124:127]\n"
"s_add_u32 m0, s91, 51200\n"
"s_waitcnt vmcnt(10) lgkmcnt(6)\n"
"v_mfma_f32_16x16x32_bf16 v[192:195], a[128:131], a[32:35], v[192:195]\n"
"s_waitcnt vmcnt(9) lgkmcnt(4)\n"
"s_lshr_b32 s98, s15, 3\n"
"s_and_b32 s99, s38, 1\n"
"s_mul_i32 s14, s99, -1024\n"
"s_add_u32 s14, s14, 32768\n"
"s_mul_i32 s99, s99, -3072\n"
"s_sub_u32 s99, s99, 8192\n"
"s_cmp_gt_u32 s38, 1\n"
"s_cselect_b32 s14, s99, s14\n"
"s_cselect_b32 s16, s56, s52\n"
"s_cselect_b32 s17, s57, s53\n"
"s_cselect_b32 s18, s58, s54\n"
"s_mov_b32 s19, 0x20000\n"
"s_add_u32 s14, s14, s98\n"
"buffer_load_dwordx4 v247, s[44:47], s85 offen lds\n"
"ds_read_b64_tr_b16 a[144:145], v241 offset:8704\n"
"v_mfma_f32_16x16x32_bf16 v[192:195], a[132:135], a[36:39], v[192:195]\n"
"s_waitcnt vmcnt(9)\n"
"v_cmp_gt_u32 vcc, 256, v251\n"
"v_mov_b32 v254, 0x40000000\n"
"v_lshlrev_b32 v255, 2, v251\n"
"v_mfma_f32_16x16x32_bf16 v[200:203], a[128:131], a[40:43], v[200:203]\n"
"s_waitcnt vmcnt(8)\n"
"ds_read_b64_tr_b16 a[146:147], v241 offset:10752\n"
"v_cndmask_b32 v255, v254, v255, vcc\n"
"v_mfma_f32_16x16x32_bf16 v[200:203], a[132:135], a[44:47], v[200:203]\n"
"s_waitcnt vmcnt(7)\n"
"s_add_u32 m0, s91, 52096\n"
"v_mfma_f32_16x16x32_bf16 v[208:211], a[128:131], a[48:51], v[208:211]\n"
"s_waitcnt vmcnt(6)\n"
"buffer_load_dwordx4 v247, s[44:47], s85 offen offset:128 lds\n"
"ds_read_b64_tr_b16 a[148:149], v241 offset:12800\n"
"v_mfma_f32_16x16x32_bf16 v[208:211], a[132:135], a[52:55], v[208:211]\n"
"s_waitcnt vmcnt(6)\n"
"v_mfma_f32_16x16x32_bf16 v[216:219], a[128:131], a[56:59], v[216:219]\n"
"s_waitcnt vmcnt(5)\n"
"s_add_u32 s85, s85, 524288\n"
"ds_read_b64_tr_b16 a[150:151], v241 offset:14848\n"
"v_mfma_f32_16x16x32_bf16 v[216:219], a[132:135], a[60:63], v[216:219]\n"
"s_waitcnt lgkmcnt(6)\n"
"s_add_u32 m0, s92, 25600\n"
"v_mfma_f32_16x16x32_bf16 v[196:199], a[136:139], a[32:35], v[196:199]\n"
"s_waitcnt lgkmcnt(4)\n"
"buffer_load_dwordx4 v248, s[48:51], s87 offen lds\n"
"ds_read_b64_tr_b16 a[128:129], v242 offset:8704\n"
"v_mfma_f32_16x16x32_bf16 v[196:199], a[140:143], a[36:39], v[196:199]\n"
"s_add_u32 s87, s87, 176128\n"
"v_mfma_f32_16x16x32_bf16 v[204:207], a[136:139], a[40:43], v[204:207]\n"
"ds_read_b64_tr_b16 a[130:131], v242 offset:10752\n"
"v_mfma_f32_16x16x32_bf16 v[204:207], a[140:143], a[44:47], v[204:207]\n"
"v_mfma_f32_16x16x32_bf16 v[212:215], a[136:139], a[48:51], v[212:215]\n"
"ds_read_b64_tr_b16 a[132:133], v242 offset:12800\n"
"v_mfma_f32_16x16x32_bf16 v[212:215], a[140:143], a[52:55], v[212:215]\n"
"buffer_load_dwordx4 a[64:67], v251, s[40:43], s80 offen sc1\n"
"v_mfma_f32_16x16x32_bf16 v[220:223], a[136:139], a[56:59], v[220:223]\n"
"ds_read_b64_tr_b16 a[134:135], v242 offset:14848\n"
"v_mfma_f32_16x16x32_bf16 v[220:223], a[140:143], a[60:63], v[220:223]\n"
"s_waitcnt lgkmcnt(6)\n"
"v_mfma_f32_16x16x32_bf16 v[224:227], a[144:147], a[32:35], v[224:227]\n"
"s_waitcnt lgkmcnt(4)\n"
"ds_read_b64_tr_b16 a[136:137], v240 offset:16384\n"
"v_mfma_f32_16x16x32_bf16 v[224:227], a[148:151], a[36:39], v[224:227]\n"
"ds_read_b64_tr_b16 a[138:139], v240 offset:20480\n"
"buffer_load_dwordx4 a[68:71], v251, s[40:43], s80 offen offset:1024 sc1\n"
"v_mfma_f32_16x16x32_bf16 v[232:235], a[144:147], a[40:43], v[232:235]\n"
"ds_read_b64_tr_b16 a[140:141], v240 offset:24576\n"
"v_mfma_f32_16x16x32_bf16 v[232:235], a[148:151], a[44:47], v[232:235]\n"
"s_waitcnt lgkmcnt(5)\n"
"ds_read_b64_tr_b16 a[142:143], v240 offset:28672\n"
"v_mfma_f32_16x16x32_bf16 v[228:231], a[128:131], a[32:35], v[228:231]\n"
"s_waitcnt lgkmcnt(4)\n"
"ds_read_b64_tr_b16 a[144:145], v240 offset:16392\n"
"v_mfma_f32_16x16x32_bf16 v[228:231], a[132:135], a[36:39], v[228:231]\n"
"ds_read_b64_tr_b16 a[146:147], v240 offset:20488\n"
"buffer_load_dwordx4 a[72:75], v251, s[40:43], s81 offen sc1\n"
"v_mfma_f32_16x16x32_bf16 v[236:239], a[128:131], a[40:43], v[236:239]\n"
"ds_read_b64_tr_b16 a[148:149], v240 offset:24584\n"
"v_mfma_f32_16x16x32_bf16 v[236:239], a[132:135], a[44:47], v[236:239]\n"
"s_waitcnt lgkmcnt(5)\n"
"ds_read_b64_tr_b16 a[150:151], v240 offset:28680\n"
"v_mfma_f32_16x16x32_bf16 v[0:3], a[136:139], a[32:35], v[0:3]\n"
"s_waitcnt lgkmcnt(4)\n"
"ds_read_b64_tr_b16 a[128:129], v240 offset:16896\n"
"v_mfma_f32_16x16x32_bf16 v[0:3], a[140:143], a[36:39], v[0:3]\n"
"buffer_load_dwordx4 a[76:79], v251, s[40:43], s81 offen offset:1024 sc1\n"
"v_mfma_f32_16x16x32_bf16 v[8:11], a[136:139], a[40:43], v[8:11]\n"
"ds_read_b64_tr_b16 a[130:131], v240 offset:20992\n"
"v_mfma_f32_16x16x32_bf16 v[8:11], a[140:143], a[44:47], v[8:11]\n"
"v_mfma_f32_16x16x32_bf16 v[16:19], a[136:139], a[48:51], v[16:19]\n"
"ds_read_b64_tr_b16 a[132:133], v240 offset:25088\n"
"v_mfma_f32_16x16x32_bf16 v[16:19], a[140:143], a[52:55], v[16:19]\n"
"buffer_load_dwordx4 a[80:83], v251, s[40:43], s82 offen sc1\n"
"v_mfma_f32_16x16x32_bf16 v[24:27], a[136:139], a[56:59], v[24:27]\n"
"ds_read_b64_tr_b16 a[134:135], v240 offset:29184\n"
"v_mfma_f32_16x16x32_bf16 v[24:27], a[140:143], a[60:63], v[24:27]\n"
"s_waitcnt lgkmcnt(6)\n"
"v_mfma_f32_16x16x32_bf16 v[4:7], a[144:147], a[32:35], v[4:7]\n"
"s_waitcnt lgkmcnt(4)\n"
"ds_read_b64_tr_b16 a[136:137], v240 offset:16904\n"
"v_mfma_f32_16x16x32_bf16 v[4:7], a[148:151], a[36:39], v[4:7]\n"
"buffer_load_dwordx4 a[84:87], v251, s[40:43], s82 offen offset:1024 sc1\n"
"v_mfma_f32_16x16x32_bf16 v[12:15], a[144:147], a[40:43], v[12:15]\n"
"ds_read_b64_tr_b16 a[138:139], v240 offset:21000\n"
"v_mfma_f32_16x16x32_bf16 v[12:15], a[148:151], a[44:47], v[12:15]\n"
"v_mfma_f32_16x16x32_bf16 v[20:23], a[144:147], a[48:51], v[20:23]\n"
"ds_read_b64_tr_b16 a[140:141], v240 offset:25096\n"
"v_mfma_f32_16x16x32_bf16 v[20:23], a[148:151], a[52:55], v[20:23]\n"
"buffer_load_dwordx4 a[88:91], v251, s[40:43], s83 offen sc1\n"
"v_mfma_f32_16x16x32_bf16 v[28:31], a[144:147], a[56:59], v[28:31]\n"
"ds_read_b64_tr_b16 a[142:143], v240 offset:29192\n"
"v_mfma_f32_16x16x32_bf16 v[28:31], a[148:151], a[60:63], v[28:31]\n"
"s_waitcnt lgkmcnt(6)\n"
"v_mfma_f32_16x16x32_bf16 v[32:35], a[128:131], a[32:35], v[32:35]\n"
"s_waitcnt lgkmcnt(4)\n"
"ds_read_b64_tr_b16 a[144:145], v240 offset:17408\n"
"v_mfma_f32_16x16x32_bf16 v[32:35], a[132:135], a[36:39], v[32:35]\n"
"buffer_load_dwordx4 a[92:95], v251, s[40:43], s83 offen offset:1024 sc1\n"
"v_mfma_f32_16x16x32_bf16 v[40:43], a[128:131], a[40:43], v[40:43]\n"
"ds_read_b64_tr_b16 a[146:147], v240 offset:21504\n"
"v_mfma_f32_16x16x32_bf16 v[40:43], a[132:135], a[44:47], v[40:43]\n"
"v_mfma_f32_16x16x32_bf16 v[48:51], a[128:131], a[48:51], v[48:51]\n"
"ds_read_b64_tr_b16 a[148:149], v240 offset:25600\n"
"v_mfma_f32_16x16x32_bf16 v[48:51], a[132:135], a[52:55], v[48:51]\n"
"v_mfma_f32_16x16x32_bf16 v[56:59], a[128:131], a[56:59], v[56:59]\n"
"ds_read_b64_tr_b16 a[150:151], v240 offset:29696\n"
"v_mfma_f32_16x16x32_bf16 v[56:59], a[132:135], a[60:63], v[56:59]\n"
"s_waitcnt lgkmcnt(6)\n"
"v_mfma_f32_16x16x32_bf16 v[36:39], a[136:139], a[32:35], v[36:39]\n"
"s_waitcnt lgkmcnt(4)\n"
"ds_read_b64_tr_b16 a[128:129], v240 offset:17416\n"
"v_mfma_f32_16x16x32_bf16 v[36:39], a[140:143], a[36:39], v[36:39]\n"
"v_mfma_f32_16x16x32_bf16 v[44:47], a[136:139], a[40:43], v[44:47]\n"
"ds_read_b64_tr_b16 a[130:131], v240 offset:21512\n"
"v_mfma_f32_16x16x32_bf16 v[44:47], a[140:143], a[44:47], v[44:47]\n"
"v_mfma_f32_16x16x32_bf16 v[52:55], a[136:139], a[48:51], v[52:55]\n"
"ds_read_b64_tr_b16 a[132:133], v240 offset:25608\n"
"v_mfma_f32_16x16x32_bf16 v[52:55], a[140:143], a[52:55], v[52:55]\n"
"v_mfma_f32_16x16x32_bf16 v[60:63], a[136:139], a[56:59], v[60:63]\n"
"ds_read_b64_tr_b16 a[134:135], v240 offset:29704\n"
"v_mfma_f32_16x16x32_bf16 v[60:63], a[140:143], a[60:63], v[60:63]\n"
"s_waitcnt lgkmcnt(6)\n"
"v_mfma_f32_16x16x32_bf16 v[64:67], a[144:147], a[32:35], v[64:67]\n"
"s_waitcnt lgkmcnt(4)\n"
"ds_read_b64_tr_b16 a[136:137], v240 offset:17920\n"
"v_mfma_f32_16x16x32_bf16 v[64:67], a[148:151], a[36:39], v[64:67]\n"
"v_mfma_f32_16x16x32_bf16 v[72:75], a[144:147], a[40:43], v[72:75]\n"
"ds_read_b64_tr_b16 a[138:139], v240 offset:22016\n"
"v_mfma_f32_16x16x32_bf16 v[72:75], a[148:151], a[44:47], v[72:75]\n"
"v_mfma_f32_16x16x32_bf16 v[80:83], a[144:147], a[48:51], v[80:83]\n"
"ds_read_b64_tr_b16 a[140:141], v240 offset:26112\n"
"v_mfma_f32_16x16x32_bf16 v[80:83], a[148:151], a[52:55], v[80:83]\n"
"v_mfma_f32_16x16x32_bf16 v[88:91], a[144:147], a[56:59], v[88:91]\n"
"ds_read_b64_tr_b16 a[142:143], v240 offset:30208\n"
"v_mfma_f32_16x16x32_bf16 v[88:91], a[148:151], a[60:63], v[88:91]\n"
"s_waitcnt lgkmcnt(6)\n"
"v_mfma_f32_16x16x32_bf16 v[68:71], a[128:131], a[32:35], v[68:71]\n"
"s_waitcnt lgkmcnt(4)\n"
"ds_read_b64_tr_b16 a[144:145], v240 offset:17928\n"
"v_mfma_f32_16x16x32_bf16 v[68:71], a[132:135], a[36:39], v[68:71]\n"
"v_mfma_f32_16x16x32_bf16 v[76:79], a[128:131], a[40:43], v[76:79]\n"
"ds_read_b64_tr_b16 a[146:147], v240 offset:22024\n"
"v_mfma_f32_16x16x32_bf16 v[76:79], a[132:135], a[44:47], v[76:79]\n"
"v_mfma_f32_16x16x32_bf16 v[84:87], a[128:131], a[48:51], v[84:87]\n"
"ds_read_b64_tr_b16 a[148:149], v240 offset:26120\n"
"v_mfma_f32_16x16x32_bf16 v[84:87], a[132:135], a[52:55], v[84:87]\n"
"v_mfma_f32_16x16x32_bf16 v[92:95], a[128:131], a[56:59], v[92:95]\n"
"ds_read_b64_tr_b16 a[150:151], v240 offset:30216\n"
"v_mfma_f32_16x16x32_bf16 v[92:95], a[132:135], a[60:63], v[92:95]\n"
"s_waitcnt lgkmcnt(6)\n"
"v_mfma_f32_16x16x32_bf16 v[96:99], a[136:139], a[32:35], v[96:99]\n"
"s_barrier\n"
"s_waitcnt lgkmcnt(4)\n"
"v_mfma_f32_16x16x32_bf16 v[96:99], a[140:143], a[36:39], v[96:99]\n"
"ds_read_b64_tr_b16 a[128:129], v241 offset:16384\n"
"v_mfma_f32_16x16x32_bf16 v[104:107], a[136:139], a[40:43], v[104:107]\n"
"v_mfma_f32_16x16x32_bf16 v[104:107], a[140:143], a[44:47], v[104:107]\n"
"s_add_u32 m0, s93, 0\n"
"ds_read_b64_tr_b16 a[130:131], v241 offset:18432\n"
"v_mfma_f32_16x16x32_bf16 v[112:115], a[136:139], a[48:51], v[112:115]\n"
"buffer_load_dwordx4 v249, s[52:55], s88 offen lds\n"
"v_mfma_f32_16x16x32_bf16 v[112:115], a[140:143], a[52:55], v[112:115]\n"
"ds_read_b64_tr_b16 a[132:133], v241 offset:20480\n"
"v_mfma_f32_16x16x32_bf16 v[120:123], a[136:139], a[56:59], v[120:123]\n"
"s_add_u32 m0, s93, 1920\n"
"v_mfma_f32_16x16x32_bf16 v[120:123], a[140:143], a[60:63], v[120:123]\n"
"s_waitcnt lgkmcnt(5)\n"
"ds_read_b64_tr_b16 a[134:135], v241 offset:22528\n"
"buffer_load_dwordx4 v249, s[52:55], s88 offen offset:128 lds\n"
"v_mfma_f32_16x16x32_bf16 v[100:103], a[144:147], a[32:35], v[100:103]\n"
"s_waitcnt lgkmcnt(4)\n"
"s_add_u32 s88, s88, 16384\n"
"ds_read_b64_tr_b16 a[136:137], v242 offset:16384\n"
"v_mfma_f32_16x16x32_bf16 v[100:103], a[148:151], a[36:39], v[100:103]\n"
"s_add_u32 m0, s93, 4096\n"
"v_mfma_f32_16x16x32_bf16 v[108:111], a[144:147], a[40:43], v[108:111]\n"
"ds_read_b64_tr_b16 a[138:139], v242 offset:18432\n"
"buffer_load_dwordx4 v249, s[52:55], s89 offen lds\n"
"v_mfma_f32_16x16x32_bf16 v[108:111], a[148:151], a[44:47], v[108:111]\n"
"v_mfma_f32_16x16x32_bf16 v[116:119], a[144:147], a[48:51], v[116:119]\n"
"s_add_u32 m0, s93, 6016\n"
"ds_read_b64_tr_b16 a[140:141], v242 offset:20480\n"
"v_mfma_f32_16x16x32_bf16 v[116:119], a[148:151], a[52:55], v[116:119]\n"
"buffer_load_dwordx4 v249, s[52:55], s89 offen offset:128 lds\n"
"v_mfma_f32_16x16x32_bf16 v[124:127], a[144:147], a[56:59], v[124:127]\n"
"s_add_u32 s89, s89, 16384\n"
"ds_read_b64_tr_b16 a[142:143], v242 offset:22528\n"
"v_mfma_f32_16x16x32_bf16 v[124:127], a[148:151], a[60:63], v[124:127]\n"
"s_add_u32 m0, s94, 0\n"
"s_waitcnt vmcnt(11) lgkmcnt(6)\n"
"v_mfma_f32_16x16x32_bf16 v[192:195], a[128:131], a[64:67], v[192:195]\n"
"s_waitcnt vmcnt(10) lgkmcnt(4)\n"
"buffer_load_dwordx4 v250, s[56:59], s90 offen lds\n"
"ds_read_b64_tr_b16 a[144:145], v241 offset:16896\n"
"v_mfma_f32_16x16x32_bf16 v[192:195], a[132:135], a[68:71], v[192:195]\n"
"s_waitcnt vmcnt(10)\n"
"v_mfma_f32_16x16x32_bf16 v[200:203], a[128:131], a[72:75], v[200:203]\n"
"s_waitcnt vmcnt(9)\n"
"s_add_u32 m0, s94, 896\n"
"ds_read_b64_tr_b16 a[146:147], v241 offset:18944\n"
"v_mfma_f32_16x16x32_bf16 v[200:203], a[132:135], a[76:79], v[200:203]\n"
"s_waitcnt vmcnt(8)\n"
"buffer_load_dwordx4 v250, s[56:59], s90 offen offset:128 lds\n"
"v_mfma_f32_16x16x32_bf16 v[208:211], a[128:131], a[80:83], v[208:211]\n"
"s_waitcnt vmcnt(8)\n"
"ds_read_b64_tr_b16 a[148:149], v241 offset:20992\n"
"v_mfma_f32_16x16x32_bf16 v[208:211], a[132:135], a[84:87], v[208:211]\n"
"s_waitcnt vmcnt(7)\n"
"s_add_u32 m0, s94, 0\n"
"v_mfma_f32_16x16x32_bf16 v[216:219], a[128:131], a[88:91], v[216:219]\n"
"s_waitcnt vmcnt(6)\n"
"buffer_load_dwordx4 v250, s[56:59], s90 offen offset:2048 lds\n"
"ds_read_b64_tr_b16 a[150:151], v241 offset:23040\n"
"v_mfma_f32_16x16x32_bf16 v[216:219], a[132:135], a[92:95], v[216:219]\n"
"s_waitcnt lgkmcnt(6)\n"
"v_mfma_f32_16x16x32_bf16 v[196:199], a[136:139], a[64:67], v[196:199]\n"
"s_waitcnt lgkmcnt(4)\n"
"s_add_u32 m0, s94, 896\n"
"ds_read_b64_tr_b16 a[128:129], v242 offset:16896\n"
"v_mfma_f32_16x16x32_bf16 v[196:199], a[140:143], a[68:71], v[196:199]\n"
"buffer_load_dwordx4 v250, s[56:59], s90 offen offset:2176 lds\n"
"v_mfma_f32_16x16x32_bf16 v[204:207], a[136:139], a[72:75], v[204:207]\n"
"s_add_u32 s90, s90, 16384\n"
"ds_read_b64_tr_b16 a[130:131], v242 offset:18944\n"
"v_mfma_f32_16x16x32_bf16 v[204:207], a[140:143], a[76:79], v[204:207]\n"
"v_mfma_f32_16x16x32_bf16 v[212:215], a[136:139], a[80:83], v[212:215]\n"
"ds_read_b64_tr_b16 a[132:133], v242 offset:20992\n"
"v_mfma_f32_16x16x32_bf16 v[212:215], a[140:143], a[84:87], v[212:215]\n"
"buffer_load_dwordx4 a[96:99], v251, s[40:43], s80 offen offset:2048 sc1\n"
"v_mfma_f32_16x16x32_bf16 v[220:223], a[136:139], a[88:91], v[220:223]\n"
"ds_read_b64_tr_b16 a[134:135], v242 offset:23040\n"
"v_mfma_f32_16x16x32_bf16 v[220:223], a[140:143], a[92:95], v[220:223]\n"
"s_waitcnt lgkmcnt(6)\n"
"v_mfma_f32_16x16x32_bf16 v[224:227], a[144:147], a[64:67], v[224:227]\n"
"s_waitcnt lgkmcnt(4)\n"
"ds_read_b64_tr_b16 a[136:137], v240 offset:32768\n"
"v_mfma_f32_16x16x32_bf16 v[224:227], a[148:151], a[68:71], v[224:227]\n"
"ds_read_b64_tr_b16 a[138:139], v240 offset:36864\n"
"buffer_load_dwordx4 a[100:103], v251, s[40:43], s80 offen offset:3072 sc1\n"
"v_mfma_f32_16x16x32_bf16 v[232:235], a[144:147], a[72:75], v[232:235]\n"
"ds_read_b64_tr_b16 a[140:141], v240 offset:40960\n"
"v_mfma_f32_16x16x32_bf16 v[232:235], a[148:151], a[76:79], v[232:235]\n"
"s_waitcnt lgkmcnt(5)\n"
"ds_read_b64_tr_b16 a[142:143], v240 offset:45056\n"
"v_mfma_f32_16x16x32_bf16 v[228:231], a[128:131], a[64:67], v[228:231]\n"
"s_waitcnt lgkmcnt(4)\n"
"s_add_u32 s80, s80, 4096\n"
"ds_read_b64_tr_b16 a[144:145], v240 offset:32776\n"
"v_mfma_f32_16x16x32_bf16 v[228:231], a[132:135], a[68:71], v[228:231]\n"
"ds_read_b64_tr_b16 a[146:147], v240 offset:36872\n"
"buffer_load_dwordx4 a[104:107], v251, s[40:43], s81 offen offset:2048 sc1\n"
"v_mfma_f32_16x16x32_bf16 v[236:239], a[128:131], a[72:75], v[236:239]\n"
"ds_read_b64_tr_b16 a[148:149], v240 offset:40968\n"
"v_mfma_f32_16x16x32_bf16 v[236:239], a[132:135], a[76:79], v[236:239]\n"
"s_waitcnt lgkmcnt(5)\n"
"ds_read_b64_tr_b16 a[150:151], v240 offset:45064\n"
"v_mfma_f32_16x16x32_bf16 v[0:3], a[136:139], a[64:67], v[0:3]\n"
"s_waitcnt lgkmcnt(4)\n"
"ds_read_b64_tr_b16 a[128:129], v240 offset:33280\n"
"v_mfma_f32_16x16x32_bf16 v[0:3], a[140:143], a[68:71], v[0:3]\n"
"buffer_load_dwordx4 a[108:111], v251, s[40:43], s81 offen offset:3072 sc1\n"
"v_mfma_f32_16x16x32_bf16 v[8:11], a[136:139], a[72:75], v[8:11]\n"
"ds_read_b64_tr_b16 a[130:131], v240 offset:37376\n"
"v_mfma_f32_16x16x32_bf16 v[8:11], a[140:143], a[76:79], v[8:11]\n"
"v_mfma_f32_16x16x32_bf16 v[16:19], a[136:139], a[80:83], v[16:19]\n"
"s_add_u32 s81, s81, 4096\n"
"ds_read_b64_tr_b16 a[132:133], v240 offset:41472\n"
"v_mfma_f32_16x16x32_bf16 v[16:19], a[140:143], a[84:87], v[16:19]\n"
"buffer_load_dwordx4 a[112:115], v251, s[40:43], s82 offen offset:2048 sc1\n"
"v_mfma_f32_16x16x32_bf16 v[24:27], a[136:139], a[88:91], v[24:27]\n"
"ds_read_b64_tr_b16 a[134:135], v240 offset:45568\n"
"v_mfma_f32_16x16x32_bf16 v[24:27], a[140:143], a[92:95], v[24:27]\n"
"s_waitcnt lgkmcnt(6)\n"
"v_mfma_f32_16x16x32_bf16 v[4:7], a[144:147], a[64:67], v[4:7]\n"
"s_waitcnt lgkmcnt(4)\n"
"ds_read_b64_tr_b16 a[136:137], v240 offset:33288\n"
"v_mfma_f32_16x16x32_bf16 v[4:7], a[148:151], a[68:71], v[4:7]\n"
"buffer_load_dwordx4 a[116:119], v251, s[40:43], s82 offen offset:3072 sc1\n"
"v_mfma_f32_16x16x32_bf16 v[12:15], a[144:147], a[72:75], v[12:15]\n"
"ds_read_b64_tr_b16 a[138:139], v240 offset:37384\n"
"v_mfma_f32_16x16x32_bf16 v[12:15], a[148:151], a[76:79], v[12:15]\n"
"v_mfma_f32_16x16x32_bf16 v[20:23], a[144:147], a[80:83], v[20:23]\n"
"s_add_u32 s82, s82, 4096\n"
"ds_read_b64_tr_b16 a[140:141], v240 offset:41480\n"
"v_mfma_f32_16x16x32_bf16 v[20:23], a[148:151], a[84:87], v[20:23]\n"
"buffer_load_dwordx4 a[120:123], v251, s[40:43], s83 offen offset:2048 sc1\n"
"v_mfma_f32_16x16x32_bf16 v[28:31], a[144:147], a[88:91], v[28:31]\n"
"ds_read_b64_tr_b16 a[142:143], v240 offset:45576\n"
"v_mfma_f32_16x16x32_bf16 v[28:31], a[148:151], a[92:95], v[28:31]\n"
"s_waitcnt lgkmcnt(6)\n"
"v_mfma_f32_16x16x32_bf16 v[32:35], a[128:131], a[64:67], v[32:35]\n"
"s_waitcnt lgkmcnt(4)\n"
"ds_read_b64_tr_b16 a[144:145], v240 offset:33792\n"
"v_mfma_f32_16x16x32_bf16 v[32:35], a[132:135], a[68:71], v[32:35]\n"
"buffer_load_dwordx4 a[124:127], v251, s[40:43], s83 offen offset:3072 sc1\n"
"v_mfma_f32_16x16x32_bf16 v[40:43], a[128:131], a[72:75], v[40:43]\n"
"s_add_u32 s83, s83, 4096\n"
"ds_read_b64_tr_b16 a[146:147], v240 offset:37888\n"
"v_mfma_f32_16x16x32_bf16 v[40:43], a[132:135], a[76:79], v[40:43]\n"
"v_mfma_f32_16x16x32_bf16 v[48:51], a[128:131], a[80:83], v[48:51]\n"
"ds_read_b64_tr_b16 a[148:149], v240 offset:41984\n"
"v_mfma_f32_16x16x32_bf16 v[48:51], a[132:135], a[84:87], v[48:51]\n"
"v_mfma_f32_16x16x32_bf16 v[56:59], a[128:131], a[88:91], v[56:59]\n"
"ds_read_b64_tr_b16 a[150:151], v240 offset:46080\n"
"v_mfma_f32_16x16x32_bf16 v[56:59], a[132:135], a[92:95], v[56:59]\n"
"s_waitcnt lgkmcnt(6)\n"
"v_mfma_f32_16x16x32_bf16 v[36:39], a[136:139], a[64:67], v[36:39]\n"
"s_waitcnt lgkmcnt(4)\n"
"ds_read_b64_tr_b16 a[128:129], v240 offset:33800\n"
"v_mfma_f32_16x16x32_bf16 v[36:39], a[140:143], a[68:71], v[36:39]\n"
"v_mfma_f32_16x16x32_bf16 v[44:47], a[136:139], a[72:75], v[44:47]\n"
"ds_read_b64_tr_b16 a[130:131], v240 offset:37896\n"
"v_mfma_f32_16x16x32_bf16 v[44:47], a[140:143], a[76:79], v[44:47]\n"
"v_mfma_f32_16x16x32_bf16 v[52:55], a[136:139], a[80:83], v[52:55]\n"
"ds_read_b64_tr_b16 a[132:133], v240 offset:41992\n"
"v_mfma_f32_16x16x32_bf16 v[52:55], a[140:143], a[84:87], v[52:55]\n"
"v_mfma_f32_16x16x32_bf16 v[60:63], a[136:139], a[88:91], v[60:63]\n"
"ds_read_b64_tr_b16 a[134:135], v240 offset:46088\n"
"v_mfma_f32_16x16x32_bf16 v[60:63], a[140:143], a[92:95], v[60:63]\n"
"s_waitcnt lgkmcnt(6)\n"
"v_mfma_f32_16x16x32_bf16 v[64:67], a[144:147], a[64:67], v[64:67]\n"
"s_waitcnt lgkmcnt(4)\n"
"ds_read_b64_tr_b16 a[136:137], v240 offset:34304\n"
"v_mfma_f32_16x16x32_bf16 v[64:67], a[148:151], a[68:71], v[64:67]\n"
"v_mfma_f32_16x16x32_bf16 v[72:75], a[144:147], a[72:75], v[72:75]\n"
"ds_read_b64_tr_b16 a[138:139], v240 offset:38400\n"
"v_mfma_f32_16x16x32_bf16 v[72:75], a[148:151], a[76:79], v[72:75]\n"
"v_mfma_f32_16x16x32_bf16 v[80:83], a[144:147], a[80:83], v[80:83]\n"
"ds_read_b64_tr_b16 a[140:141], v240 offset:42496\n"
"v_mfma_f32_16x16x32_bf16 v[80:83], a[148:151], a[84:87], v[80:83]\n"
"v_mfma_f32_16x16x32_bf16 v[88:91], a[144:147], a[88:91], v[88:91]\n"
"ds_read_b64_tr_b16 a[142:143], v240 offset:46592\n"
"v_mfma_f32_16x16x32_bf16 v[88:91], a[148:151], a[92:95], v[88:91]\n"
"s_waitcnt lgkmcnt(6)\n"
"v_mfma_f32_16x16x32_bf16 v[68:71], a[128:131], a[64:67], v[68:71]\n"
"s_waitcnt lgkmcnt(4)\n"
"ds_read_b64_tr_b16 a[144:145], v240 offset:34312\n"
"v_mfma_f32_16x16x32_bf16 v[68:71], a[132:135], a[68:71], v[68:71]\n"
"v_mfma_f32_16x16x32_bf16 v[76:79], a[128:131], a[72:75], v[76:79]\n"
"ds_read_b64_tr_b16 a[146:147], v240 offset:38408\n"
"v_mfma_f32_16x16x32_bf16 v[76:79], a[132:135], a[76:79], v[76:79]\n"
"v_mfma_f32_16x16x32_bf16 v[84:87], a[128:131], a[80:83], v[84:87]\n"
"ds_read_b64_tr_b16 a[148:149], v240 offset:42504\n"
"v_mfma_f32_16x16x32_bf16 v[84:87], a[132:135], a[84:87], v[84:87]\n"
"v_mfma_f32_16x16x32_bf16 v[92:95], a[128:131], a[88:91], v[92:95]\n"
"ds_read_b64_tr_b16 a[150:151], v240 offset:46600\n"
"v_mfma_f32_16x16x32_bf16 v[92:95], a[132:135], a[92:95], v[92:95]\n"
"s_waitcnt lgkmcnt(6)\n"
"v_mfma_f32_16x16x32_bf16 v[96:99], a[136:139], a[64:67], v[96:99]\n"
"s_barrier\n"
"s_waitcnt lgkmcnt(4)\n"
"v_mfma_f32_16x16x32_bf16 v[96:99], a[140:143], a[68:71], v[96:99]\n"
"ds_read_b64_tr_b16 a[128:129], v241 offset:24576\n"
"v_mfma_f32_16x16x32_bf16 v[104:107], a[136:139], a[72:75], v[104:107]\n"
"v_mfma_f32_16x16x32_bf16 v[104:107], a[140:143], a[76:79], v[104:107]\n"
"s_add_u32 m0, s93, 16384\n"
"ds_read_b64_tr_b16 a[130:131], v241 offset:26624\n"
"v_mfma_f32_16x16x32_bf16 v[112:115], a[136:139], a[80:83], v[112:115]\n"
"buffer_load_dwordx4 v249, s[52:55], s88 offen lds\n"
"v_mfma_f32_16x16x32_bf16 v[112:115], a[140:143], a[84:87], v[112:115]\n"
"ds_read_b64_tr_b16 a[132:133], v241 offset:28672\n"
"v_mfma_f32_16x16x32_bf16 v[120:123], a[136:139], a[88:91], v[120:123]\n"
"v_mfma_f32_16x16x32_bf16 v[120:123], a[140:143], a[92:95], v[120:123]\n"
"s_waitcnt lgkmcnt(5)\n"
"s_add_u32 m0, s93, 18304\n"
"ds_read_b64_tr_b16 a[134:135], v241 offset:30720\n"
"v_mfma_f32_16x16x32_bf16 v[100:103], a[144:147], a[64:67], v[100:103]\n"
"s_waitcnt lgkmcnt(4)\n"
"ds_read_b64_tr_b16 a[136:137], v242 offset:24576\n"
"buffer_load_dwordx4 v249, s[52:55], s88 offen offset:128 lds\n"
"v_mfma_f32_16x16x32_bf16 v[100:103], a[148:151], a[68:71], v[100:103]\n"
"v_mfma_f32_16x16x32_bf16 v[108:111], a[144:147], a[72:75], v[108:111]\n"
"s_add_u32 s88, s88, 16384\n"
"ds_read_b64_tr_b16 a[138:139], v242 offset:26624\n"
"v_mfma_f32_16x16x32_bf16 v[108:111], a[148:151], a[76:79], v[108:111]\n"
"s_add_u32 m0, s93, 20480\n"
"v_mfma_f32_16x16x32_bf16 v[116:119], a[144:147], a[80:83], v[116:119]\n"
"ds_read_b64_tr_b16 a[140:141], v242 offset:28672\n"
"buffer_load_dwordx4 v249, s[52:55], s89 offen lds\n"
"v_mfma_f32_16x16x32_bf16 v[116:119], a[148:151], a[84:87], v[116:119]\n"
"v_mfma_f32_16x16x32_bf16 v[124:127], a[144:147], a[88:91], v[124:127]\n"
"ds_read_b64_tr_b16 a[142:143], v242 offset:30720\n"
"v_mfma_f32_16x16x32_bf16 v[124:127], a[148:151], a[92:95], v[124:127]\n"
"s_add_u32 m0, s93, 22400\n"
"s_waitcnt vmcnt(10) lgkmcnt(6)\n"
"v_mfma_f32_16x16x32_bf16 v[192:195], a[128:131], a[96:99], v[192:195]\n"
"s_waitcnt vmcnt(9) lgkmcnt(4)\n"
"buffer_load_dwordx4 v249, s[52:55], s89 offen offset:128 lds\n"
"ds_read_b64_tr_b16 a[144:145], v241 offset:25088\n"
"v_mfma_f32_16x16x32_bf16 v[192:195], a[132:135], a[100:103], v[192:195]\n"
"s_waitcnt vmcnt(9)\n"
"s_add_u32 s89, s89, 16384\n"
"v_mfma_f32_16x16x32_bf16 v[200:203], a[128:131], a[104:107], v[200:203]\n"
"s_waitcnt vmcnt(8)\n"
"ds_read_b64_tr_b16 a[146:147], v241 offset:27136\n"
"v_mfma_f32_16x16x32_bf16 v[200:203], a[132:135], a[108:111], v[200:203]\n"
"s_waitcnt vmcnt(7)\n"
"v_mfma_f32_16x16x32_bf16 v[208:211], a[128:131], a[112:115], v[208:211]\n"
"s_waitcnt vmcnt(6)\n"
"ds_read_b64_tr_b16 a[148:149], v241 offset:29184\n"
"v_mfma_f32_16x16x32_bf16 v[208:211], a[132:135], a[116:119], v[208:211]\n"
"s_waitcnt vmcnt(5)\n"
"v_mfma_f32_16x16x32_bf16 v[216:219], a[128:131], a[120:123], v[216:219]\n"
"s_waitcnt vmcnt(4)\n"
"ds_read_b64_tr_b16 a[150:151], v241 offset:31232\n"
"v_mfma_f32_16x16x32_bf16 v[216:219], a[132:135], a[124:127], v[216:219]\n"
"s_waitcnt lgkmcnt(6)\n"
"v_mfma_f32_16x16x32_bf16 v[196:199], a[136:139], a[96:99], v[196:199]\n"
"s_waitcnt lgkmcnt(4)\n"
"ds_read_b64_tr_b16 a[128:129], v242 offset:25088\n"
"v_mfma_f32_16x16x32_bf16 v[196:199], a[140:143], a[100:103], v[196:199]\n"
"v_mfma_f32_16x16x32_bf16 v[204:207], a[136:139], a[104:107], v[204:207]\n"
"ds_read_b64_tr_b16 a[130:131], v242 offset:27136\n"
"v_mfma_f32_16x16x32_bf16 v[204:207], a[140:143], a[108:111], v[204:207]\n"
"v_mfma_f32_16x16x32_bf16 v[212:215], a[136:139], a[112:115], v[212:215]\n"
"ds_read_b64_tr_b16 a[132:133], v242 offset:29184\n"
"v_mfma_f32_16x16x32_bf16 v[212:215], a[140:143], a[116:119], v[212:215]\n"
"v_max_f32 v192, 0, v192\n"
"v_max_f32 v193, 0, v193\n"
"v_mfma_f32_16x16x32_bf16 v[220:223], a[136:139], a[120:123], v[220:223]\n"
"ds_read_b64_tr_b16 a[134:135], v242 offset:31232\n"
"v_mul_f32 v192, v192, v192\n"
"v_mfma_f32_16x16x32_bf16 v[220:223], a[140:143], a[124:127], v[220:223]\n"
"s_waitcnt lgkmcnt(6)\n"
"v_mul_f32 v193, v193, v193\n"
"v_cvt_pk_bf16_f32 v192, v192, v193\n"
"v_mfma_f32_16x16x32_bf16 v[224:227], a[144:147], a[96:99], v[224:227]\n"
"s_waitcnt lgkmcnt(4)\n"
"ds_read_b64_tr_b16 a[136:137], v240 offset:49152\n"
"v_max_f32 v194, 0, v194\n"
"v_mfma_f32_16x16x32_bf16 v[224:227], a[148:151], a[100:103], v[224:227]\n"
"ds_read_b64_tr_b16 a[138:139], v240 offset:53248\n"
"v_max_f32 v195, 0, v195\n"
"v_mfma_f32_16x16x32_bf16 v[232:235], a[144:147], a[104:107], v[232:235]\n"
"ds_read_b64_tr_b16 a[140:141], v240 offset:57344\n"
"buffer_load_dword v254, v255, s[16:19], s14 offen\n"
"v_mul_f32 v194, v194, v194\n"
"v_mfma_f32_16x16x32_bf16 v[232:235], a[148:151], a[108:111], v[232:235]\n"
"s_waitcnt lgkmcnt(5)\n"
"s_add_u32 s14, s14, 16384\n"
"ds_read_b64_tr_b16 a[142:143], v240 offset:61440\n"
"v_mul_f32 v195, v195, v195\n"
"v_mfma_f32_16x16x32_bf16 v[228:231], a[128:131], a[96:99], v[228:231]\n"
"s_waitcnt lgkmcnt(4)\n"
"ds_read_b64_tr_b16 a[144:145], v240 offset:49160\n"
"v_cvt_pk_bf16_f32 v193, v194, v195\n"
"v_mfma_f32_16x16x32_bf16 v[228:231], a[132:135], a[100:103], v[228:231]\n"
"ds_read_b64_tr_b16 a[146:147], v240 offset:53256\n"
"v_max_f32 v196, 0, v196\n"
"v_mfma_f32_16x16x32_bf16 v[236:239], a[128:131], a[104:107], v[236:239]\n"
"ds_read_b64_tr_b16 a[148:149], v240 offset:57352\n"
"v_max_f32 v197, 0, v197\n"
"v_mfma_f32_16x16x32_bf16 v[236:239], a[132:135], a[108:111], v[236:239]\n"
"s_waitcnt lgkmcnt(5)\n"
"ds_read_b64_tr_b16 a[150:151], v240 offset:61448\n"
"v_mul_f32 v196, v196, v196\n"
"v_mfma_f32_16x16x32_bf16 v[0:3], a[136:139], a[96:99], v[0:3]\n"
"s_waitcnt lgkmcnt(4)\n"
"ds_read_b64_tr_b16 a[128:129], v240 offset:49664\n"
"v_mul_f32 v197, v197, v197\n"
"v_mfma_f32_16x16x32_bf16 v[0:3], a[140:143], a[100:103], v[0:3]\n"
"v_max_f32 v224, 0, v224\n"
"v_max_f32 v225, 0, v225\n"
"v_mfma_f32_16x16x32_bf16 v[8:11], a[136:139], a[104:107], v[8:11]\n"
"ds_read_b64_tr_b16 a[130:131], v240 offset:53760\n"
"v_mul_f32 v224, v224, v224\n"
"v_mfma_f32_16x16x32_bf16 v[8:11], a[140:143], a[108:111], v[8:11]\n"
"v_mul_f32 v225, v225, v225\n"
"v_cvt_pk_bf16_f32 v224, v224, v225\n"
"v_mfma_f32_16x16x32_bf16 v[16:19], a[136:139], a[112:115], v[16:19]\n"
"ds_read_b64_tr_b16 a[132:133], v240 offset:57856\n"
"v_max_f32 v226, 0, v226\n"
"v_mfma_f32_16x16x32_bf16 v[16:19], a[140:143], a[116:119], v[16:19]\n"
"v_max_f32 v227, 0, v227\n"
"v_mul_f32 v226, v226, v226\n"
"v_mfma_f32_16x16x32_bf16 v[24:27], a[136:139], a[120:123], v[24:27]\n"
"ds_read_b64_tr_b16 a[134:135], v240 offset:61952\n"
"v_mul_f32 v227, v227, v227\n"
"v_mfma_f32_16x16x32_bf16 v[24:27], a[140:143], a[124:127], v[24:27]\n"
"s_waitcnt lgkmcnt(6)\n"
"v_cvt_pk_bf16_f32 v225, v226, v227\n"
"v_max_f32 v228, 0, v228\n"
"v_mfma_f32_16x16x32_bf16 v[4:7], a[144:147], a[96:99], v[4:7]\n"
"s_waitcnt lgkmcnt(4)\n"
"ds_read_b64_tr_b16 a[136:137], v240 offset:49672\n"
"v_max_f32 v229, 0, v229\n"
"v_mfma_f32_16x16x32_bf16 v[4:7], a[148:151], a[100:103], v[4:7]\n"
"v_mul_f32 v228, v228, v228\n"
"v_mul_f32 v229, v229, v229\n"
"v_mfma_f32_16x16x32_bf16 v[12:15], a[144:147], a[104:107], v[12:15]\n"
"ds_read_b64_tr_b16 a[138:139], v240 offset:53768\n"
"v_cvt_pk_bf16_f32 v226, v228, v229\n"
"v_mfma_f32_16x16x32_bf16 v[12:15], a[148:151], a[108:111], v[12:15]\n"
"v_max_f32 v230, 0, v230\n"
"v_max_f32 v231, 0, v231\n"
"v_mfma_f32_16x16x32_bf16 v[20:23], a[144:147], a[112:115], v[20:23]\n"
"ds_read_b64_tr_b16 a[140:141], v240 offset:57864\n"
"v_mul_f32 v230, v230, v230\n"
"v_mfma_f32_16x16x32_bf16 v[20:23], a[148:151], a[116:119], v[20:23]\n"
"v_cvt_pk_bf16_f32 v128, v0, v1\n"
"v_cvt_pk_bf16_f32 v129, v2, v3\n"
"v_mfma_f32_16x16x32_bf16 v[28:31], a[144:147], a[120:123], v[28:31]\n"
"ds_read_b64_tr_b16 a[142:143], v240 offset:61960\n"
"v_cvt_pk_bf16_f32 v130, v4, v5\n"
"v_mfma_f32_16x16x32_bf16 v[28:31], a[148:151], a[124:127], v[28:31]\n"
"s_waitcnt lgkmcnt(6)\n"
"v_cvt_pk_bf16_f32 v131, v6, v7\n"
"v_cvt_pk_bf16_f32 v144, v8, v9\n"
"v_mfma_f32_16x16x32_bf16 v[32:35], a[128:131], a[96:99], v[32:35]\n"
"s_waitcnt lgkmcnt(4)\n"
"ds_read_b64_tr_b16 a[144:145], v240 offset:50176\n"
"v_cvt_pk_bf16_f32 v145, v10, v11\n"
"v_mfma_f32_16x16x32_bf16 v[32:35], a[132:135], a[100:103], v[32:35]\n"
"v_cvt_pk_bf16_f32 v146, v12, v13\n"
"v_cvt_pk_bf16_f32 v147, v14, v15\n"
"v_mfma_f32_16x16x32_bf16 v[40:43], a[128:131], a[104:107], v[40:43]\n"
"ds_read_b64_tr_b16 a[146:147], v240 offset:54272\n"
"v_cvt_pk_bf16_f32 v160, v16, v17\n"
"v_mfma_f32_16x16x32_bf16 v[40:43], a[132:135], a[108:111], v[40:43]\n"
"v_cvt_pk_bf16_f32 v161, v18, v19\n"
"v_cvt_pk_bf16_f32 v162, v20, v21\n"
"v_mfma_f32_16x16x32_bf16 v[48:51], a[128:131], a[112:115], v[48:51]\n"
"ds_read_b64_tr_b16 a[148:149], v240 offset:58368\n"
"v_cvt_pk_bf16_f32 v163, v22, v23\n"
"v_mfma_f32_16x16x32_bf16 v[48:51], a[132:135], a[116:119], v[48:51]\n"
"v_cvt_pk_bf16_f32 v176, v24, v25\n"
"v_cvt_pk_bf16_f32 v177, v26, v27\n"
"v_mfma_f32_16x16x32_bf16 v[56:59], a[128:131], a[120:123], v[56:59]\n"
"ds_read_b64_tr_b16 a[150:151], v240 offset:62464\n"
"v_cvt_pk_bf16_f32 v178, v28, v29\n"
"v_mfma_f32_16x16x32_bf16 v[56:59], a[132:135], a[124:127], v[56:59]\n"
"s_waitcnt lgkmcnt(6)\n"
"v_cvt_pk_bf16_f32 v179, v30, v31\n"
"v_mul_f32 v231, v231, v231\n"
"v_mfma_f32_16x16x32_bf16 v[36:39], a[136:139], a[96:99], v[36:39]\n"
"s_waitcnt lgkmcnt(4)\n"
"ds_read_b64_tr_b16 a[128:129], v240 offset:50184\n"
"v_cvt_pk_bf16_f32 v227, v230, v231\n"
"v_mfma_f32_16x16x32_bf16 v[36:39], a[140:143], a[100:103], v[36:39]\n"
"buffer_store_dwordx4 v[224:227], v251, s[40:43], s28 offen sc1\n"
"v_max_f32 v232, 0, v232\n"
"v_max_f32 v233, 0, v233\n"
"v_mfma_f32_16x16x32_bf16 v[44:47], a[136:139], a[104:107], v[44:47]\n"
"ds_read_b64_tr_b16 a[130:131], v240 offset:54280\n"
"v_mul_f32 v232, v232, v232\n"
"v_mfma_f32_16x16x32_bf16 v[44:47], a[140:143], a[108:111], v[44:47]\n"
"v_mul_f32 v233, v233, v233\n"
"v_cvt_pk_bf16_f32 v232, v232, v233\n"
"v_mfma_f32_16x16x32_bf16 v[52:55], a[136:139], a[112:115], v[52:55]\n"
"ds_read_b64_tr_b16 a[132:133], v240 offset:58376\n"
"v_max_f32 v234, 0, v234\n"
"v_mfma_f32_16x16x32_bf16 v[52:55], a[140:143], a[116:119], v[52:55]\n"
"v_cvt_pk_bf16_f32 v132, v32, v33\n"
"v_cvt_pk_bf16_f32 v133, v34, v35\n"
"v_mfma_f32_16x16x32_bf16 v[60:63], a[136:139], a[120:123], v[60:63]\n"
"ds_read_b64_tr_b16 a[134:135], v240 offset:62472\n"
"v_cvt_pk_bf16_f32 v134, v36, v37\n"
"v_mfma_f32_16x16x32_bf16 v[60:63], a[140:143], a[124:127], v[60:63]\n"
"s_waitcnt lgkmcnt(6)\n"
"v_cvt_pk_bf16_f32 v135, v38, v39\n"
"v_cvt_pk_bf16_f32 v148, v40, v41\n"
"v_mfma_f32_16x16x32_bf16 v[64:67], a[144:147], a[96:99], v[64:67]\n"
"s_waitcnt lgkmcnt(4)\n"
"ds_read_b64_tr_b16 a[136:137], v240 offset:50688\n"
"v_cvt_pk_bf16_f32 v149, v42, v43\n"
"v_mfma_f32_16x16x32_bf16 v[64:67], a[148:151], a[100:103], v[64:67]\n"
"v_cvt_pk_bf16_f32 v150, v44, v45\n"
"v_cvt_pk_bf16_f32 v151, v46, v47\n"
"v_mfma_f32_16x16x32_bf16 v[72:75], a[144:147], a[104:107], v[72:75]\n"
"ds_read_b64_tr_b16 a[138:139], v240 offset:54784\n"
"v_cvt_pk_bf16_f32 v164, v48, v49\n"
"v_mfma_f32_16x16x32_bf16 v[72:75], a[148:151], a[108:111], v[72:75]\n"
"v_cvt_pk_bf16_f32 v165, v50, v51\n"
"v_cvt_pk_bf16_f32 v166, v52, v53\n"
"v_mfma_f32_16x16x32_bf16 v[80:83], a[144:147], a[112:115], v[80:83]\n"
"ds_read_b64_tr_b16 a[140:141], v240 offset:58880\n"
"v_cvt_pk_bf16_f32 v167, v54, v55\n"
"v_mfma_f32_16x16x32_bf16 v[80:83], a[148:151], a[116:119], v[80:83]\n"
"v_cvt_pk_bf16_f32 v180, v56, v57\n"
"v_cvt_pk_bf16_f32 v181, v58, v59\n"
"v_mfma_f32_16x16x32_bf16 v[88:91], a[144:147], a[120:123], v[88:91]\n"
"ds_read_b64_tr_b16 a[142:143], v240 offset:62976\n"
"v_cvt_pk_bf16_f32 v182, v60, v61\n"
"v_mfma_f32_16x16x32_bf16 v[88:91], a[148:151], a[124:127], v[88:91]\n"
"s_waitcnt lgkmcnt(6)\n"
"v_cvt_pk_bf16_f32 v183, v62, v63\n"
"v_max_f32 v235, 0, v235\n"
"v_mfma_f32_16x16x32_bf16 v[68:71], a[128:131], a[96:99], v[68:71]\n"
"s_waitcnt lgkmcnt(4)\n"
"ds_read_b64_tr_b16 a[144:145], v240 offset:50696\n"
"v_mul_f32 v234, v234, v234\n"
"v_mfma_f32_16x16x32_bf16 v[68:71], a[132:135], a[100:103], v[68:71]\n"
"v_mul_f32 v235, v235, v235\n"
"v_cvt_pk_bf16_f32 v233, v234, v235\n"
"v_mfma_f32_16x16x32_bf16 v[76:79], a[128:131], a[104:107], v[76:79]\n"
"ds_read_b64_tr_b16 a[146:147], v240 offset:54792\n"
"v_max_f32 v236, 0, v236\n"
"v_mfma_f32_16x16x32_bf16 v[76:79], a[132:135], a[108:111], v[76:79]\n"
"v_max_f32 v237, 0, v237\n"
"v_mul_f32 v236, v236, v236\n"
"v_mfma_f32_16x16x32_bf16 v[84:87], a[128:131], a[112:115], v[84:87]\n"
"ds_read_b64_tr_b16 a[148:149], v240 offset:58888\n"
"v_mul_f32 v237, v237, v237\n"
"v_mfma_f32_16x16x32_bf16 v[84:87], a[132:135], a[116:119], v[84:87]\n"
"v_cvt_pk_bf16_f32 v136, v64, v65\n"
"v_cvt_pk_bf16_f32 v137, v66, v67\n"
"v_mfma_f32_16x16x32_bf16 v[92:95], a[128:131], a[120:123], v[92:95]\n"
"ds_read_b64_tr_b16 a[150:151], v240 offset:62984\n"
"v_cvt_pk_bf16_f32 v138, v68, v69\n"
"v_mfma_f32_16x16x32_bf16 v[92:95], a[132:135], a[124:127], v[92:95]\n"
"s_waitcnt lgkmcnt(6)\n"
"v_cvt_pk_bf16_f32 v139, v70, v71\n"
"v_cvt_pk_bf16_f32 v152, v72, v73\n"
"v_mfma_f32_16x16x32_bf16 v[96:99], a[136:139], a[96:99], v[96:99]\n"
"s_barrier\n"
"v_cvt_pk_bf16_f32 v153, v74, v75\n"
"s_waitcnt lgkmcnt(4)\n"
"v_mfma_f32_16x16x32_bf16 v[96:99], a[140:143], a[100:103], v[96:99]\n"
"v_cvt_pk_bf16_f32 v154, v76, v77\n"
"v_cvt_pk_bf16_f32 v155, v78, v79\n"
"v_mfma_f32_16x16x32_bf16 v[104:107], a[136:139], a[104:107], v[104:107]\n"
"ds_read_b128 a[160:163], v243 offset:0\n"
"v_cvt_pk_bf16_f32 v168, v80, v81\n"
"v_mfma_f32_16x16x32_bf16 v[104:107], a[140:143], a[108:111], v[104:107]\n"
"v_cvt_pk_bf16_f32 v169, v82, v83\n"
"v_cvt_pk_bf16_f32 v170, v84, v85\n"
"v_mfma_f32_16x16x32_bf16 v[112:115], a[136:139], a[112:115], v[112:115]\n"
"ds_read_b128 a[164:167], v244 offset:0\n"
"v_cvt_pk_bf16_f32 v171, v86, v87\n"
"v_mfma_f32_16x16x32_bf16 v[112:115], a[140:143], a[116:119], v[112:115]\n"
"v_cvt_pk_bf16_f32 v184, v88, v89\n"
"v_cvt_pk_bf16_f32 v185, v90, v91\n"
"v_mfma_f32_16x16x32_bf16 v[120:123], a[136:139], a[120:123], v[120:123]\n"
"ds_read_b128 a[168:171], v243 offset:2048\n"
"v_cvt_pk_bf16_f32 v186, v92, v93\n"
"v_mfma_f32_16x16x32_bf16 v[120:123], a[140:143], a[124:127], v[120:123]\n"
"s_waitcnt lgkmcnt(5)\n"
"v_cvt_pk_bf16_f32 v187, v94, v95\n"
"v_cvt_pk_bf16_f32 v234, v236, v237\n"
"v_mfma_f32_16x16x32_bf16 v[100:103], a[144:147], a[96:99], v[100:103]\n"
"s_waitcnt lgkmcnt(3)\n"
"ds_read_b128 a[172:175], v244 offset:2048\n"
"v_max_f32 v238, 0, v238\n"
"v_mfma_f32_16x16x32_bf16 v[100:103], a[148:151], a[100:103], v[100:103]\n"
"v_max_f32 v239, 0, v239\n"
"v_mul_f32 v238, v238, v238\n"
"v_mfma_f32_16x16x32_bf16 v[108:111], a[144:147], a[104:107], v[108:111]\n"
"v_mul_f32 v239, v239, v239\n"
"v_cvt_pk_bf16_f32 v235, v238, v239\n"
"v_mfma_f32_16x16x32_bf16 v[108:111], a[148:151], a[108:111], v[108:111]\n"
"buffer_store_dwordx4 v[232:235], v251, s[40:43], s29 offen sc1\n"
"v_cvt_pk_bf16_f32 v194, v196, v197\n"
"v_max_f32 v198, 0, v198\n"
"v_mfma_f32_16x16x32_bf16 v[116:119], a[144:147], a[112:115], v[116:119]\n"
"v_max_f32 v199, 0, v199\n"
"v_mul_f32 v198, v198, v198\n"
"v_mfma_f32_16x16x32_bf16 v[116:119], a[148:151], a[116:119], v[116:119]\n"
"v_cvt_pk_bf16_f32 v140, v96, v97\n"
"v_cvt_pk_bf16_f32 v141, v98, v99\n"
"v_cvt_pk_bf16_f32 v142, v100, v101\n"
"v_cvt_pk_bf16_f32 v143, v102, v103\n"
"v_mfma_f32_16x16x32_bf16 v[124:127], a[144:147], a[120:123], v[124:127]\n"
"v_mul_f32 v199, v199, v199\n"
"v_cvt_pk_bf16_f32 v195, v198, v199\n"
"v_mfma_f32_16x16x32_bf16 v[124:127], a[148:151], a[124:127], v[124:127]\n"
"buffer_store_dwordx4 v[192:195], v251, s[40:43], s24 offen sc1\n"
"v_cvt_pk_bf16_f32 v156, v104, v105\n"
"v_cvt_pk_bf16_f32 v157, v106, v107\n"
"v_cvt_pk_bf16_f32 v158, v108, v109\n"
"v_cvt_pk_bf16_f32 v159, v110, v111\n"
"s_waitcnt lgkmcnt(3)\n"
"v_mfma_f32_16x16x32_bf16 v[0:3], a[160:163], v[128:131], 0\n"
"s_waitcnt lgkmcnt(2)\n"
"ds_read_b128 a[176:179], v243 offset:4096\n"
"v_max_f32 v200, 0, v200\n"
"v_mfma_f32_16x16x32_bf16 v[0:3], a[164:167], v[132:135], v[0:3]\n"
"s_waitcnt lgkmcnt(2)\n"
"v_cvt_pk_bf16_f32 v172, v112, v113\n"
"v_cvt_pk_bf16_f32 v173, v114, v115\n"
"v_cvt_pk_bf16_f32 v174, v116, v117\n"
"v_cvt_pk_bf16_f32 v175, v118, v119\n"
"v_mfma_f32_16x16x32_bf16 v[0:3], a[168:171], v[136:139], v[0:3]\n"
"s_waitcnt lgkmcnt(1)\n"
"ds_read_b128 a[180:183], v244 offset:4096\n"
"v_max_f32 v201, 0, v201\n"
"v_mov_b32 v248, v255\n"
"v_mfma_f32_16x16x32_bf16 v[0:3], a[172:175], v[140:143], v[0:3]\n"
"v_cvt_pk_bf16_f32 v188, v120, v121\n"
"v_cvt_pk_bf16_f32 v189, v122, v123\n"
"v_cvt_pk_bf16_f32 v190, v124, v125\n"
"v_cvt_pk_bf16_f32 v191, v126, v127\n"
"v_mfma_f32_16x16x32_bf16 v[8:11], a[160:163], v[144:147], 0\n"
"ds_read_b128 a[184:187], v243 offset:6144\n"
"v_mul_f32 v200, v200, v200\n"
"v_mfma_f32_16x16x32_bf16 v[8:11], a[164:167], v[148:151], v[8:11]\n"
"v_mul_f32 v201, v201, v201\n"
"v_cvt_pk_bf16_f32 v200, v200, v201\n"
"v_mfma_f32_16x16x32_bf16 v[8:11], a[168:171], v[152:155], v[8:11]\n"
"ds_read_b128 a[188:191], v244 offset:6144\n"
"v_max_f32 v202, 0, v202\n"
"v_mfma_f32_16x16x32_bf16 v[8:11], a[172:175], v[156:159], v[8:11]\n"
"v_max_f32 v203, 0, v203\n"
"v_mul_f32 v202, v202, v202\n"
"v_mfma_f32_16x16x32_bf16 v[16:19], a[160:163], v[160:163], 0\n"
"v_mul_f32 v203, v203, v203\n"
"v_cvt_pk_bf16_f32 v201, v202, v203\n"
"v_mfma_f32_16x16x32_bf16 v[16:19], a[164:167], v[164:167], v[16:19]\n"
"v_max_f32 v204, 0, v204\n"
"v_max_f32 v205, 0, v205\n"
"v_mfma_f32_16x16x32_bf16 v[16:19], a[168:171], v[168:171], v[16:19]\n"
"v_mul_f32 v204, v204, v204\n"
"v_mul_f32 v205, v205, v205\n"
"v_mfma_f32_16x16x32_bf16 v[16:19], a[172:175], v[172:175], v[16:19]\n"
"v_cvt_pk_bf16_f32 v202, v204, v205\n"
"v_max_f32 v206, 0, v206\n"
"v_mfma_f32_16x16x32_bf16 v[24:27], a[160:163], v[176:179], 0\n"
"v_max_f32 v207, 0, v207\n"
"v_mul_f32 v206, v206, v206\n"
"v_mfma_f32_16x16x32_bf16 v[24:27], a[164:167], v[180:183], v[24:27]\n"
"v_mul_f32 v207, v207, v207\n"
"v_cvt_pk_bf16_f32 v203, v206, v207\n"
"v_mfma_f32_16x16x32_bf16 v[24:27], a[168:171], v[184:187], v[24:27]\n"
"buffer_store_dwordx4 v[200:203], v251, s[40:43], s25 offen sc1\n"
"v_max_f32 v208, 0, v208\n"
"v_max_f32 v209, 0, v209\n"
"v_mfma_f32_16x16x32_bf16 v[24:27], a[172:175], v[188:191], v[24:27]\n"
"s_waitcnt lgkmcnt(3)\n"
"v_mul_f32 v208, v208, v208\n"
"v_mul_f32 v209, v209, v209\n"
"v_mfma_f32_16x16x32_bf16 v[4:7], a[176:179], v[128:131], 0\n"
"s_waitcnt lgkmcnt(2)\n"
"ds_read_b128 a[160:163], v243 offset:8192\n"
"v_max3_f32 v252, v0, v1, v2\n"
"v_mfma_f32_16x16x32_bf16 v[4:7], a[180:183], v[132:135], v[4:7]\n"
"s_waitcnt lgkmcnt(2)\n"
"v_cvt_pk_bf16_f32 v208, v208, v209\n"
"v_max_f32 v210, 0, v210\n"
"v_mfma_f32_16x16x32_bf16 v[4:7], a[184:187], v[136:139], v[4:7]\n"
"s_waitcnt lgkmcnt(1)\n"
"ds_read_b128 a[164:167], v244 offset:8192\n"
"v_max3_f32 v253, v8, v9, v10\n"
"v_mfma_f32_16x16x32_bf16 v[4:7], a[188:191], v[140:143], v[4:7]\n"
"v_max_f32 v211, 0, v211\n"
"v_mul_f32 v210, v210, v210\n"
"v_mfma_f32_16x16x32_bf16 v[12:15], a[176:179], v[144:147], 0\n"
"ds_read_b128 a[168:171], v243 offset:10240\n"
"s_waitcnt vmcnt(4)\n"
"v_max3_f32 v254, v16, v17, v18\n"
"v_mfma_f32_16x16x32_bf16 v[12:15], a[180:183], v[148:151], v[12:15]\n"
"v_mul_f32 v211, v211, v211\n"
"v_cvt_pk_bf16_f32 v209, v210, v211\n"
"v_mfma_f32_16x16x32_bf16 v[12:15], a[184:187], v[152:155], v[12:15]\n"
"ds_read_b128 a[172:175], v244 offset:10240\n"
"v_max3_f32 v255, v24, v25, v26\n"
"v_mfma_f32_16x16x32_bf16 v[12:15], a[188:191], v[156:159], v[12:15]\n"
"v_max_f32 v212, 0, v212\n"
"v_max_f32 v213, 0, v213\n"
"v_mfma_f32_16x16x32_bf16 v[20:23], a[176:179], v[160:163], 0\n"
"v_mul_f32 v212, v212, v212\n"
"v_mul_f32 v213, v213, v213\n"
"v_mfma_f32_16x16x32_bf16 v[20:23], a[180:183], v[164:167], v[20:23]\n"
"v_cvt_pk_bf16_f32 v210, v212, v213\n"
"v_max_f32 v214, 0, v214\n"
"v_mfma_f32_16x16x32_bf16 v[20:23], a[184:187], v[168:171], v[20:23]\n"
"v_max_f32 v215, 0, v215\n"
"v_mul_f32 v214, v214, v214\n"
"v_mfma_f32_16x16x32_bf16 v[20:23], a[188:191], v[172:175], v[20:23]\n"
"v_mul_f32 v215, v215, v215\n"
"v_cvt_pk_bf16_f32 v211, v214, v215\n"
"v_mfma_f32_16x16x32_bf16 v[28:31], a[176:179], v[176:179], 0\n"
"buffer_store_dwordx4 v[208:211], v251, s[40:43], s26 offen sc1\n"
"v_max_f32 v216, 0, v216\n"
"v_max_f32 v217, 0, v217\n"
"v_mfma_f32_16x16x32_bf16 v[28:31], a[180:183], v[180:183], v[28:31]\n"
"v_mul_f32 v216, v216, v216\n"
"v_mul_f32 v217, v217, v217\n"
"v_mfma_f32_16x16x32_bf16 v[28:31], a[184:187], v[184:187], v[28:31]\n"
"v_cvt_pk_bf16_f32 v216, v216, v217\n"
"v_max_f32 v218, 0, v218\n"
"v_mfma_f32_16x16x32_bf16 v[28:31], a[188:191], v[188:191], v[28:31]\n"
"s_waitcnt lgkmcnt(3)\n"
"v_max_f32 v219, 0, v219\n"
"v_mul_f32 v218, v218, v218\n"
"v_mfma_f32_16x16x32_bf16 v[32:35], a[160:163], v[128:131], 0\n"
"s_waitcnt lgkmcnt(2)\n"
"ds_read_b128 a[176:179], v243 offset:12288\n"
"v_max3_f32 v252, v252, v3, v4\n"
"v_mfma_f32_16x16x32_bf16 v[32:35], a[164:167], v[132:135], v[32:35]\n"
"s_waitcnt lgkmcnt(2)\n"
"v_mul_f32 v219, v219, v219\n"
"v_cvt_pk_bf16_f32 v217, v218, v219\n"
"v_mfma_f32_16x16x32_bf16 v[32:35], a[168:171], v[136:139], v[32:35]\n"
"s_waitcnt lgkmcnt(1)\n"
"ds_read_b128 a[180:183], v244 offset:12288\n"
"v_max3_f32 v253, v253, v11, v12\n"
"v_mfma_f32_16x16x32_bf16 v[32:35], a[172:175], v[140:143], v[32:35]\n"
"v_max_f32 v220, 0, v220\n"
"v_max_f32 v221, 0, v221\n"
"v_mfma_f32_16x16x32_bf16 v[40:43], a[160:163], v[144:147], 0\n"
"ds_read_b128 a[184:187], v243 offset:14336\n"
"v_max3_f32 v254, v254, v19, v20\n"
"v_mfma_f32_16x16x32_bf16 v[40:43], a[164:167], v[148:151], v[40:43]\n"
"v_mul_f32 v220, v220, v220\n"
"v_mul_f32 v221, v221, v221\n"
"v_mfma_f32_16x16x32_bf16 v[40:43], a[168:171], v[152:155], v[40:43]\n"
"ds_read_b128 a[188:191], v244 offset:14336\n"
"v_max3_f32 v255, v255, v27, v28\n"
"v_mfma_f32_16x16x32_bf16 v[40:43], a[172:175], v[156:159], v[40:43]\n"
"v_cvt_pk_bf16_f32 v218, v220, v221\n"
"v_max_f32 v222, 0, v222\n"
"v_mfma_f32_16x16x32_bf16 v[48:51], a[160:163], v[160:163], 0\n"
"v_max_f32 v223, 0, v223\n"
"v_mul_f32 v222, v222, v222\n"
"v_mul_f32 v223, v223, v223\n"
"v_cvt_pk_bf16_f32 v219, v222, v223\n"
"v_mfma_f32_16x16x32_bf16 v[48:51], a[164:167], v[164:167], v[48:51]\n"
"buffer_store_dwordx4 v[216:219], v251, s[40:43], s27 offen sc1\n"
"v_mfma_f32_16x16x32_bf16 v[48:51], a[168:171], v[168:171], v[48:51]\n"
"v_mov_b32 v224, 0\n"
"v_mov_b32 v225, 0\n"
"v_mfma_f32_16x16x32_bf16 v[48:51], a[172:175], v[172:175], v[48:51]\n"
"v_mov_b32 v226, 0\n"
"v_mov_b32 v227, 0\n"
"v_mfma_f32_16x16x32_bf16 v[56:59], a[160:163], v[176:179], 0\n"
"v_mov_b32 v228, 0\n"
"v_mov_b32 v229, 0\n"
"v_mfma_f32_16x16x32_bf16 v[56:59], a[164:167], v[180:183], v[56:59]\n"
"v_mov_b32 v230, 0\n"
"v_mov_b32 v231, 0\n"
"v_mfma_f32_16x16x32_bf16 v[56:59], a[168:171], v[184:187], v[56:59]\n"
"v_mov_b32 v232, 0\n"
"v_mov_b32 v233, 0\n"
"v_mfma_f32_16x16x32_bf16 v[56:59], a[172:175], v[188:191], v[56:59]\n"
"s_waitcnt lgkmcnt(3)\n"
"v_mov_b32 v234, 0\n"
"v_mov_b32 v235, 0\n"
"v_mfma_f32_16x16x32_bf16 v[36:39], a[176:179], v[128:131], 0\n"
"s_waitcnt lgkmcnt(0)\n"
"s_barrier\n"
"v_mfma_f32_16x16x32_bf16 v[36:39], a[180:183], v[132:135], v[36:39]\n"
"s_add_u32 m0, s93, 0\n"
"v_mov_b32 v236, 0\n"
"v_mov_b32 v237, 0\n"
"v_mfma_f32_16x16x32_bf16 v[36:39], a[184:187], v[136:139], v[36:39]\n"
"buffer_load_dwordx4 v249, s[52:55], s88 offen lds\n"
"v_mfma_f32_16x16x32_bf16 v[36:39], a[188:191], v[140:143], v[36:39]\n"
"s_add_u32 m0, s93, 1920\n"
"v_mov_b32 v238, 0\n"
"v_mov_b32 v239, 0\n"
"v_mfma_f32_16x16x32_bf16 v[44:47], a[176:179], v[144:147], 0\n"
"buffer_load_dwordx4 v249, s[52:55], s88 offen offset:128 lds\n"
"v_mfma_f32_16x16x32_bf16 v[44:47], a[180:183], v[148:151], v[44:47]\n"
"s_add_u32 s88, s88, 16384\n"
"s_add_u32 m0, s93, 4096\n"
"v_mfma_f32_16x16x32_bf16 v[44:47], a[184:187], v[152:155], v[44:47]\n"
"buffer_load_dwordx4 v249, s[52:55], s89 offen lds\n"
"v_mfma_f32_16x16x32_bf16 v[44:47], a[188:191], v[156:159], v[44:47]\n"
"s_add_u32 m0, s93, 6016\n"
"v_mfma_f32_16x16x32_bf16 v[52:55], a[176:179], v[160:163], 0\n"
"buffer_load_dwordx4 v249, s[52:55], s89 offen offset:128 lds\n"
"v_mfma_f32_16x16x32_bf16 v[52:55], a[180:183], v[164:167], v[52:55]\n"
"s_add_u32 s89, s89, 16384\n"
"v_max3_f32 v252, v252, v5, v6\n"
"v_mfma_f32_16x16x32_bf16 v[52:55], a[184:187], v[168:171], v[52:55]\n"
"v_mfma_f32_16x16x32_bf16 v[52:55], a[188:191], v[172:175], v[52:55]\n"
"v_max3_f32 v252, v252, v7, v32\n"
"v_mfma_f32_16x16x32_bf16 v[60:63], a[176:179], v[176:179], 0\n"
"v_mfma_f32_16x16x32_bf16 v[60:63], a[180:183], v[180:183], v[60:63]\n"
"v_max3_f32 v253, v253, v13, v14\n"
"v_mfma_f32_16x16x32_bf16 v[60:63], a[184:187], v[184:187], v[60:63]\n"
"v_mfma_f32_16x16x32_bf16 v[60:63], a[188:191], v[188:191], v[60:63]\n"
"v_max3_f32 v253, v253, v15, v40\n"
"v_max3_f32 v254, v254, v21, v22\n"
"v_mov_b32 v218, v248\n"
"v_max3_f32 v254, v254, v23, v48\n"
"v_max3_f32 v255, v255, v29, v30\n"
"v_max3_f32 v255, v255, v31, v56\n"
"v_max3_f32 v252, v252, v33, v34\n"
"v_max3_f32 v252, v252, v35, v36\n"
"buffer_load_dword v199, v218, s[16:19], s14 offen\n"
"v_max3_f32 v253, v253, v41, v42\n"
"s_add_u32 s14, s14, 16384\n"
"v_max3_f32 v253, v253, v43, v44\n"
"v_max3_f32 v254, v254, v49, v50\n"
"v_max3_f32 v254, v254, v51, v52\n"
"v_max3_f32 v255, v255, v57, v58\n"
"v_max3_f32 v255, v255, v59, v60\n"
"v_max3_f32 v252, v252, v37, v38\n"
"v_max_f32 v252, v252, v39\n"
"v_max3_f32 v253, v253, v45, v46\n"
"v_max_f32 v253, v253, v47\n"
"v_max3_f32 v254, v254, v53, v54\n"
"v_max_f32 v254, v254, v55\n"
"v_max3_f32 v255, v255, v61, v62\n"
"v_max_f32 v255, v255, v63\n"
"v_max3_f32 v196, v252, v253, v254\n"
"v_max_f32 v196, v196, v255\n"
"v_cmp_lt_f32 vcc, s96, v196\n"
"v_min3_f32 v197, v252, v253, v254\n"
"v_min_f32 v197, v197, v255\n"
"v_cmp_gt_f32 s[98:99], -s96, v197\n"
"s_nop 0\n"
"s_or_b64 vcc, vcc, s[98:99]\n"
"s_cbranch_vccnz RA0_%=\n"
"RAr0_%=:\n"
"v_exp_f32 v0, v0\n"
"v_exp_f32 v1, v1\n"
"s_nop 0\n"
"v_cvt_pk_bf16_f32 v0, v0, v1\n"
"v_exp_f32 v2, v2\n"
"v_exp_f32 v3, v3\n"
"s_nop 0\n"
"v_cvt_pk_bf16_f32 v1, v2, v3\n"
"v_exp_f32 v4, v4\n"
"s_and_b32 s98, s37, 1\n"
"s_lshl_b32 s98, s98, 7\n"
"v_lshrrev_b32 v198, 8, v250\n"
"s_movk_i32 s99, 768\n"
"v_mad_u32_u24 v247, v198, s99, v250\n"
"v_add_u32 v247, s98, v247\n"
"v_xor_b32 v248, 128, v247\n"
"v_subrev_u32 v204, 65536, v241\n"
"v_subrev_u32 v205, 65536, v242\n"
"v_subrev_u32 v208, 32768, v245\n"
"v_subrev_u32 v209, 32768, v246\n"
"v_lshrrev_b32 v207, 1, v251\n"
"v_and_b32 v207, 248, v207\n"
"v_lshrrev_b32 v198, 8, v251\n"
"v_lshlrev_b32 v198, 3, v198\n"
"v_and_b32 v206, 240, v251\n"
"v_lshl_add_u32 v206, v206, 6, v198\n"
"s_lshl_b32 s86, s38, 11\n"
"s_lshl_b32 s87, s38, 10\n"
"s_mov_b32 s84, 0\n"
"s_mov_b32 s85, 0\n"
"v_cmp_gt_u32 vcc, 512, v251\n"
"v_lshlrev_b32 v213, 2, v251\n"
"v_and_b32 v198, 64, v213\n"
"v_and_b32 v214, 992, v251\n"
"v_lshl_add_u32 v214, v214, 5, v198\n"
"v_mov_b32 v198, 0x40000000\n"
"v_cndmask_b32 v213, v198, v213, vcc\n"
"v_cndmask_b32 v214, v198, v214, vcc\n"
"s_lshl_b32 s98, s12, 15\n"
"s_lshl_b32 s99, s13, 8\n"
"s_sub_u32 s13, s98, s99\n"
"s_lshr_b32 s98, s12, 2\n"
"s_mul_i32 s98, s98, 172\n"
"s_mul_i32 s99, s38, 35\n"
"s_add_u32 s98, s98, s99\n"
"s_lshl_b32 s98, s98, 3\n"
"s_and_b32 s99, s37, 1\n"
"s_add_u32 s98, s98, s99\n"
"s_lshl_b32 s12, s98, 7\n"
"v_exp_f32 v5, v5\n"
"s_nop 0\n"
"v_cvt_pk_bf16_f32 v2, v4, v5\n"
"v_exp_f32 v6, v6\n"
"v_exp_f32 v7, v7\n"
"s_nop 0\n"
"v_cvt_pk_bf16_f32 v3, v6, v7\n"
"v_exp_f32 v32, v32\n"
"v_exp_f32 v33, v33\n"
"s_nop 0\n"
"v_cvt_pk_bf16_f32 v32, v32, v33\n"
"v_exp_f32 v34, v34\n"
"v_exp_f32 v35, v35\n"
"s_nop 0\n"
"v_cvt_pk_bf16_f32 v33, v34, v35\n"
"v_exp_f32 v36, v36\n"
"v_exp_f32 v37, v37\n"
"s_nop 0\n"
"v_cvt_pk_bf16_f32 v34, v36, v37\n"
"v_exp_f32 v38, v38\n"
"v_exp_f32 v39, v39\n"
"s_nop 0\n"
"v_cvt_pk_bf16_f32 v35, v38, v39\n"
"v_exp_f32 v8, v8\n"
"ds_read_b128 a[160:163], v243 offset:16384\n"
"v_exp_f32 v9, v9\n"
"ds_read_b128 a[164:167], v244 offset:16384\n"
"v_cvt_pk_bf16_f32 v8, v8, v9\n"
"v_exp_f32 v10, v10\n"
"ds_read_b128 a[168:171], v243 offset:18432\n"
"buffer_load_dword v216, v214, s[60:63], s12 offen\n"
"s_add_u32 s12, s12, 16384\n"
"v_exp_f32 v11, v11\n"
"ds_read_b128 a[172:175], v244 offset:18432\n"
"v_cvt_pk_bf16_f32 v9, v10, v11\n"
"v_exp_f32 v12, v12\n"
"v_exp_f32 v13, v13\n"
"v_exp_f32 v14, v14\n"
"v_exp_f32 v15, v15\n"
"v_exp_f32 v40, v40\n"
"v_exp_f32 v41, v41\n"
"v_exp_f32 v42, v42\n"
"v_exp_f32 v43, v43\n"
"v_exp_f32 v44, v44\n"
"s_waitcnt lgkmcnt(3)\n"
"v_mfma_f32_16x16x32_bf16 v[64:67], a[160:163], v[128:131], v[224:227]\n"
"s_waitcnt lgkmcnt(2)\n"
"ds_read_b128 a[176:179], v243 offset:20480\n"
"v_cvt_pk_bf16_f32 v10, v12, v13\n"
"v_mfma_f32_16x16x32_bf16 v[64:67], a[164:167], v[132:135], v[64:67]\n"
"s_waitcnt lgkmcnt(2)\n"
"v_exp_f32 v45, v45\n"
"v_mfma_f32_16x16x32_bf16 v[64:67], a[168:171], v[136:139], v[64:67]\n"
"s_waitcnt lgkmcnt(1)\n"
"ds_read_b128 a[180:183], v244 offset:20480\n"
"v_cvt_pk_bf16_f32 v11, v14, v15\n"
"v_mfma_f32_16x16x32_bf16 v[64:67], a[172:175], v[140:143], v[64:67]\n"
"v_exp_f32 v46, v46\n"
"v_mfma_f32_16x16x32_bf16 v[72:75], a[160:163], v[144:147], v[228:231]\n"
"ds_read_b128 a[184:187], v243 offset:22528\n"
"v_cvt_pk_bf16_f32 v40, v40, v41\n"
"v_mfma_f32_16x16x32_bf16 v[72:75], a[164:167], v[148:151], v[72:75]\n"
"v_exp_f32 v47, v47\n"
"v_mfma_f32_16x16x32_bf16 v[72:75], a[168:171], v[152:155], v[72:75]\n"
"ds_read_b128 a[188:191], v244 offset:22528\n"
"v_cvt_pk_bf16_f32 v41, v42, v43\n"
"v_mfma_f32_16x16x32_bf16 v[72:75], a[172:175], v[156:159], v[72:75]\n"
"v_exp_f32 v16, v16\n"
"v_mfma_f32_16x16x32_bf16 v[80:83], a[160:163], v[160:163], v[232:235]\n"
"v_exp_f32 v17, v17\n"
"v_mfma_f32_16x16x32_bf16 v[80:83], a[164:167], v[164:167], v[80:83]\n"
"v_exp_f32 v18, v18\n"
"v_mfma_f32_16x16x32_bf16 v[80:83], a[168:171], v[168:171], v[80:83]\n"
"v_exp_f32 v19, v19\n"
"v_mfma_f32_16x16x32_bf16 v[80:83], a[172:175], v[172:175], v[80:83]\n"
"v_exp_f32 v20, v20\n"
"v_mfma_f32_16x16x32_bf16 v[88:91], a[160:163], v[176:179], v[236:239]\n"
"v_exp_f32 v21, v21\n"
"v_mfma_f32_16x16x32_bf16 v[88:91], a[164:167], v[180:183], v[88:91]\n"
"v_exp_f32 v22, v22\n"
"v_mfma_f32_16x16x32_bf16 v[88:91], a[168:171], v[184:187], v[88:91]\n"
"v_exp_f32 v23, v23\n"
"v_mfma_f32_16x16x32_bf16 v[88:91], a[172:175], v[188:191], v[88:91]\n"
"s_waitcnt lgkmcnt(3)\n"
"v_exp_f32 v48, v48\n"
"v_mfma_f32_16x16x32_bf16 v[68:71], a[176:179], v[128:131], v[224:227]\n"
"s_waitcnt lgkmcnt(2)\n"
"ds_read_b128 a[160:163], v243 offset:24576\n"
"v_max3_f32 v252, v64, v65, v66\n"
"v_mfma_f32_16x16x32_bf16 v[68:71], a[180:183], v[132:135], v[68:71]\n"
"s_waitcnt lgkmcnt(2)\n"
"v_exp_f32 v49, v49\n"
"v_mfma_f32_16x16x32_bf16 v[68:71], a[184:187], v[136:139], v[68:71]\n"
"s_waitcnt lgkmcnt(1)\n"
"ds_read_b128 a[164:167], v244 offset:24576\n"
"v_max3_f32 v253, v72, v73, v74\n"
"v_mfma_f32_16x16x32_bf16 v[68:71], a[188:191], v[140:143], v[68:71]\n"
"v_exp_f32 v50, v50\n"
"v_mfma_f32_16x16x32_bf16 v[76:79], a[176:179], v[144:147], v[228:231]\n"
"ds_read_b128 a[168:171], v243 offset:26624\n"
"v_max3_f32 v254, v80, v81, v82\n"
"v_mfma_f32_16x16x32_bf16 v[76:79], a[180:183], v[148:151], v[76:79]\n"
"v_cvt_pk_bf16_f32 v42, v44, v45\n"
"v_cvt_pk_bf16_f32 v43, v46, v47\n"
"v_mfma_f32_16x16x32_bf16 v[76:79], a[184:187], v[152:155], v[76:79]\n"
"ds_read_b128 a[172:175], v244 offset:26624\n"
"v_max3_f32 v255, v88, v89, v90\n"
"v_mfma_f32_16x16x32_bf16 v[76:79], a[188:191], v[156:159], v[76:79]\n"
"v_exp_f32 v51, v51\n"
"v_mfma_f32_16x16x32_bf16 v[84:87], a[176:179], v[160:163], v[232:235]\n"
"v_exp_f32 v52, v52\n"
"v_mfma_f32_16x16x32_bf16 v[84:87], a[180:183], v[164:167], v[84:87]\n"
"v_exp_f32 v53, v53\n"
"v_mfma_f32_16x16x32_bf16 v[84:87], a[184:187], v[168:171], v[84:87]\n"
"v_exp_f32 v54, v54\n"
"v_mfma_f32_16x16x32_bf16 v[84:87], a[188:191], v[172:175], v[84:87]\n"
"v_exp_f32 v55, v55\n"
"v_mfma_f32_16x16x32_bf16 v[92:95], a[176:179], v[176:179], v[236:239]\n"
"v_exp_f32 v24, v24\n"
"v_mfma_f32_16x16x32_bf16 v[92:95], a[180:183], v[180:183], v[92:95]\n"
"v_exp_f32 v25, v25\n"
"v_mfma_f32_16x16x32_bf16 v[92:95], a[184:187], v[184:187], v[92:95]\n"
"v_cvt_pk_bf16_f32 v16, v16, v17\n"
"v_cvt_pk_bf16_f32 v17, v18, v19\n"
"v_mfma_f32_16x16x32_bf16 v[92:95], a[188:191], v[188:191], v[92:95]\n"
"s_waitcnt lgkmcnt(3)\n"
"v_cvt_pk_bf16_f32 v18, v20, v21\n"
"v_cvt_pk_bf16_f32 v19, v22, v23\n"
"v_mfma_f32_16x16x32_bf16 v[96:99], a[160:163], v[128:131], v[224:227]\n"
"s_waitcnt lgkmcnt(2)\n"
"ds_read_b128 a[176:179], v243 offset:28672\n"
"v_max3_f32 v252, v252, v67, v68\n"
"v_mfma_f32_16x16x32_bf16 v[96:99], a[164:167], v[132:135], v[96:99]\n"
"s_waitcnt lgkmcnt(2)\n"
"v_exp_f32 v26, v26\n"
"v_mfma_f32_16x16x32_bf16 v[96:99], a[168:171], v[136:139], v[96:99]\n"
"s_waitcnt lgkmcnt(1)\n"
"ds_read_b128 a[180:183], v244 offset:28672\n"
"v_max3_f32 v253, v253, v75, v76\n"
"v_mfma_f32_16x16x32_bf16 v[96:99], a[172:175], v[140:143], v[96:99]\n"
"v_exp_f32 v27, v27\n"
"v_mfma_f32_16x16x32_bf16 v[104:107], a[160:163], v[144:147], v[228:231]\n"
"ds_read_b128 a[184:187], v243 offset:30720\n"
"v_max3_f32 v254, v254, v83, v84\n"
"v_mfma_f32_16x16x32_bf16 v[104:107], a[164:167], v[148:151], v[104:107]\n"
"v_exp_f32 v28, v28\n"
"v_mfma_f32_16x16x32_bf16 v[104:107], a[168:171], v[152:155], v[104:107]\n"
"ds_read_b128 a[188:191], v244 offset:30720\n"
"v_max3_f32 v255, v255, v91, v92\n"
"v_mfma_f32_16x16x32_bf16 v[104:107], a[172:175], v[156:159], v[104:107]\n"
"v_exp_f32 v29, v29\n"
"v_mfma_f32_16x16x32_bf16 v[112:115], a[160:163], v[160:163], v[232:235]\n"
"v_exp_f32 v30, v30\n"
"v_mfma_f32_16x16x32_bf16 v[112:115], a[164:167], v[164:167], v[112:115]\n"
"v_cvt_pk_bf16_f32 v48, v48, v49\n"
"v_cvt_pk_bf16_f32 v49, v50, v51\n"
"v_mfma_f32_16x16x32_bf16 v[112:115], a[168:171], v[168:171], v[112:115]\n"
"v_cvt_pk_bf16_f32 v50, v52, v53\n"
"v_cvt_pk_bf16_f32 v51, v54, v55\n"
"v_mfma_f32_16x16x32_bf16 v[112:115], a[172:175], v[172:175], v[112:115]\n"
"v_exp_f32 v31, v31\n"
"v_mfma_f32_16x16x32_bf16 v[120:123], a[160:163], v[176:179], v[236:239]\n"
"v_exp_f32 v56, v56\n"
"v_mfma_f32_16x16x32_bf16 v[120:123], a[164:167], v[180:183], v[120:123]\n"
"v_exp_f32 v57, v57\n"
"v_mfma_f32_16x16x32_bf16 v[120:123], a[168:171], v[184:187], v[120:123]\n"
"v_exp_f32 v58, v58\n"
"v_mfma_f32_16x16x32_bf16 v[120:123], a[172:175], v[188:191], v[120:123]\n"
"s_waitcnt lgkmcnt(3)\n"
"v_exp_f32 v59, v59\n"
"v_mfma_f32_16x16x32_bf16 v[100:103], a[176:179], v[128:131], v[224:227]\n"
"s_waitcnt vmcnt(2) lgkmcnt(0)\n"
"s_barrier\n"
"v_exp_f32 v60, v60\n"
"v_mfma_f32_16x16x32_bf16 v[100:103], a[180:183], v[132:135], v[100:103]\n"
"s_add_u32 m0, s94, 16384\n"
"v_exp_f32 v61, v61\n"
"v_mfma_f32_16x16x32_bf16 v[100:103], a[184:187], v[136:139], v[100:103]\n"
"buffer_load_dwordx4 v250, s[56:59], s90 offen lds\n"
"v_mfma_f32_16x16x32_bf16 v[100:103], a[188:191], v[140:143], v[100:103]\n"
"s_add_u32 m0, s94, 17280\n"
"v_exp_f32 v62, v62\n"
"v_mfma_f32_16x16x32_bf16 v[108:111], a[176:179], v[144:147], v[228:231]\n"
"buffer_load_dwordx4 v250, s[56:59], s90 offen offset:128 lds\n"
"v_mfma_f32_16x16x32_bf16 v[108:111], a[180:183], v[148:151], v[108:111]\n"
"s_add_u32 m0, s94, 16384\n"
"v_cvt_pk_bf16_f32 v24, v24, v25\n"
"v_cvt_pk_bf16_f32 v25, v26, v27\n"
"v_mfma_f32_16x16x32_bf16 v[108:111], a[184:187], v[152:155], v[108:111]\n"
"buffer_load_dwordx4 v250, s[56:59], s90 offen offset:2048 lds\n"
"v_mfma_f32_16x16x32_bf16 v[108:111], a[188:191], v[156:159], v[108:111]\n"
"s_add_u32 m0, s94, 17280\n"
"v_cvt_pk_bf16_f32 v26, v28, v29\n"
"v_cvt_pk_bf16_f32 v27, v30, v31\n"
"v_mfma_f32_16x16x32_bf16 v[116:119], a[176:179], v[160:163], v[232:235]\n"
"buffer_load_dwordx4 v250, s[56:59], s90 offen offset:2176 lds\n"
"v_mfma_f32_16x16x32_bf16 v[116:119], a[180:183], v[164:167], v[116:119]\n"
"ds_read_b64_tr_b16 a[192:193], v245 offset:0\n"
"v_max3_f32 v252, v252, v69, v70\n"
"v_mfma_f32_16x16x32_bf16 v[116:119], a[184:187], v[168:171], v[116:119]\n"
"v_exp_f32 v63, v63\n"
"v_mfma_f32_16x16x32_bf16 v[116:119], a[188:191], v[172:175], v[116:119]\n"
"ds_read_b64_tr_b16 a[194:195], v245 offset:4096\n"
"v_max3_f32 v252, v252, v71, v96\n"
"v_mfma_f32_16x16x32_bf16 v[124:127], a[176:179], v[176:179], v[236:239]\n"
"v_cvt_pk_bf16_f32 v56, v56, v57\n"
"v_cvt_pk_bf16_f32 v57, v58, v59\n"
"v_mfma_f32_16x16x32_bf16 v[124:127], a[180:183], v[180:183], v[124:127]\n"
"ds_read_b64_tr_b16 a[196:197], v245 offset:8192\n"
"v_max3_f32 v253, v253, v77, v78\n"
"v_mfma_f32_16x16x32_bf16 v[124:127], a[184:187], v[184:187], v[124:127]\n"
"s_add_u32 s90, s90, 16384\n"
"v_cvt_pk_bf16_f32 v58, v60, v61\n"
"v_cvt_pk_bf16_f32 v59, v62, v63\n"
"v_mfma_f32_16x16x32_bf16 v[124:127], a[188:191], v[188:191], v[124:127]\n"
"s_add_u32 m0, s93, 16384\n"
"ds_read_b64_tr_b16 a[198:199], v245 offset:12288\n"
"v_max3_f32 v253, v253, v79, v104\n"
"v_mfma_f32_16x16x32_bf16 a[128:131], a[224:227], v[0:3], 0\n"
"buffer_load_dwordx4 v249, s[52:55], s88 offen lds\n"
"v_mfma_f32_16x16x32_bf16 a[128:131], a[224:227], v[32:35], a[128:131]\n"
"s_add_u32 m0, s93, 18304\n"
"ds_read_b64_tr_b16 a[200:201], v246 offset:0\n"
"v_max3_f32 v254, v254, v85, v86\n"
"v_mfma_f32_16x16x32_bf16 a[132:135], a[224:227], v[8:11], 0\n"
"buffer_load_dwordx4 v249, s[52:55], s88 offen offset:128 lds\n"
"v_mfma_f32_16x16x32_bf16 a[132:135], a[224:227], v[40:43], a[132:135]\n"
"s_add_u32 s88, s88, 16384\n"
"s_add_u32 m0, s93, 20480\n"
"ds_read_b64_tr_b16 a[202:203], v246 offset:4096\n"
"v_max3_f32 v254, v254, v87, v112\n"
"v_mfma_f32_16x16x32_bf16 a[136:139], a[224:227], v[16:19], 0\n"
"buffer_load_dwordx4 v249, s[52:55], s89 offen lds\n"
"v_mfma_f32_16x16x32_bf16 a[136:139], a[224:227], v[48:51], a[136:139]\n"
"s_add_u32 m0, s93, 22400\n"
"ds_read_b64_tr_b16 a[204:205], v246 offset:8192\n"
"v_max3_f32 v255, v255, v93, v94\n"
"v_mfma_f32_16x16x32_bf16 a[140:143], a[224:227], v[24:27], 0\n"
"buffer_load_dwordx4 v249, s[52:55], s89 offen offset:128 lds\n"
"v_mfma_f32_16x16x32_bf16 a[140:143], a[224:227], v[56:59], a[140:143]\n"
"s_waitcnt lgkmcnt(5)\n"
"s_add_u32 s89, s89, 16384\n"
"ds_read_b64_tr_b16 a[206:207], v246 offset:12288\n"
"v_max3_f32 v255, v255, v95, v120\n"
"v_mfma_f32_16x16x32_bf16 a[0:3], a[192:195], v[0:3], 0\n"
"s_waitcnt lgkmcnt(4)\n"
"v_max3_f32 v252, v252, v97, v98\n"
"v_max3_f32 v252, v252, v99, v100\n"
"v_mfma_f32_16x16x32_bf16 a[0:3], a[196:199], v[32:35], a[0:3]\n"
"ds_read_b64_tr_b16 a[208:209], v245 offset:512\n"
"buffer_load_dword v199, v218, s[16:19], s14 offen\n"
"v_max3_f32 v253, v253, v105, v106\n"
"v_mfma_f32_16x16x32_bf16 a[4:7], a[192:195], v[8:11], 0\n"
"s_add_u32 s14, s14, 16384\n"
"v_max3_f32 v253, v253, v107, v108\n"
"v_max3_f32 v254, v254, v113, v114\n"
"v_mfma_f32_16x16x32_bf16 a[4:7], a[196:199], v[40:43], a[4:7]\n"
"ds_read_b64_tr_b16 a[210:211], v245 offset:4608\n"
"v_max3_f32 v254, v254, v115, v116\n"
"v_mfma_f32_16x16x32_bf16 a[8:11], a[192:195], v[16:19], 0\n"
"v_max3_f32 v255, v255, v121, v122\n"
"v_max3_f32 v255, v255, v123, v124\n"
"v_mfma_f32_16x16x32_bf16 a[8:11], a[196:199], v[48:51], a[8:11]\n"
"ds_read_b64_tr_b16 a[212:213], v245 offset:8704\n"
"v_max3_f32 v252, v252, v101, v102\n"
"v_mfma_f32_16x16x32_bf16 a[12:15], a[192:195], v[24:27], 0\n"
"v_max_f32 v252, v252, v103\n"
"v_max3_f32 v253, v253, v109, v110\n"
"v_mfma_f32_16x16x32_bf16 a[12:15], a[196:199], v[56:59], a[12:15]\n"
"s_waitcnt lgkmcnt(5)\n"
"ds_read_b64_tr_b16 a[214:215], v245 offset:12800\n"
"v_max_f32 v253, v253, v111\n"
"v_mfma_f32_16x16x32_bf16 a[16:19], a[200:203], v[0:3], 0\n"
"s_waitcnt lgkmcnt(4)\n"
"v_max3_f32 v254, v254, v117, v118\n"
"v_max_f32 v254, v254, v119\n"
"v_mfma_f32_16x16x32_bf16 a[16:19], a[204:207], v[32:35], a[16:19]\n"
"ds_read_b64_tr_b16 a[216:217], v246 offset:512\n"
"v_max3_f32 v255, v255, v125, v126\n"
"v_mfma_f32_16x16x32_bf16 a[20:23], a[200:203], v[8:11], 0\n"
"v_max_f32 v255, v255, v127\n"
"v_max3_f32 v196, v252, v253, v254\n"
"v_mfma_f32_16x16x32_bf16 a[20:23], a[204:207], v[40:43], a[20:23]\n"
"ds_read_b64_tr_b16 a[218:219], v246 offset:4608\n"
"v_max_f32 v196, v196, v255\n"
"v_mfma_f32_16x16x32_bf16 a[24:27], a[200:203], v[16:19], 0\n"
"v_cmp_lt_f32 vcc, s96, v196\n"
"v_mfma_f32_16x16x32_bf16 a[24:27], a[204:207], v[48:51], a[24:27]\n"
"ds_read_b64_tr_b16 a[220:221], v246 offset:8704\n"
"v_mfma_f32_16x16x32_bf16 a[28:31], a[200:203], v[24:27], 0\n"
"s_cbranch_vccnz RA1_%=\n"
"RAr1_%=:\n"
"v_exp_f32 v64, v64\n"
"v_mfma_f32_16x16x32_bf16 a[28:31], a[204:207], v[56:59], a[28:31]\n"
"s_waitcnt lgkmcnt(5)\n"
"ds_read_b64_tr_b16 a[222:223], v246 offset:12800\n"
"v_mfma_f32_16x16x32_bf16 a[32:35], a[208:211], v[0:3], 0\n"
"s_waitcnt lgkmcnt(4)\n"
"v_exp_f32 v65, v65\n"
"v_mfma_f32_16x16x32_bf16 a[32:35], a[212:215], v[32:35], a[32:35]\n"
"ds_read_b64_tr_b16 a[192:193], v245 offset:1024\n"
"v_cvt_pk_bf16_f32 v64, v64, v65\n"
"v_mfma_f32_16x16x32_bf16 a[36:39], a[208:211], v[8:11], 0\n"
"v_exp_f32 v66, v66\n"
"v_mfma_f32_16x16x32_bf16 a[36:39], a[212:215], v[40:43], a[36:39]\n"
"ds_read_b64_tr_b16 a[194:195], v245 offset:5120\n"
"v_mfma_f32_16x16x32_bf16 a[40:43], a[208:211], v[16:19], 0\n"
"v_exp_f32 v67, v67\n"
"v_mfma_f32_16x16x32_bf16 a[40:43], a[212:215], v[48:51], a[40:43]\n"
"ds_read_b64_tr_b16 a[196:197], v245 offset:9216\n"
"v_cvt_pk_bf16_f32 v65, v66, v67\n"
"v_mfma_f32_16x16x32_bf16 a[44:47], a[208:211], v[24:27], 0\n"
"v_exp_f32 v68, v68\n"
"v_mfma_f32_16x16x32_bf16 a[44:47], a[212:215], v[56:59], a[44:47]\n"
"s_waitcnt lgkmcnt(5)\n"
"ds_read_b64_tr_b16 a[198:199], v245 offset:13312\n"
"v_mfma_f32_16x16x32_bf16 a[48:51], a[216:219], v[0:3], 0\n"
"s_waitcnt lgkmcnt(4)\n"
"v_exp_f32 v69, v69\n"
"v_mfma_f32_16x16x32_bf16 a[48:51], a[220:223], v[32:35], a[48:51]\n"
"ds_read_b64_tr_b16 a[200:201], v246 offset:1024\n"
"v_cvt_pk_bf16_f32 v66, v68, v69\n"
"v_mfma_f32_16x16x32_bf16 a[52:55], a[216:219], v[8:11], 0\n"
"v_exp_f32 v70, v70\n"
"v_mfma_f32_16x16x32_bf16 a[52:55], a[220:223], v[40:43], a[52:55]\n"
"ds_read_b64_tr_b16 a[202:203], v246 offset:5120\n"
"v_mfma_f32_16x16x32_bf16 a[56:59], a[216:219], v[16:19], 0\n"
"v_exp_f32 v71, v71\n"
"v_mfma_f32_16x16x32_bf16 a[56:59], a[220:223], v[48:51], a[56:59]\n"
"ds_read_b64_tr_b16 a[204:205], v246 offset:9216\n"
"v_cvt_pk_bf16_f32 v67, v70, v71\n"
"v_mfma_f32_16x16x32_bf16 a[60:63], a[216:219], v[24:27], 0\n"
"v_exp_f32 v96, v96\n"
"v_mfma_f32_16x16x32_bf16 a[60:63], a[220:223], v[56:59], a[60:63]\n"
"s_waitcnt lgkmcnt(5)\n"
"ds_read_b64_tr_b16 a[206:207], v246 offset:13312\n"
"v_mfma_f32_16x16x32_bf16 a[64:67], a[192:195], v[0:3], 0\n"
"s_waitcnt lgkmcnt(4)\n"
"v_exp_f32 v97, v97\n"
"v_mfma_f32_16x16x32_bf16 a[64:67], a[196:199], v[32:35], a[64:67]\n"
"ds_read_b64_tr_b16 a[208:209], v245 offset:1536\n"
"v_cvt_pk_bf16_f32 v96, v96, v97\n"
"v_mfma_f32_16x16x32_bf16 a[68:71], a[192:195], v[8:11], 0\n"
"v_exp_f32 v98, v98\n"
"v_mfma_f32_16x16x32_bf16 a[68:71], a[196:199], v[40:43], a[68:71]\n"
"ds_read_b64_tr_b16 a[210:211], v245 offset:5632\n"
"v_mfma_f32_16x16x32_bf16 a[72:75], a[192:195], v[16:19], 0\n"
"v_exp_f32 v99, v99\n"
"v_mfma_f32_16x16x32_bf16 a[72:75], a[196:199], v[48:51], a[72:75]\n"
"ds_read_b64_tr_b16 a[212:213], v245 offset:9728\n"
"v_cvt_pk_bf16_f32 v97, v98, v99\n"
"v_mfma_f32_16x16x32_bf16 a[76:79], a[192:195], v[24:27], 0\n"
"v_exp_f32 v100, v100\n"
"v_mfma_f32_16x16x32_bf16 a[76:79], a[196:199], v[56:59], a[76:79]\n"
"s_waitcnt lgkmcnt(5)\n"
"ds_read_b64_tr_b16 a[214:215], v245 offset:13824\n"
"buffer_load_dword v217, v214, s[60:63], s12 offen\n"
"v_mfma_f32_16x16x32_bf16 a[80:83], a[200:203], v[0:3], 0\n"
"s_waitcnt lgkmcnt(4)\n"
"s_add_u32 s12, s12, 16384\n"
"v_exp_f32 v101, v101\n"
"v_mfma_f32_16x16x32_bf16 a[80:83], a[204:207], v[32:35], a[80:83]\n"
"ds_read_b64_tr_b16 a[216:217], v246 offset:1536\n"
"v_cvt_pk_bf16_f32 v98, v100, v101\n"
"v_mfma_f32_16x16x32_bf16 a[84:87], a[200:203], v[8:11], 0\n"
"v_exp_f32 v102, v102\n"
"v_mfma_f32_16x16x32_bf16 a[84:87], a[204:207], v[40:43], a[84:87]\n"
"ds_read_b64_tr_b16 a[218:219], v246 offset:5632\n"
"v_mfma_f32_16x16x32_bf16 a[88:91], a[200:203], v[16:19], 0\n"
"v_exp_f32 v103, v103\n"
"v_mfma_f32_16x16x32_bf16 a[88:91], a[204:207], v[48:51], a[88:91]\n"
"ds_read_b64_tr_b16 a[220:221], v246 offset:9728\n"
"v_cvt_pk_bf16_f32 v99, v102, v103\n"
"v_mfma_f32_16x16x32_bf16 a[92:95], a[200:203], v[24:27], 0\n"
"v_exp_f32 v72, v72\n"
"v_mfma_f32_16x16x32_bf16 a[92:95], a[204:207], v[56:59], a[92:95]\n"
"s_waitcnt lgkmcnt(5)\n"
"ds_read_b64_tr_b16 a[222:223], v246 offset:13824\n"
"v_mfma_f32_16x16x32_bf16 a[96:99], a[208:211], v[0:3], 0\n"
"s_waitcnt lgkmcnt(4)\n"
"ds_read_b128 a[160:163], v243 offset:0\n"
"v_mfma_f32_16x16x32_bf16 a[96:99], a[212:215], v[32:35], a[96:99]\n"
"v_exp_f32 v73, v73\n"
"v_mfma_f32_16x16x32_bf16 a[100:103], a[208:211], v[8:11], 0\n"
"ds_read_b128 a[164:167], v244 offset:0\n"
"v_cvt_pk_bf16_f32 v72, v72, v73\n"
"v_mfma_f32_16x16x32_bf16 a[100:103], a[212:215], v[40:43], a[100:103]\n"
"v_exp_f32 v74, v74\n"
"v_mfma_f32_16x16x32_bf16 a[104:107], a[208:211], v[16:19], 0\n"
"ds_read_b128 a[168:171], v243 offset:2048\n"
"v_mfma_f32_16x16x32_bf16 a[104:107], a[212:215], v[48:51], a[104:107]\n"
"v_exp_f32 v75, v75\n"
"v_mfma_f32_16x16x32_bf16 a[108:111], a[208:211], v[24:27], 0\n"
"ds_read_b128 a[172:175], v244 offset:2048\n"
"v_cvt_pk_bf16_f32 v73, v74, v75\n"
"v_mfma_f32_16x16x32_bf16 a[108:111], a[212:215], v[56:59], a[108:111]\n"
"s_waitcnt lgkmcnt(6)\n"
"v_exp_f32 v76, v76\n"
"v_mfma_f32_16x16x32_bf16 a[112:115], a[216:219], v[0:3], 0\n"
"s_waitcnt lgkmcnt(4)\n"
"v_exp_f32 v77, v77\n"
"v_mfma_f32_16x16x32_bf16 a[112:115], a[220:223], v[32:35], a[112:115]\n"
"v_exp_f32 v78, v78\n"
"v_mfma_f32_16x16x32_bf16 a[116:119], a[216:219], v[8:11], 0\n"
"v_exp_f32 v79, v79\n"
"v_mfma_f32_16x16x32_bf16 a[116:119], a[220:223], v[40:43], a[116:119]\n"
"v_exp_f32 v104, v104\n"
"v_mfma_f32_16x16x32_bf16 a[120:123], a[216:219], v[16:19], 0\n"
"v_exp_f32 v105, v105\n"
"v_mfma_f32_16x16x32_bf16 a[120:123], a[220:223], v[48:51], a[120:123]\n"
"v_exp_f32 v106, v106\n"
"v_mfma_f32_16x16x32_bf16 a[124:127], a[216:219], v[24:27], 0\n"
"v_exp_f32 v107, v107\n"
"v_mfma_f32_16x16x32_bf16 a[124:127], a[220:223], v[56:59], a[124:127]\n"
"v_exp_f32 v108, v108\n"
"s_waitcnt lgkmcnt(3)\n"
"v_mfma_f32_16x16x32_bf16 v[0:3], a[160:163], v[128:131], v[224:227]\n"
"s_cmp_lg_u32 s95, 0\n"
"s_cbranch_scc1 RB1_%=\n"
"RBr1_%=:\n"
"ds_read_b128 a[176:179], v243 offset:4096\n"
"v_cvt_pk_bf16_f32 v74, v76, v77\n"
"s_waitcnt lgkmcnt(3)\n"
"v_mfma_f32_16x16x32_bf16 v[0:3], a[164:167], v[132:135], v[0:3]\n"
"s_waitcnt lgkmcnt(2)\n"
"v_exp_f32 v109, v109\n"
"v_mfma_f32_16x16x32_bf16 v[0:3], a[168:171], v[136:139], v[0:3]\n"
"s_waitcnt lgkmcnt(1)\n"
"ds_read_b128 a[180:183], v244 offset:4096\n"
"v_cvt_pk_bf16_f32 v75, v78, v79\n"
"v_mfma_f32_16x16x32_bf16 v[0:3], a[172:175], v[140:143], v[0:3]\n"
"v_exp_f32 v110, v110\n"
"v_mfma_f32_16x16x32_bf16 v[8:11], a[160:163], v[144:147], v[228:231]\n"
"ds_read_b128 a[184:187], v243 offset:6144\n"
"v_cvt_pk_bf16_f32 v104, v104, v105\n"
"v_mfma_f32_16x16x32_bf16 v[8:11], a[164:167], v[148:151], v[8:11]\n"
"v_exp_f32 v111, v111\n"
"v_mfma_f32_16x16x32_bf16 v[8:11], a[168:171], v[152:155], v[8:11]\n"
"ds_read_b128 a[188:191], v244 offset:6144\n"
"v_cvt_pk_bf16_f32 v105, v106, v107\n"
"v_mfma_f32_16x16x32_bf16 v[8:11], a[172:175], v[156:159], v[8:11]\n"
"v_exp_f32 v80, v80\n"
"v_mfma_f32_16x16x32_bf16 v[16:19], a[160:163], v[160:163], v[232:235]\n"
"v_exp_f32 v81, v81\n"
"v_mfma_f32_16x16x32_bf16 v[16:19], a[164:167], v[164:167], v[16:19]\n"
"v_exp_f32 v82, v82\n"
"v_mfma_f32_16x16x32_bf16 v[16:19], a[168:171], v[168:171], v[16:19]\n"
"v_exp_f32 v83, v83\n"
"v_mfma_f32_16x16x32_bf16 v[16:19], a[172:175], v[172:175], v[16:19]\n"
"v_exp_f32 v84, v84\n"
"v_mfma_f32_16x16x32_bf16 v[24:27], a[160:163], v[176:179], v[236:239]\n"
"v_exp_f32 v85, v85\n"
"v_mfma_f32_16x16x32_bf16 v[24:27], a[164:167], v[180:183], v[24:27]\n"
"v_exp_f32 v86, v86\n"
"v_mfma_f32_16x16x32_bf16 v[24:27], a[168:171], v[184:187], v[24:27]\n"
"v_exp_f32 v87, v87\n"
"v_mfma_f32_16x16x32_bf16 v[24:27], a[172:175], v[188:191], v[24:27]\n"
"s_waitcnt lgkmcnt(3)\n"
"v_exp_f32 v112, v112\n"
"v_mfma_f32_16x16x32_bf16 v[4:7], a[176:179], v[128:131], v[224:227]\n"
"s_waitcnt lgkmcnt(2)\n"
"ds_read_b128 a[160:163], v243 offset:8192\n"
"v_max3_f32 v252, v0, v1, v2\n"
"v_mfma_f32_16x16x32_bf16 v[4:7], a[180:183], v[132:135], v[4:7]\n"
"s_waitcnt lgkmcnt(2)\n"
"v_exp_f32 v113, v113\n"
"v_mfma_f32_16x16x32_bf16 v[4:7], a[184:187], v[136:139], v[4:7]\n"
"s_waitcnt lgkmcnt(1)\n"
"ds_read_b128 a[164:167], v244 offset:8192\n"
"v_max3_f32 v253, v8, v9, v10\n"
"v_mfma_f32_16x16x32_bf16 v[4:7], a[188:191], v[140:143], v[4:7]\n"
"v_exp_f32 v114, v114\n"
"v_mfma_f32_16x16x32_bf16 v[12:15], a[176:179], v[144:147], v[228:231]\n"
"ds_read_b128 a[168:171], v243 offset:10240\n"
"v_max3_f32 v254, v16, v17, v18\n"
"v_mfma_f32_16x16x32_bf16 v[12:15], a[180:183], v[148:151], v[12:15]\n"
"v_cvt_pk_bf16_f32 v106, v108, v109\n"
"v_cvt_pk_bf16_f32 v107, v110, v111\n"
"v_mfma_f32_16x16x32_bf16 v[12:15], a[184:187], v[152:155], v[12:15]\n"
"ds_read_b128 a[172:175], v244 offset:10240\n"
"v_max3_f32 v255, v24, v25, v26\n"
"v_mfma_f32_16x16x32_bf16 v[12:15], a[188:191], v[156:159], v[12:15]\n"
"v_exp_f32 v115, v115\n"
"v_mfma_f32_16x16x32_bf16 v[20:23], a[176:179], v[160:163], v[232:235]\n"
"v_exp_f32 v116, v116\n"
"v_mfma_f32_16x16x32_bf16 v[20:23], a[180:183], v[164:167], v[20:23]\n"
"v_exp_f32 v117, v117\n"
"v_mfma_f32_16x16x32_bf16 v[20:23], a[184:187], v[168:171], v[20:23]\n"
"v_exp_f32 v118, v118\n"
"v_mfma_f32_16x16x32_bf16 v[20:23], a[188:191], v[172:175], v[20:23]\n"
"v_exp_f32 v119, v119\n"
"v_mfma_f32_16x16x32_bf16 v[28:31], a[176:179], v[176:179], v[236:239]\n"
"v_exp_f32 v88, v88\n"
"v_mfma_f32_16x16x32_bf16 v[28:31], a[180:183], v[180:183], v[28:31]\n"
"v_exp_f32 v89, v89\n"
"v_mfma_f32_16x16x32_bf16 v[28:31], a[184:187], v[184:187], v[28:31]\n"
"v_cvt_pk_bf16_f32 v80, v80, v81\n"
"v_cvt_pk_bf16_f32 v81, v82, v83\n"
"v_mfma_f32_16x16x32_bf16 v[28:31], a[188:191], v[188:191], v[28:31]\n"
"s_waitcnt lgkmcnt(3)\n"
"v_cvt_pk_bf16_f32 v82, v84, v85\n"
"v_cvt_pk_bf16_f32 v83, v86, v87\n"
"v_mfma_f32_16x16x32_bf16 v[32:35], a[160:163], v[128:131], v[224:227]\n"
"s_waitcnt lgkmcnt(2)\n"
"ds_read_b128 a[176:179], v243 offset:12288\n"
"v_max3_f32 v252, v252, v3, v4\n"
"v_mfma_f32_16x16x32_bf16 v[32:35], a[164:167], v[132:135], v[32:35]\n"
"s_waitcnt lgkmcnt(2)\n"
"v_exp_f32 v90, v90\n"
"v_mfma_f32_16x16x32_bf16 v[32:35], a[168:171], v[136:139], v[32:35]\n"
"s_waitcnt lgkmcnt(1)\n"
"ds_read_b128 a[180:183], v244 offset:12288\n"
"v_max3_f32 v253, v253, v11, v12\n"
"v_mfma_f32_16x16x32_bf16 v[32:35], a[172:175], v[140:143], v[32:35]\n"
"v_exp_f32 v91, v91\n"
"v_mfma_f32_16x16x32_bf16 v[40:43], a[160:163], v[144:147], v[228:231]\n"
"ds_read_b128 a[184:187], v243 offset:14336\n"
"v_max3_f32 v254, v254, v19, v20\n"
"v_mfma_f32_16x16x32_bf16 v[40:43], a[164:167], v[148:151], v[40:43]\n"
"v_exp_f32 v92, v92\n"
"v_mfma_f32_16x16x32_bf16 v[40:43], a[168:171], v[152:155], v[40:43]\n"
"ds_read_b128 a[188:191], v244 offset:14336\n"
"v_max3_f32 v255, v255, v27, v28\n"
"v_mfma_f32_16x16x32_bf16 v[40:43], a[172:175], v[156:159], v[40:43]\n"
"v_exp_f32 v93, v93\n"
"v_mfma_f32_16x16x32_bf16 v[48:51], a[160:163], v[160:163], v[232:235]\n"
"v_exp_f32 v94, v94\n"
"v_mfma_f32_16x16x32_bf16 v[48:51], a[164:167], v[164:167], v[48:51]\n"
"v_cvt_pk_bf16_f32 v112, v112, v113\n"
"v_cvt_pk_bf16_f32 v113, v114, v115\n"
"v_mfma_f32_16x16x32_bf16 v[48:51], a[168:171], v[168:171], v[48:51]\n"
"v_cvt_pk_bf16_f32 v114, v116, v117\n"
"v_cvt_pk_bf16_f32 v115, v118, v119\n"
"v_mfma_f32_16x16x32_bf16 v[48:51], a[172:175], v[172:175], v[48:51]\n"
"v_exp_f32 v95, v95\n"
"v_mfma_f32_16x16x32_bf16 v[56:59], a[160:163], v[176:179], v[236:239]\n"
"v_exp_f32 v120, v120\n"
"v_mfma_f32_16x16x32_bf16 v[56:59], a[164:167], v[180:183], v[56:59]\n"
"v_exp_f32 v121, v121\n"
"v_mfma_f32_16x16x32_bf16 v[56:59], a[168:171], v[184:187], v[56:59]\n"
"v_exp_f32 v122, v122\n"
"v_mfma_f32_16x16x32_bf16 v[56:59], a[172:175], v[188:191], v[56:59]\n"
"s_waitcnt lgkmcnt(3)\n"
"v_exp_f32 v123, v123\n"
"v_mfma_f32_16x16x32_bf16 v[36:39], a[176:179], v[128:131], v[224:227]\n"
"s_waitcnt vmcnt(2) lgkmcnt(0)\n"
"s_barrier\n"
"v_exp_f32 v124, v124\n"
"v_mfma_f32_16x16x32_bf16 v[36:39], a[180:183], v[132:135], v[36:39]\n"
"s_add_u32 m0, s94, 0\n"
"v_exp_f32 v125, v125\n"
"v_mfma_f32_16x16x32_bf16 v[36:39], a[184:187], v[136:139], v[36:39]\n"
"buffer_load_dwordx4 v250, s[56:59], s90 offen lds\n"
"v_mfma_f32_16x16x32_bf16 v[36:39], a[188:191], v[140:143], v[36:39]\n"
"s_add_u32 m0, s94, 896\n"
"v_exp_f32 v126, v126\n"
"v_mfma_f32_16x16x32_bf16 v[44:47], a[176:179], v[144:147], v[228:231]\n"
"buffer_load_dwordx4 v250, s[56:59], s90 offen offset:128 lds\n"
"v_mfma_f32_16x16x32_bf16 v[44:47], a[180:183], v[148:151], v[44:47]\n"
"s_add_u32 m0, s94, 0\n"
"v_cvt_pk_bf16_f32 v88, v88, v89\n"
"v_cvt_pk_bf16_f32 v89, v90, v91\n"
"v_mfma_f32_16x16x32_bf16 v[44:47], a[184:187], v[152:155], v[44:47]\n"
"buffer_load_dwordx4 v250, s[56:59], s90 offen offset:2048 lds\n"
"v_mfma_f32_16x16x32_bf16 v[44:47], a[188:191], v[156:159], v[44:47]\n"
"s_add_u32 m0, s94, 896\n"
"v_cvt_pk_bf16_f32 v90, v92, v93\n"
"v_cvt_pk_bf16_f32 v91, v94, v95\n"
"v_mfma_f32_16x16x32_bf16 v[52:55], a[176:179], v[160:163], v[232:235]\n"
"buffer_load_dwordx4 v250, s[56:59], s90 offen offset:2176 lds\n"
"v_mfma_f32_16x16x32_bf16 v[52:55], a[180:183], v[164:167], v[52:55]\n"
"ds_read_b64_tr_b16 a[192:193], v245 offset:16384\n"
"v_max3_f32 v252, v252, v5, v6\n"
"v_mfma_f32_16x16x32_bf16 v[52:55], a[184:187], v[168:171], v[52:55]\n"
"v_exp_f32 v127, v127\n"
"v_mfma_f32_16x16x32_bf16 v[52:55], a[188:191], v[172:175], v[52:55]\n"
"ds_read_b64_tr_b16 a[194:195], v245 offset:20480\n"
"v_max3_f32 v252, v252, v7, v32\n"
"v_mfma_f32_16x16x32_bf16 v[60:63], a[176:179], v[176:179], v[236:239]\n"
"v_cvt_pk_bf16_f32 v120, v120, v121\n"
"v_cvt_pk_bf16_f32 v121, v122, v123\n"
"v_mfma_f32_16x16x32_bf16 v[60:63], a[180:183], v[180:183], v[60:63]\n"
"ds_read_b64_tr_b16 a[196:197], v245 offset:24576\n"
"v_max3_f32 v253, v253, v13, v14\n"
"v_mfma_f32_16x16x32_bf16 v[60:63], a[184:187], v[184:187], v[60:63]\n"
"s_add_u32 s90, s90, 16384\n"
"v_cvt_pk_bf16_f32 v122, v124, v125\n"
"v_cvt_pk_bf16_f32 v123, v126, v127\n"
"v_mfma_f32_16x16x32_bf16 v[60:63], a[188:191], v[188:191], v[60:63]\n"
"s_add_u32 m0, s93, 0\n"
"ds_read_b64_tr_b16 a[198:199], v245 offset:28672\n"
"v_max3_f32 v253, v253, v15, v40\n"
"v_mfma_f32_16x16x32_bf16 a[128:131], a[224:227], v[64:67], a[128:131]\n"
"buffer_load_dwordx4 v249, s[52:55], s88 offen lds\n"
"v_mfma_f32_16x16x32_bf16 a[128:131], a[224:227], v[96:99], a[128:131]\n"
"s_add_u32 m0, s93, 1920\n"
"ds_read_b64_tr_b16 a[200:201], v246 offset:16384\n"
"v_max3_f32 v254, v254, v21, v22\n"
"v_mfma_f32_16x16x32_bf16 a[132:135], a[224:227], v[72:75], a[132:135]\n"
"buffer_load_dwordx4 v249, s[52:55], s88 offen offset:128 lds\n"
"v_mfma_f32_16x16x32_bf16 a[132:135], a[224:227], v[104:107], a[132:135]\n"
"s_add_u32 s88, s88, 16384\n"
"s_add_u32 m0, s93, 4096\n"
"ds_read_b64_tr_b16 a[202:203], v246 offset:20480\n"
"v_max3_f32 v254, v254, v23, v48\n"
"v_mfma_f32_16x16x32_bf16 a[136:139], a[224:227], v[80:83], a[136:139]\n"
"buffer_load_dwordx4 v249, s[52:55], s89 offen lds\n"
"v_mfma_f32_16x16x32_bf16 a[136:139], a[224:227], v[112:115], a[136:139]\n"
"s_add_u32 m0, s93, 6016\n"
"ds_read_b64_tr_b16 a[204:205], v246 offset:24576\n"
"v_max3_f32 v255, v255, v29, v30\n"
"v_mfma_f32_16x16x32_bf16 a[140:143], a[224:227], v[88:91], a[140:143]\n"
"buffer_load_dwordx4 v249, s[52:55], s89 offen offset:128 lds\n"
"v_mfma_f32_16x16x32_bf16 a[140:143], a[224:227], v[120:123], a[140:143]\n"
"s_waitcnt lgkmcnt(5)\n"
"s_add_u32 s89, s89, 16384\n"
"ds_read_b64_tr_b16 a[206:207], v246 offset:28672\n"
"v_max3_f32 v255, v255, v31, v56\n"
"v_mfma_f32_16x16x32_bf16 a[0:3], a[192:195], v[64:67], a[0:3]\n"
"s_waitcnt lgkmcnt(4)\n"
"v_max3_f32 v252, v252, v33, v34\n"
"v_max3_f32 v252, v252, v35, v36\n"
"v_mfma_f32_16x16x32_bf16 a[0:3], a[196:199], v[96:99], a[0:3]\n"
"ds_read_b64_tr_b16 a[208:209], v245 offset:16896\n"
"buffer_load_dword v199, v218, s[16:19], s14 offen\n"
"v_max3_f32 v253, v253, v41, v42\n"
"v_mfma_f32_16x16x32_bf16 a[4:7], a[192:195], v[72:75], a[4:7]\n"
"s_add_u32 s14, s14, 16384\n"
"v_max3_f32 v253, v253, v43, v44\n"
"v_max3_f32 v254, v254, v49, v50\n"
"v_mfma_f32_16x16x32_bf16 a[4:7], a[196:199], v[104:107], a[4:7]\n"
"ds_read_b64_tr_b16 a[210:211], v245 offset:20992\n"
"v_max3_f32 v254, v254, v51, v52\n"
"v_mfma_f32_16x16x32_bf16 a[8:11], a[192:195], v[80:83], a[8:11]\n"
"v_max3_f32 v255, v255, v57, v58\n"
"v_max3_f32 v255, v255, v59, v60\n"
"v_mfma_f32_16x16x32_bf16 a[8:11], a[196:199], v[112:115], a[8:11]\n"
"ds_read_b64_tr_b16 a[212:213], v245 offset:25088\n"
"v_max3_f32 v252, v252, v37, v38\n"
"v_mfma_f32_16x16x32_bf16 a[12:15], a[192:195], v[88:91], a[12:15]\n"
"v_max_f32 v252, v252, v39\n"
"v_max3_f32 v253, v253, v45, v46\n"
"v_mfma_f32_16x16x32_bf16 a[12:15], a[196:199], v[120:123], a[12:15]\n"
"s_waitcnt lgkmcnt(5)\n"
"ds_read_b64_tr_b16 a[214:215], v245 offset:29184\n"
"v_max_f32 v253, v253, v47\n"
"v_mfma_f32_16x16x32_bf16 a[16:19], a[200:203], v[64:67], a[16:19]\n"
"s_waitcnt lgkmcnt(4)\n"
"v_max3_f32 v254, v254, v53, v54\n"
"v_max_f32 v254, v254, v55\n"
"v_mfma_f32_16x16x32_bf16 a[16:19], a[204:207], v[96:99], a[16:19]\n"
"ds_read_b64_tr_b16 a[216:217], v246 offset:16896\n"
"v_max3_f32 v255, v255, v61, v62\n"
"v_mfma_f32_16x16x32_bf16 a[20:23], a[200:203], v[72:75], a[20:23]\n"
"v_max_f32 v255, v255, v63\n"
"v_max3_f32 v196, v252, v253, v254\n"
"v_mfma_f32_16x16x32_bf16 a[20:23], a[204:207], v[104:107], a[20:23]\n"
"ds_read_b64_tr_b16 a[218:219], v246 offset:20992\n"
"v_max_f32 v196, v196, v255\n"
"v_mfma_f32_16x16x32_bf16 a[24:27], a[200:203], v[80:83], a[24:27]\n"
"v_cmp_lt_f32 vcc, s96, v196\n"
"v_mfma_f32_16x16x32_bf16 a[24:27], a[204:207], v[112:115], a[24:27]\n"
"ds_read_b64_tr_b16 a[220:221], v246 offset:25088\n"
"v_mfma_f32_16x16x32_bf16 a[28:31], a[200:203], v[88:91], a[28:31]\n"
"s_cbranch_vccnz RA2_%=\n"
"RAr2_%=:\n"
"v_exp_f32 v0, v0\n"
"v_mfma_f32_16x16x32_bf16 a[28:31], a[204:207], v[120:123], a[28:31]\n"
"s_waitcnt lgkmcnt(5)\n"
"ds_read_b64_tr_b16 a[222:223], v246 offset:29184\n"
"v_mfma_f32_16x16x32_bf16 a[32:35], a[208:211], v[64:67], a[32:35]\n"
"s_waitcnt lgkmcnt(4)\n"
"v_exp_f32 v1, v1\n"
"v_mfma_f32_16x16x32_bf16 a[32:35], a[212:215], v[96:99], a[32:35]\n"
"ds_read_b64_tr_b16 a[192:193], v245 offset:17408\n"
"v_cvt_pk_bf16_f32 v0, v0, v1\n"
"v_mfma_f32_16x16x32_bf16 a[36:39], a[208:211], v[72:75], a[36:39]\n"
"v_exp_f32 v2, v2\n"
"v_mfma_f32_16x16x32_bf16 a[36:39], a[212:215], v[104:107], a[36:39]\n"
"ds_read_b64_tr_b16 a[194:195], v245 offset:21504\n"
"v_mfma_f32_16x16x32_bf16 a[40:43], a[208:211], v[80:83], a[40:43]\n"
"v_exp_f32 v3, v3\n"
"v_mfma_f32_16x16x32_bf16 a[40:43], a[212:215], v[112:115], a[40:43]\n"
"ds_read_b64_tr_b16 a[196:197], v245 offset:25600\n"
"v_cvt_pk_bf16_f32 v1, v2, v3\n"
"v_mfma_f32_16x16x32_bf16 a[44:47], a[208:211], v[88:91], a[44:47]\n"
"v_exp_f32 v4, v4\n"
"v_mfma_f32_16x16x32_bf16 a[44:47], a[212:215], v[120:123], a[44:47]\n"
"s_waitcnt lgkmcnt(5)\n"
"ds_read_b64_tr_b16 a[198:199], v245 offset:29696\n"
"v_mfma_f32_16x16x32_bf16 a[48:51], a[216:219], v[64:67], a[48:51]\n"
"s_waitcnt lgkmcnt(4)\n"
"v_exp_f32 v5, v5\n"
"v_mfma_f32_16x16x32_bf16 a[48:51], a[220:223], v[96:99], a[48:51]\n"
"ds_read_b64_tr_b16 a[200:201], v246 offset:17408\n"
"v_cvt_pk_bf16_f32 v2, v4, v5\n"
"v_mfma_f32_16x16x32_bf16 a[52:55], a[216:219], v[72:75], a[52:55]\n"
"v_exp_f32 v6, v6\n"
"v_mfma_f32_16x16x32_bf16 a[52:55], a[220:223], v[104:107], a[52:55]\n"
"ds_read_b64_tr_b16 a[202:203], v246 offset:21504\n"
"v_mfma_f32_16x16x32_bf16 a[56:59], a[216:219], v[80:83], a[56:59]\n"
"v_exp_f32 v7, v7\n"
"v_mfma_f32_16x16x32_bf16 a[56:59], a[220:223], v[112:115], a[56:59]\n"
"ds_read_b64_tr_b16 a[204:205], v246 offset:25600\n"
"v_cvt_pk_bf16_f32 v3, v6, v7\n"
"v_mfma_f32_16x16x32_bf16 a[60:63], a[216:219], v[88:91], a[60:63]\n"
"v_exp_f32 v32, v32\n"
"v_mfma_f32_16x16x32_bf16 a[60:63], a[220:223], v[120:123], a[60:63]\n"
"s_waitcnt lgkmcnt(5)\n"
"ds_read_b64_tr_b16 a[206:207], v246 offset:29696\n"
"v_mfma_f32_16x16x32_bf16 a[64:67], a[192:195], v[64:67], a[64:67]\n"
"s_waitcnt lgkmcnt(4)\n"
"v_exp_f32 v33, v33\n"
"v_mfma_f32_16x16x32_bf16 a[64:67], a[196:199], v[96:99], a[64:67]\n"
"ds_read_b64_tr_b16 a[208:209], v245 offset:17920\n"
"v_cvt_pk_bf16_f32 v32, v32, v33\n"
"v_mfma_f32_16x16x32_bf16 a[68:71], a[192:195], v[72:75], a[68:71]\n"
"v_exp_f32 v34, v34\n"
"v_mfma_f32_16x16x32_bf16 a[68:71], a[196:199], v[104:107], a[68:71]\n"
"ds_read_b64_tr_b16 a[210:211], v245 offset:22016\n"
"v_mfma_f32_16x16x32_bf16 a[72:75], a[192:195], v[80:83], a[72:75]\n"
"v_exp_f32 v35, v35\n"
"v_mfma_f32_16x16x32_bf16 a[72:75], a[196:199], v[112:115], a[72:75]\n"
"ds_read_b64_tr_b16 a[212:213], v245 offset:26112\n"
"v_cvt_pk_bf16_f32 v33, v34, v35\n"
"v_mfma_f32_16x16x32_bf16 a[76:79], a[192:195], v[88:91], a[76:79]\n"
"v_exp_f32 v36, v36\n"
"v_mfma_f32_16x16x32_bf16 a[76:79], a[196:199], v[120:123], a[76:79]\n"
"s_waitcnt lgkmcnt(5)\n"
"ds_read_b64_tr_b16 a[214:215], v245 offset:30208\n"
"buffer_load_dword v218, v214, s[60:63], s12 offen\n"
"v_mfma_f32_16x16x32_bf16 a[80:83], a[200:203], v[64:67], a[80:83]\n"
"s_waitcnt lgkmcnt(4)\n"
"s_add_u32 s12, s12, 16384\n"
"v_exp_f32 v37, v37\n"
"v_mfma_f32_16x16x32_bf16 a[80:83], a[204:207], v[96:99], a[80:83]\n"
"ds_read_b64_tr_b16 a[216:217], v246 offset:17920\n"
"v_cvt_pk_bf16_f32 v34, v36, v37\n"
"v_mfma_f32_16x16x32_bf16 a[84:87], a[200:203], v[72:75], a[84:87]\n"
"v_exp_f32 v38, v38\n"
"v_mfma_f32_16x16x32_bf16 a[84:87], a[204:207], v[104:107], a[84:87]\n"
"ds_read_b64_tr_b16 a[218:219], v246 offset:22016\n"
"buffer_load_dword v219, v213, s[64:67], s13 offen\n"
"v_mfma_f32_16x16x32_bf16 a[88:91], a[200:203], v[80:83], a[88:91]\n"
"s_add_u32 s13, s13, 2048\n"
"v_exp_f32 v39, v39\n"
"v_mov_b32 v210, s34\n"
"v_mov_b32 v211, s35\n"
"v_lshlrev_b32 v212, 4, v251\n"
"v_mfma_f32_16x16x32_bf16 a[88:91], a[204:207], v[112:115], a[88:91]\n"
"ds_read_b64_tr_b16 a[220:221], v246 offset:26112\n"
"v_cvt_pk_bf16_f32 v35, v38, v39\n"
"v_mfma_f32_16x16x32_bf16 a[92:95], a[200:203], v[88:91], a[92:95]\n"
"s_cmp_lg_u32 s38, 0\n"
"s_cbranch_scc1 SG1_%=\n"
"s_mov_b32 exec_hi, 0\n"
"s_mov_b32 exec_lo, 0xffffffff\n"
"buffer_store_dwordx2 v[210:211], v212, s[40:43], s31 offen sc1\n"
"s_mov_b64 exec, -1\n"
"SG1_%=:\n"
"s_add_u32 s31, s31, 8192\n"
"v_exp_f32 v8, v8\n"
"v_mfma_f32_16x16x32_bf16 a[92:95], a[204:207], v[120:123], a[92:95]\n"
"s_waitcnt lgkmcnt(5)\n"
"ds_read_b64_tr_b16 a[222:223], v246 offset:30208\n"
"v_mfma_f32_16x16x32_bf16 a[96:99], a[208:211], v[64:67], a[96:99]\n"
"s_waitcnt lgkmcnt(4)\n"
"ds_read_b128 a[160:163], v243 offset:16384\n"
"v_mfma_f32_16x16x32_bf16 a[96:99], a[212:215], v[96:99], a[96:99]\n"
"v_exp_f32 v9, v9\n"
"v_mfma_f32_16x16x32_bf16 a[100:103], a[208:211], v[72:75], a[100:103]\n"
"ds_read_b128 a[164:167], v244 offset:16384\n"
"v_cvt_pk_bf16_f32 v8, v8, v9\n"
"v_mfma_f32_16x16x32_bf16 a[100:103], a[212:215], v[104:107], a[100:103]\n"
"v_exp_f32 v10, v10\n"
"v_mfma_f32_16x16x32_bf16 a[104:107], a[208:211], v[80:83], a[104:107]\n"
"ds_read_b128 a[168:171], v243 offset:18432\n"
"v_mfma_f32_16x16x32_bf16 a[104:107], a[212:215], v[112:115], a[104:107]\n"
"v_exp_f32 v11, v11\n"
"v_mfma_f32_16x16x32_bf16 a[108:111], a[208:211], v[88:91], a[108:111]\n"
"ds_read_b128 a[172:175], v244 offset:18432\n"
"v_cvt_pk_bf16_f32 v9, v10, v11\n"
"v_mfma_f32_16x16x32_bf16 a[108:111], a[212:215], v[120:123], a[108:111]\n"
"s_waitcnt lgkmcnt(6)\n"
"v_exp_f32 v12, v12\n"
"v_mfma_f32_16x16x32_bf16 a[112:115], a[216:219], v[64:67], a[112:115]\n"
"s_waitcnt lgkmcnt(4)\n"
"v_exp_f32 v13, v13\n"
"v_mfma_f32_16x16x32_bf16 a[112:115], a[220:223], v[96:99], a[112:115]\n"
"v_exp_f32 v14, v14\n"
"v_mfma_f32_16x16x32_bf16 a[116:119], a[216:219], v[72:75], a[116:119]\n"
"v_exp_f32 v15, v15\n"
"v_mfma_f32_16x16x32_bf16 a[116:119], a[220:223], v[104:107], a[116:119]\n"
"v_exp_f32 v40, v40\n"
"v_mfma_f32_16x16x32_bf16 a[120:123], a[216:219], v[80:83], a[120:123]\n"
"v_exp_f32 v41, v41\n"
"v_mfma_f32_16x16x32_bf16 a[120:123], a[220:223], v[112:115], a[120:123]\n"
"v_exp_f32 v42, v42\n"
"v_mfma_f32_16x16x32_bf16 a[124:127], a[216:219], v[88:91], a[124:127]\n"
"v_exp_f32 v43, v43\n"
"v_mfma_f32_16x16x32_bf16 a[124:127], a[220:223], v[120:123], a[124:127]\n"
"v_exp_f32 v44, v44\n"
"s_waitcnt lgkmcnt(3)\n"
"v_mfma_f32_16x16x32_bf16 v[64:67], a[160:163], v[128:131], v[224:227]\n"
"s_cmp_lg_u32 s95, 0\n"
"s_cbranch_scc1 RB2_%=\n"
"RBr2_%=:\n"
"ds_read_b128 a[176:179], v243 offset:20480\n"
"v_cvt_pk_bf16_f32 v10, v12, v13\n"
"s_waitcnt lgkmcnt(3)\n"
"v_mfma_f32_16x16x32_bf16 v[64:67], a[164:167], v[132:135], v[64:67]\n"
"s_waitcnt lgkmcnt(2)\n"
"v_exp_f32 v45, v45\n"
"v_mfma_f32_16x16x32_bf16 v[64:67], a[168:171], v[136:139], v[64:67]\n"
"s_waitcnt lgkmcnt(1)\n"
"ds_read_b128 a[180:183], v244 offset:20480\n"
"v_cvt_pk_bf16_f32 v11, v14, v15\n"
"v_mfma_f32_16x16x32_bf16 v[64:67], a[172:175], v[140:143], v[64:67]\n"
"v_exp_f32 v46, v46\n"
"v_mfma_f32_16x16x32_bf16 v[72:75], a[160:163], v[144:147], v[228:231]\n"
"ds_read_b128 a[184:187], v243 offset:22528\n"
"v_cvt_pk_bf16_f32 v40, v40, v41\n"
"v_mfma_f32_16x16x32_bf16 v[72:75], a[164:167], v[148:151], v[72:75]\n"
"v_exp_f32 v47, v47\n"
"v_mfma_f32_16x16x32_bf16 v[72:75], a[168:171], v[152:155], v[72:75]\n"
"ds_read_b128 a[188:191], v244 offset:22528\n"
"v_cvt_pk_bf16_f32 v41, v42, v43\n"
"v_mfma_f32_16x16x32_bf16 v[72:75], a[172:175], v[156:159], v[72:75]\n"
"v_exp_f32 v16, v16\n"
"v_mfma_f32_16x16x32_bf16 v[80:83], a[160:163], v[160:163], v[232:235]\n"
"v_exp_f32 v17, v17\n"
"v_mfma_f32_16x16x32_bf16 v[80:83], a[164:167], v[164:167], v[80:83]\n"
"v_exp_f32 v18, v18\n"
"v_mfma_f32_16x16x32_bf16 v[80:83], a[168:171], v[168:171], v[80:83]\n"
"v_exp_f32 v19, v19\n"
"v_mfma_f32_16x16x32_bf16 v[80:83], a[172:175], v[172:175], v[80:83]\n"
"v_exp_f32 v20, v20\n"
"v_mfma_f32_16x16x32_bf16 v[88:91], a[160:163], v[176:179], v[236:239]\n"
"v_exp_f32 v21, v21\n"
"v_mfma_f32_16x16x32_bf16 v[88:91], a[164:167], v[180:183], v[88:91]\n"
"v_exp_f32 v22, v22\n"
"v_mfma_f32_16x16x32_bf16 v[88:91], a[168:171], v[184:187], v[88:91]\n"
"v_exp_f32 v23, v23\n"
"v_mfma_f32_16x16x32_bf16 v[88:91], a[172:175], v[188:191], v[88:91]\n"
"s_waitcnt lgkmcnt(3)\n"
"v_exp_f32 v48, v48\n"
"v_mfma_f32_16x16x32_bf16 v[68:71], a[176:179], v[128:131], v[224:227]\n"
"s_waitcnt lgkmcnt(2)\n"
"ds_read_b128 a[160:163], v243 offset:24576\n"
"v_max3_f32 v252, v64, v65, v66\n"
"v_mfma_f32_16x16x32_bf16 v[68:71], a[180:183], v[132:135], v[68:71]\n"
"s_waitcnt lgkmcnt(2)\n"
"v_exp_f32 v49, v49\n"
"v_mfma_f32_16x16x32_bf16 v[68:71], a[184:187], v[136:139], v[68:71]\n"
"s_waitcnt lgkmcnt(1)\n"
"ds_read_b128 a[164:167], v244 offset:24576\n"
"v_max3_f32 v253, v72, v73, v74\n"
"v_mfma_f32_16x16x32_bf16 v[68:71], a[188:191], v[140:143], v[68:71]\n"
"v_exp_f32 v50, v50\n"
"v_mfma_f32_16x16x32_bf16 v[76:79], a[176:179], v[144:147], v[228:231]\n"
"ds_read_b128 a[168:171], v243 offset:26624\n"
"v_max3_f32 v254, v80, v81, v82\n"
"v_mfma_f32_16x16x32_bf16 v[76:79], a[180:183], v[148:151], v[76:79]\n"
"v_cvt_pk_bf16_f32 v42, v44, v45\n"
"v_cvt_pk_bf16_f32 v43, v46, v47\n"
"v_mfma_f32_16x16x32_bf16 v[76:79], a[184:187], v[152:155], v[76:79]\n"
"ds_read_b128 a[172:175], v244 offset:26624\n"
"v_max3_f32 v255, v88, v89, v90\n"
"v_mfma_f32_16x16x32_bf16 v[76:79], a[188:191], v[156:159], v[76:79]\n"
"v_exp_f32 v51, v51\n"
"v_mfma_f32_16x16x32_bf16 v[84:87], a[176:179], v[160:163], v[232:235]\n"
"v_exp_f32 v52, v52\n"
"v_mfma_f32_16x16x32_bf16 v[84:87], a[180:183], v[164:167], v[84:87]\n"
"v_exp_f32 v53, v53\n"
"v_mfma_f32_16x16x32_bf16 v[84:87], a[184:187], v[168:171], v[84:87]\n"
"v_exp_f32 v54, v54\n"
"v_mfma_f32_16x16x32_bf16 v[84:87], a[188:191], v[172:175], v[84:87]\n"
"v_exp_f32 v55, v55\n"
"v_mfma_f32_16x16x32_bf16 v[92:95], a[176:179], v[176:179], v[236:239]\n"
"v_exp_f32 v24, v24\n"
"v_mfma_f32_16x16x32_bf16 v[92:95], a[180:183], v[180:183], v[92:95]\n"
"v_exp_f32 v25, v25\n"
"v_mfma_f32_16x16x32_bf16 v[92:95], a[184:187], v[184:187], v[92:95]\n"
"v_cvt_pk_bf16_f32 v16, v16, v17\n"
"v_cvt_pk_bf16_f32 v17, v18, v19\n"
"v_mfma_f32_16x16x32_bf16 v[92:95], a[188:191], v[188:191], v[92:95]\n"
"s_waitcnt lgkmcnt(3)\n"
"v_cvt_pk_bf16_f32 v18, v20, v21\n"
"v_cvt_pk_bf16_f32 v19, v22, v23\n"
"v_mfma_f32_16x16x32_bf16 v[96:99], a[160:163], v[128:131], v[224:227]\n"
"s_waitcnt lgkmcnt(2)\n"
"ds_read_b128 a[176:179], v243 offset:28672\n"
"v_max3_f32 v252, v252, v67, v68\n"
"v_mfma_f32_16x16x32_bf16 v[96:99], a[164:167], v[132:135], v[96:99]\n"
"s_waitcnt lgkmcnt(2)\n"
"v_exp_f32 v26, v26\n"
"v_mfma_f32_16x16x32_bf16 v[96:99], a[168:171], v[136:139], v[96:99]\n"
"s_waitcnt lgkmcnt(1)\n"
"ds_read_b128 a[180:183], v244 offset:28672\n"
"v_max3_f32 v253, v253, v75, v76\n"
"v_mfma_f32_16x16x32_bf16 v[96:99], a[172:175], v[140:143], v[96:99]\n"
"v_exp_f32 v27, v27\n"
"v_mfma_f32_16x16x32_bf16 v[104:107], a[160:163], v[144:147], v[228:231]\n"
"ds_read_b128 a[184:187], v243 offset:30720\n"
"v_max3_f32 v254, v254, v83, v84\n"
"v_mfma_f32_16x16x32_bf16 v[104:107], a[164:167], v[148:151], v[104:107]\n"
"v_exp_f32 v28, v28\n"
"v_mfma_f32_16x16x32_bf16 v[104:107], a[168:171], v[152:155], v[104:107]\n"
"ds_read_b128 a[188:191], v244 offset:30720\n"
"v_max3_f32 v255, v255, v91, v92\n"
"v_mfma_f32_16x16x32_bf16 v[104:107], a[172:175], v[156:159], v[104:107]\n"
"v_exp_f32 v29, v29\n"
"v_mfma_f32_16x16x32_bf16 v[112:115], a[160:163], v[160:163], v[232:235]\n"
"v_exp_f32 v30, v30\n"
"v_mfma_f32_16x16x32_bf16 v[112:115], a[164:167], v[164:167], v[112:115]\n"
"v_cvt_pk_bf16_f32 v48, v48, v49\n"
"v_cvt_pk_bf16_f32 v49, v50, v51\n"
"v_mfma_f32_16x16x32_bf16 v[112:115], a[168:171], v[168:171], v[112:115]\n"
"v_cvt_pk_bf16_f32 v50, v52, v53\n"
"v_cvt_pk_bf16_f32 v51, v54, v55\n"
"v_mfma_f32_16x16x32_bf16 v[112:115], a[172:175], v[172:175], v[112:115]\n"
"v_exp_f32 v31, v31\n"
"v_mfma_f32_16x16x32_bf16 v[120:123], a[160:163], v[176:179], v[236:239]\n"
"v_exp_f32 v56, v56\n"
"v_mfma_f32_16x16x32_bf16 v[120:123], a[164:167], v[180:183], v[120:123]\n"
"v_exp_f32 v57, v57\n"
"v_mfma_f32_16x16x32_bf16 v[120:123], a[168:171], v[184:187], v[120:123]\n"
"v_exp_f32 v58, v58\n"
"v_mfma_f32_16x16x32_bf16 v[120:123], a[172:175], v[188:191], v[120:123]\n"
"s_waitcnt lgkmcnt(3)\n"
"v_exp_f32 v59, v59\n"
"v_mfma_f32_16x16x32_bf16 v[100:103], a[176:179], v[128:131], v[224:227]\n"
"s_waitcnt vmcnt(3) lgkmcnt(0)\n"
"s_barrier\n"
"v_exp_f32 v60, v60\n"
"v_mfma_f32_16x16x32_bf16 v[100:103], a[180:183], v[132:135], v[100:103]\n"
"s_add_u32 m0, s94, 16384\n"
"v_exp_f32 v61, v61\n"
"v_mfma_f32_16x16x32_bf16 v[100:103], a[184:187], v[136:139], v[100:103]\n"
"buffer_load_dwordx4 v250, s[56:59], s90 offen lds\n"
"v_mfma_f32_16x16x32_bf16 v[100:103], a[188:191], v[140:143], v[100:103]\n"
"s_add_u32 m0, s94, 17280\n"
"v_exp_f32 v62, v62\n"
"v_mfma_f32_16x16x32_bf16 v[108:111], a[176:179], v[144:147], v[228:231]\n"
"buffer_load_dwordx4 v250, s[56:59], s90 offen offset:128 lds\n"
"v_mfma_f32_16x16x32_bf16 v[108:111], a[180:183], v[148:151], v[108:111]\n"
"s_add_u32 m0, s94, 16384\n"
"v_cvt_pk_bf16_f32 v24, v24, v25\n"
"v_cvt_pk_bf16_f32 v25, v26, v27\n"
"v_mfma_f32_16x16x32_bf16 v[108:111], a[184:187], v[152:155], v[108:111]\n"
"buffer_load_dwordx4 v250, s[56:59], s90 offen offset:2048 lds\n"
"v_mfma_f32_16x16x32_bf16 v[108:111], a[188:191], v[156:159], v[108:111]\n"
"s_add_u32 m0, s94, 17280\n"
"v_cvt_pk_bf16_f32 v26, v28, v29\n"
"v_cvt_pk_bf16_f32 v27, v30, v31\n"
"v_mfma_f32_16x16x32_bf16 v[116:119], a[176:179], v[160:163], v[232:235]\n"
"buffer_load_dwordx4 v250, s[56:59], s90 offen offset:2176 lds\n"
"v_mfma_f32_16x16x32_bf16 v[116:119], a[180:183], v[164:167], v[116:119]\n"
"ds_read_b64_tr_b16 a[192:193], v245 offset:0\n"
"v_max3_f32 v252, v252, v69, v70\n"
"v_mfma_f32_16x16x32_bf16 v[116:119], a[184:187], v[168:171], v[116:119]\n"
"v_exp_f32 v63, v63\n"
"v_mfma_f32_16x16x32_bf16 v[116:119], a[188:191], v[172:175], v[116:119]\n"
"ds_read_b64_tr_b16 a[194:195], v245 offset:4096\n"
"v_max3_f32 v252, v252, v71, v96\n"
"v_mfma_f32_16x16x32_bf16 v[124:127], a[176:179], v[176:179], v[236:239]\n"
"v_cvt_pk_bf16_f32 v56, v56, v57\n"
"v_cvt_pk_bf16_f32 v57, v58, v59\n"
"v_mfma_f32_16x16x32_bf16 v[124:127], a[180:183], v[180:183], v[124:127]\n"
"ds_read_b64_tr_b16 a[196:197], v245 offset:8192\n"
"v_max3_f32 v253, v253, v77, v78\n"
"v_mfma_f32_16x16x32_bf16 v[124:127], a[184:187], v[184:187], v[124:127]\n"
"s_add_u32 s90, s90, 16384\n"
"v_cvt_pk_bf16_f32 v58, v60, v61\n"
"v_cvt_pk_bf16_f32 v59, v62, v63\n"
"v_mfma_f32_16x16x32_bf16 v[124:127], a[188:191], v[188:191], v[124:127]\n"
"s_add_u32 m0, s93, 16384\n"
"ds_read_b64_tr_b16 a[198:199], v245 offset:12288\n"
"v_max3_f32 v253, v253, v79, v104\n"
"v_mfma_f32_16x16x32_bf16 a[128:131], a[224:227], v[0:3], a[128:131]\n"
"buffer_load_dwordx4 v249, s[52:55], s88 offen lds\n"
"v_mfma_f32_16x16x32_bf16 a[128:131], a[224:227], v[32:35], a[128:131]\n"
"s_add_u32 m0, s93, 18304\n"
"ds_read_b64_tr_b16 a[200:201], v246 offset:0\n"
"v_max3_f32 v254, v254, v85, v86\n"
"v_mfma_f32_16x16x32_bf16 a[132:135], a[224:227], v[8:11], a[132:135]\n"
"buffer_load_dwordx4 v249, s[52:55], s88 offen offset:128 lds\n"
"v_mfma_f32_16x16x32_bf16 a[132:135], a[224:227], v[40:43], a[132:135]\n"
"s_add_u32 s88, s88, 16384\n"
"s_add_u32 m0, s93, 20480\n"
"ds_read_b64_tr_b16 a[202:203], v246 offset:4096\n"
"v_max3_f32 v254, v254, v87, v112\n"
"v_mfma_f32_16x16x32_bf16 a[136:139], a[224:227], v[16:19], a[136:139]\n"
"buffer_load_dwordx4 v249, s[52:55], s89 offen lds\n"
"v_mfma_f32_16x16x32_bf16 a[136:139], a[224:227], v[48:51], a[136:139]\n"
"s_add_u32 m0, s93, 22400\n"
"ds_read_b64_tr_b16 a[204:205], v246 offset:8192\n"
"v_max3_f32 v255, v255, v93, v94\n"
"v_mfma_f32_16x16x32_bf16 a[140:143], a[224:227], v[24:27], a[140:143]\n"
"buffer_load_dwordx4 v249, s[52:55], s89 offen offset:128 lds\n"
"v_mfma_f32_16x16x32_bf16 a[140:143], a[224:227], v[56:59], a[140:143]\n"
"s_waitcnt lgkmcnt(5)\n"
"ds_read_b64_tr_b16 a[206:207], v246 offset:12288\n"
"v_max3_f32 v255, v255, v95, v120\n"
"v_mfma_f32_16x16x32_bf16 a[0:3], a[192:195], v[0:3], a[0:3]\n"
"s_waitcnt lgkmcnt(4)\n"
"v_max3_f32 v252, v252, v97, v98\n"
"v_max3_f32 v252, v252, v99, v100\n"
"v_mfma_f32_16x16x32_bf16 a[0:3], a[196:199], v[32:35], a[0:3]\n"
"ds_read_b64_tr_b16 a[208:209], v245 offset:512\n"
"s_waitcnt vmcnt(9)\n"
"buffer_load_dword v199, v218, s[16:19], s14 offen\n"
"v_max3_f32 v253, v253, v105, v106\n"
"v_mfma_f32_16x16x32_bf16 a[4:7], a[192:195], v[8:11], a[4:7]\n"
"s_add_u32 s14, s14, 16384\n"
"v_max3_f32 v253, v253, v107, v108\n"
"v_max3_f32 v254, v254, v113, v114\n"
"v_mfma_f32_16x16x32_bf16 a[4:7], a[196:199], v[40:43], a[4:7]\n"
"ds_read_b64_tr_b16 a[210:211], v245 offset:4608\n"
"v_max3_f32 v254, v254, v115, v116\n"
"v_mfma_f32_16x16x32_bf16 a[8:11], a[192:195], v[16:19], a[8:11]\n"
"v_max3_f32 v255, v255, v121, v122\n"
"v_max3_f32 v255, v255, v123, v124\n"
"v_mfma_f32_16x16x32_bf16 a[8:11], a[196:199], v[48:51], a[8:11]\n"
"ds_read_b64_tr_b16 a[212:213], v245 offset:8704\n"
"v_max3_f32 v252, v252, v101, v102\n"
"v_mfma_f32_16x16x32_bf16 a[12:15], a[192:195], v[24:27], a[12:15]\n"
"v_max_f32 v252, v252, v103\n"
"v_max3_f32 v253, v253, v109, v110\n"
"v_mfma_f32_16x16x32_bf16 a[12:15], a[196:199], v[56:59], a[12:15]\n"
"s_waitcnt lgkmcnt(5)\n"
"ds_read_b64_tr_b16 a[214:215], v245 offset:12800\n"
"v_max_f32 v253, v253, v111\n"
"v_mfma_f32_16x16x32_bf16 a[16:19], a[200:203], v[0:3], a[16:19]\n"
"s_waitcnt lgkmcnt(4)\n"
"v_max3_f32 v254, v254, v117, v118\n"
"v_max_f32 v254, v254, v119\n"
"v_mfma_f32_16x16x32_bf16 a[16:19], a[204:207], v[32:35], a[16:19]\n"
"ds_read_b64_tr_b16 a[216:217], v246 offset:512\n"
"v_max3_f32 v255, v255, v125, v126\n"
"v_mfma_f32_16x16x32_bf16 a[20:23], a[200:203], v[8:11], a[20:23]\n"
"v_max_f32 v255, v255, v127\n"
"v_max3_f32 v196, v252, v253, v254\n"
"v_mfma_f32_16x16x32_bf16 a[20:23], a[204:207], v[40:43], a[20:23]\n"
"ds_read_b64_tr_b16 a[218:219], v246 offset:4608\n"
"v_max_f32 v196, v196, v255\n"
"v_mfma_f32_16x16x32_bf16 a[24:27], a[200:203], v[16:19], a[24:27]\n"
"v_cmp_lt_f32 vcc, s96, v196\n"
"v_mfma_f32_16x16x32_bf16 a[24:27], a[204:207], v[48:51], a[24:27]\n"
"s_add_u32 s89, s89, 16384\n"
"ds_read_b64_tr_b16 a[220:221], v246 offset:8704\n"
"v_mfma_f32_16x16x32_bf16 a[28:31], a[200:203], v[24:27], a[28:31]\n"
"s_add_u32 m0, s87, 0\n"
"s_cbranch_vccnz RA3_%=\n"
"RAr3_%=:\n"
"v_exp_f32 v64, v64\n"
"v_mfma_f32_16x16x32_bf16 a[28:31], a[204:207], v[56:59], a[28:31]\n"
"s_waitcnt lgkmcnt(5)\n"
"ds_read_b64_tr_b16 a[222:223], v246 offset:12800\n"
"buffer_load_dwordx4 v247, s[60:63], s84 offen lds\n"
"v_mfma_f32_16x16x32_bf16 a[32:35], a[208:211], v[0:3], a[32:35]\n"
"s_waitcnt lgkmcnt(4)\n"
"v_exp_f32 v65, v65\n"
"v_mfma_f32_16x16x32_bf16 a[32:35], a[212:215], v[32:35], a[32:35]\n"
"s_add_u32 s84, s84, 32768\n"
"ds_read_b64_tr_b16 a[192:193], v245 offset:1024\n"
"v_cvt_pk_bf16_f32 v64, v64, v65\n"
"v_mfma_f32_16x16x32_bf16 a[36:39], a[208:211], v[8:11], a[36:39]\n"
"s_add_u32 m0, s87, 4096\n"
"v_exp_f32 v66, v66\n"
"v_mfma_f32_16x16x32_bf16 a[36:39], a[212:215], v[40:43], a[36:39]\n"
"ds_read_b64_tr_b16 a[194:195], v245 offset:5120\n"
"buffer_load_dwordx4 v247, s[60:63], s84 offen lds\n"
"v_mfma_f32_16x16x32_bf16 a[40:43], a[208:211], v[16:19], a[40:43]\n"
"v_exp_f32 v67, v67\n"
"v_mfma_f32_16x16x32_bf16 a[40:43], a[212:215], v[48:51], a[40:43]\n"
"s_add_u32 s84, s84, 32768\n"
"ds_read_b64_tr_b16 a[196:197], v245 offset:9216\n"
"v_cvt_pk_bf16_f32 v65, v66, v67\n"
"v_mfma_f32_16x16x32_bf16 a[44:47], a[208:211], v[24:27], a[44:47]\n"
"s_add_u32 m0, s87, 8192\n"
"v_exp_f32 v68, v68\n"
"v_mfma_f32_16x16x32_bf16 a[44:47], a[212:215], v[56:59], a[44:47]\n"
"s_waitcnt lgkmcnt(5)\n"
"ds_read_b64_tr_b16 a[198:199], v245 offset:13312\n"
"buffer_load_dwordx4 v247, s[60:63], s84 offen lds\n"
"v_mfma_f32_16x16x32_bf16 a[48:51], a[216:219], v[0:3], a[48:51]\n"
"s_waitcnt lgkmcnt(4)\n"
"v_exp_f32 v69, v69\n"
"v_mfma_f32_16x16x32_bf16 a[48:51], a[220:223], v[32:35], a[48:51]\n"
"s_add_u32 s84, s84, 32768\n"
"ds_read_b64_tr_b16 a[200:201], v246 offset:1024\n"
"v_cvt_pk_bf16_f32 v66, v68, v69\n"
"v_mfma_f32_16x16x32_bf16 a[52:55], a[216:219], v[8:11], a[52:55]\n"
"s_add_u32 m0, s87, 12288\n"
"v_exp_f32 v70, v70\n"
"v_mfma_f32_16x16x32_bf16 a[52:55], a[220:223], v[40:43], a[52:55]\n"
"ds_read_b64_tr_b16 a[202:203], v246 offset:5120\n"
"buffer_load_dwordx4 v247, s[60:63], s84 offen lds\n"
"v_mfma_f32_16x16x32_bf16 a[56:59], a[216:219], v[16:19], a[56:59]\n"
"v_exp_f32 v71, v71\n"
"v_mfma_f32_16x16x32_bf16 a[56:59], a[220:223], v[48:51], a[56:59]\n"
"s_add_u32 s84, s84, 32768\n"
"ds_read_b64_tr_b16 a[204:205], v246 offset:9216\n"
"v_cvt_pk_bf16_f32 v67, v70, v71\n"
"v_mfma_f32_16x16x32_bf16 a[60:63], a[216:219], v[24:27], a[60:63]\n"
"s_add_u32 m0, s87, 16384\n"
"v_exp_f32 v96, v96\n"
"v_mfma_f32_16x16x32_bf16 a[60:63], a[220:223], v[56:59], a[60:63]\n"
"s_waitcnt lgkmcnt(5)\n"
"ds_read_b64_tr_b16 a[206:207], v246 offset:13312\n"
"buffer_load_dwordx4 v247, s[60:63], s84 offen lds\n"
"v_mfma_f32_16x16x32_bf16 a[64:67], a[192:195], v[0:3], a[64:67]\n"
"s_waitcnt lgkmcnt(4)\n"
"v_exp_f32 v97, v97\n"
"v_mfma_f32_16x16x32_bf16 a[64:67], a[196:199], v[32:35], a[64:67]\n"
"s_add_u32 s84, s84, 32768\n"
"ds_read_b64_tr_b16 a[208:209], v245 offset:1536\n"
"v_cvt_pk_bf16_f32 v96, v96, v97\n"
"v_mfma_f32_16x16x32_bf16 a[68:71], a[192:195], v[8:11], a[68:71]\n"
"s_add_u32 m0, s87, 20480\n"
"v_exp_f32 v98, v98\n"
"v_mfma_f32_16x16x32_bf16 a[68:71], a[196:199], v[40:43], a[68:71]\n"
"ds_read_b64_tr_b16 a[210:211], v245 offset:5632\n"
"buffer_load_dwordx4 v247, s[60:63], s84 offen lds\n"
"v_mfma_f32_16x16x32_bf16 a[72:75], a[192:195], v[16:19], a[72:75]\n"
"v_exp_f32 v99, v99\n"
"v_mfma_f32_16x16x32_bf16 a[72:75], a[196:199], v[48:51], a[72:75]\n"
"s_add_u32 s84, s84, 32768\n"
"ds_read_b64_tr_b16 a[212:213], v245 offset:9728\n"
"v_cvt_pk_bf16_f32 v97, v98, v99\n"
"v_mfma_f32_16x16x32_bf16 a[76:79], a[192:195], v[24:27], a[76:79]\n"
"s_add_u32 m0, s87, 24576\n"
"v_exp_f32 v100, v100\n"
"v_mfma_f32_16x16x32_bf16 a[76:79], a[196:199], v[56:59], a[76:79]\n"
"s_waitcnt lgkmcnt(5)\n"
"ds_read_b64_tr_b16 a[214:215], v245 offset:13824\n"
"buffer_load_dwordx4 v247, s[60:63], s84 offen lds\n"
"v_mfma_f32_16x16x32_bf16 a[80:83], a[200:203], v[0:3], a[80:83]\n"
"s_waitcnt lgkmcnt(4)\n"
"v_exp_f32 v101, v101\n"
"v_mfma_f32_16x16x32_bf16 a[80:83], a[204:207], v[32:35], a[80:83]\n"
"s_add_u32 s84, s84, 32768\n"
"ds_read_b64_tr_b16 a[216:217], v246 offset:1536\n"
"v_cvt_pk_bf16_f32 v98, v100, v101\n"
"v_mfma_f32_16x16x32_bf16 a[84:87], a[200:203], v[8:11], a[84:87]\n"
"s_add_u32 m0, s87, 28672\n"
"v_exp_f32 v102, v102\n"
"v_mfma_f32_16x16x32_bf16 a[84:87], a[204:207], v[40:43], a[84:87]\n"
"ds_read_b64_tr_b16 a[218:219], v246 offset:5632\n"
"buffer_load_dwordx4 v247, s[60:63], s84 offen lds\n"
"buffer_load_dword v220, v213, s[64:67], s13 offen\n"
"v_mfma_f32_16x16x32_bf16 a[88:91], a[200:203], v[16:19], a[88:91]\n"
"s_add_u32 s84, s84, 32768\n"
"s_add_u32 s13, s13, 2048\n"
"v_exp_f32 v103, v103\n"
"v_mfma_f32_16x16x32_bf16 a[88:91], a[204:207], v[48:51], a[88:91]\n"
"ds_read_b64_tr_b16 a[220:221], v246 offset:9728\n"
"v_cvt_pk_bf16_f32 v99, v102, v103\n"
"v_mfma_f32_16x16x32_bf16 a[92:95], a[200:203], v[24:27], a[92:95]\n"
"v_exp_f32 v72, v72\n"
"v_mfma_f32_16x16x32_bf16 a[92:95], a[204:207], v[56:59], a[92:95]\n"
"s_waitcnt lgkmcnt(5)\n"
"ds_read_b64_tr_b16 a[222:223], v246 offset:13824\n"
"v_mfma_f32_16x16x32_bf16 a[96:99], a[208:211], v[0:3], a[96:99]\n"
"s_waitcnt lgkmcnt(4)\n"
"ds_read_b128 a[160:163], v243 offset:0\n"
"v_mfma_f32_16x16x32_bf16 a[96:99], a[212:215], v[32:35], a[96:99]\n"
"v_exp_f32 v73, v73\n"
"v_mfma_f32_16x16x32_bf16 a[100:103], a[208:211], v[8:11], a[100:103]\n"
"ds_read_b128 a[164:167], v244 offset:0\n"
"v_cvt_pk_bf16_f32 v72, v72, v73\n"
"v_mfma_f32_16x16x32_bf16 a[100:103], a[212:215], v[40:43], a[100:103]\n"
"v_exp_f32 v74, v74\n"
"v_mfma_f32_16x16x32_bf16 a[104:107], a[208:211], v[16:19], a[104:107]\n"
"ds_read_b128 a[168:171], v243 offset:2048\n"
"v_mfma_f32_16x16x32_bf16 a[104:107], a[212:215], v[48:51], a[104:107]\n"
"v_exp_f32 v75, v75\n"
"v_mfma_f32_16x16x32_bf16 a[108:111], a[208:211], v[24:27], a[108:111]\n"
"ds_read_b128 a[172:175], v244 offset:2048\n"
"v_cvt_pk_bf16_f32 v73, v74, v75\n"
"v_mfma_f32_16x16x32_bf16 a[108:111], a[212:215], v[56:59], a[108:111]\n"
"s_waitcnt lgkmcnt(6)\n"
"v_exp_f32 v76, v76\n"
"v_mfma_f32_16x16x32_bf16 a[112:115], a[216:219], v[0:3], a[112:115]\n"
"s_waitcnt lgkmcnt(4)\n"
"v_exp_f32 v77, v77\n"
"v_mfma_f32_16x16x32_bf16 a[112:115], a[220:223], v[32:35], a[112:115]\n"
"v_exp_f32 v78, v78\n"
"v_mfma_f32_16x16x32_bf16 a[116:119], a[216:219], v[8:11], a[116:119]\n"
"v_exp_f32 v79, v79\n"
"v_mfma_f32_16x16x32_bf16 a[116:119], a[220:223], v[40:43], a[116:119]\n"
"v_exp_f32 v104, v104\n"
"v_mfma_f32_16x16x32_bf16 a[120:123], a[216:219], v[16:19], a[120:123]\n"
"v_exp_f32 v105, v105\n"
"v_mfma_f32_16x16x32_bf16 a[120:123], a[220:223], v[48:51], a[120:123]\n"
"v_exp_f32 v106, v106\n"
"v_mfma_f32_16x16x32_bf16 a[124:127], a[216:219], v[24:27], a[124:127]\n"
"v_exp_f32 v107, v107\n"
"v_mfma_f32_16x16x32_bf16 a[124:127], a[220:223], v[56:59], a[124:127]\n"
"v_exp_f32 v108, v108\n"
"s_waitcnt lgkmcnt(3)\n"
"v_mfma_f32_16x16x32_bf16 v[0:3], a[160:163], v[128:131], v[224:227]\n"
"s_cmp_lg_u32 s95, 0\n"
"s_cbranch_scc1 RB3_%=\n"
"RBr3_%=:\n"
"ds_read_b128 a[176:179], v243 offset:4096\n"
"v_cvt_pk_bf16_f32 v74, v76, v77\n"
"s_waitcnt lgkmcnt(3)\n"
"v_mfma_f32_16x16x32_bf16 v[0:3], a[164:167], v[132:135], v[0:3]\n"
"s_waitcnt lgkmcnt(2)\n"
"v_exp_f32 v109, v109\n"
"v_mfma_f32_16x16x32_bf16 v[0:3], a[168:171], v[136:139], v[0:3]\n"
"s_waitcnt lgkmcnt(1)\n"
"ds_read_b128 a[180:183], v244 offset:4096\n"
"v_cvt_pk_bf16_f32 v75, v78, v79\n"
"v_mfma_f32_16x16x32_bf16 v[0:3], a[172:175], v[140:143], v[0:3]\n"
"v_exp_f32 v110, v110\n"
"v_mfma_f32_16x16x32_bf16 v[8:11], a[160:163], v[144:147], v[228:231]\n"
"ds_read_b128 a[184:187], v243 offset:6144\n"
"v_cvt_pk_bf16_f32 v104, v104, v105\n"
"v_mfma_f32_16x16x32_bf16 v[8:11], a[164:167], v[148:151], v[8:11]\n"
"v_exp_f32 v111, v111\n"
"v_mfma_f32_16x16x32_bf16 v[8:11], a[168:171], v[152:155], v[8:11]\n"
"ds_read_b128 a[188:191], v244 offset:6144\n"
"v_cvt_pk_bf16_f32 v105, v106, v107\n"
"v_mfma_f32_16x16x32_bf16 v[8:11], a[172:175], v[156:159], v[8:11]\n"
"v_exp_f32 v80, v80\n"
"v_mfma_f32_16x16x32_bf16 v[16:19], a[160:163], v[160:163], v[232:235]\n"
"v_exp_f32 v81, v81\n"
"v_mfma_f32_16x16x32_bf16 v[16:19], a[164:167], v[164:167], v[16:19]\n"
"v_exp_f32 v82, v82\n"
"v_mfma_f32_16x16x32_bf16 v[16:19], a[168:171], v[168:171], v[16:19]\n"
"v_exp_f32 v83, v83\n"
"v_mfma_f32_16x16x32_bf16 v[16:19], a[172:175], v[172:175], v[16:19]\n"
"v_exp_f32 v84, v84\n"
"v_mfma_f32_16x16x32_bf16 v[24:27], a[160:163], v[176:179], v[236:239]\n"
"v_exp_f32 v85, v85\n"
"v_mfma_f32_16x16x32_bf16 v[24:27], a[164:167], v[180:183], v[24:27]\n"
"v_exp_f32 v86, v86\n"
"v_mfma_f32_16x16x32_bf16 v[24:27], a[168:171], v[184:187], v[24:27]\n"
"v_exp_f32 v87, v87\n"
"v_mfma_f32_16x16x32_bf16 v[24:27], a[172:175], v[188:191], v[24:27]\n"
"s_waitcnt lgkmcnt(3)\n"
"v_exp_f32 v112, v112\n"
"v_mfma_f32_16x16x32_bf16 v[4:7], a[176:179], v[128:131], v[224:227]\n"
"s_waitcnt lgkmcnt(2)\n"
"ds_read_b128 a[160:163], v243 offset:8192\n"
"v_max3_f32 v252, v0, v1, v2\n"
"v_mfma_f32_16x16x32_bf16 v[4:7], a[180:183], v[132:135], v[4:7]\n"
"s_waitcnt lgkmcnt(2)\n"
"v_exp_f32 v113, v113\n"
"v_mfma_f32_16x16x32_bf16 v[4:7], a[184:187], v[136:139], v[4:7]\n"
"s_waitcnt lgkmcnt(1)\n"
"ds_read_b128 a[164:167], v244 offset:8192\n"
"v_max3_f32 v253, v8, v9, v10\n"
"v_mfma_f32_16x16x32_bf16 v[4:7], a[188:191], v[140:143], v[4:7]\n"
"v_exp_f32 v114, v114\n"
"v_mfma_f32_16x16x32_bf16 v[12:15], a[176:179], v[144:147], v[228:231]\n"
"ds_read_b128 a[168:171], v243 offset:10240\n"
"v_max3_f32 v254, v16, v17, v18\n"
"v_mfma_f32_16x16x32_bf16 v[12:15], a[180:183], v[148:151], v[12:15]\n"
"v_cvt_pk_bf16_f32 v106, v108, v109\n"
"v_cvt_pk_bf16_f32 v107, v110, v111\n"
"v_mfma_f32_16x16x32_bf16 v[12:15], a[184:187], v[152:155], v[12:15]\n"
"ds_read_b128 a[172:175], v244 offset:10240\n"
"v_max3_f32 v255, v24, v25, v26\n"
"v_mfma_f32_16x16x32_bf16 v[12:15], a[188:191], v[156:159], v[12:15]\n"
"v_exp_f32 v115, v115\n"
"v_mfma_f32_16x16x32_bf16 v[20:23], a[176:179], v[160:163], v[232:235]\n"
"v_exp_f32 v116, v116\n"
"v_mfma_f32_16x16x32_bf16 v[20:23], a[180:183], v[164:167], v[20:23]\n"
"v_exp_f32 v117, v117\n"
"v_mfma_f32_16x16x32_bf16 v[20:23], a[184:187], v[168:171], v[20:23]\n"
"v_exp_f32 v118, v118\n"
"v_mfma_f32_16x16x32_bf16 v[20:23], a[188:191], v[172:175], v[20:23]\n"
"v_exp_f32 v119, v119\n"
"v_mfma_f32_16x16x32_bf16 v[28:31], a[176:179], v[176:179], v[236:239]\n"
"v_exp_f32 v88, v88\n"
"v_mfma_f32_16x16x32_bf16 v[28:31], a[180:183], v[180:183], v[28:31]\n"
"v_exp_f32 v89, v89\n"
"v_mfma_f32_16x16x32_bf16 v[28:31], a[184:187], v[184:187], v[28:31]\n"
"v_cvt_pk_bf16_f32 v80, v80, v81\n"
"v_cvt_pk_bf16_f32 v81, v82, v83\n"
"v_mfma_f32_16x16x32_bf16 v[28:31], a[188:191], v[188:191], v[28:31]\n"
"s_waitcnt lgkmcnt(3)\n"
"v_cvt_pk_bf16_f32 v82, v84, v85\n"
"v_cvt_pk_bf16_f32 v83, v86, v87\n"
"v_mfma_f32_16x16x32_bf16 v[32:35], a[160:163], v[128:131], v[224:227]\n"
"s_waitcnt lgkmcnt(2)\n"
"ds_read_b128 a[176:179], v243 offset:12288\n"
"v_max3_f32 v252, v252, v3, v4\n"
"v_mfma_f32_16x16x32_bf16 v[32:35], a[164:167], v[132:135], v[32:35]\n"
"s_waitcnt lgkmcnt(2)\n"
"v_exp_f32 v90, v90\n"
"v_mfma_f32_16x16x32_bf16 v[32:35], a[168:171], v[136:139], v[32:35]\n"
"s_waitcnt lgkmcnt(1)\n"
"ds_read_b128 a[180:183], v244 offset:12288\n"
"v_max3_f32 v253, v253, v11, v12\n"
"v_mfma_f32_16x16x32_bf16 v[32:35], a[172:175], v[140:143], v[32:35]\n"
"v_exp_f32 v91, v91\n"
"v_mfma_f32_16x16x32_bf16 v[40:43], a[160:163], v[144:147], v[228:231]\n"
"ds_read_b128 a[184:187], v243 offset:14336\n"
"v_max3_f32 v254, v254, v19, v20\n"
"v_mfma_f32_16x16x32_bf16 v[40:43], a[164:167], v[148:151], v[40:43]\n"
"v_exp_f32 v92, v92\n"
"v_mfma_f32_16x16x32_bf16 v[40:43], a[168:171], v[152:155], v[40:43]\n"
"ds_read_b128 a[188:191], v244 offset:14336\n"
"v_max3_f32 v255, v255, v27, v28\n"
"v_mfma_f32_16x16x32_bf16 v[40:43], a[172:175], v[156:159], v[40:43]\n"
"v_exp_f32 v93, v93\n"
"v_mfma_f32_16x16x32_bf16 v[48:51], a[160:163], v[160:163], v[232:235]\n"
"v_exp_f32 v94, v94\n"
"v_mfma_f32_16x16x32_bf16 v[48:51], a[164:167], v[164:167], v[48:51]\n"
"v_cvt_pk_bf16_f32 v112, v112, v113\n"
"v_cvt_pk_bf16_f32 v113, v114, v115\n"
"v_mfma_f32_16x16x32_bf16 v[48:51], a[168:171], v[168:171], v[48:51]\n"
"v_cvt_pk_bf16_f32 v114, v116, v117\n"
"v_cvt_pk_bf16_f32 v115, v118, v119\n"
"v_mfma_f32_16x16x32_bf16 v[48:51], a[172:175], v[172:175], v[48:51]\n"
"v_exp_f32 v95, v95\n"
"v_mfma_f32_16x16x32_bf16 v[56:59], a[160:163], v[176:179], v[236:239]\n"
"v_exp_f32 v120, v120\n"
"v_mfma_f32_16x16x32_bf16 v[56:59], a[164:167], v[180:183], v[56:59]\n"
"v_exp_f32 v121, v121\n"
"v_mfma_f32_16x16x32_bf16 v[56:59], a[168:171], v[184:187], v[56:59]\n"
"v_exp_f32 v122, v122\n"
"v_mfma_f32_16x16x32_bf16 v[56:59], a[172:175], v[188:191], v[56:59]\n"
"s_waitcnt lgkmcnt(3)\n"
"v_exp_f32 v123, v123\n"
"v_mfma_f32_16x16x32_bf16 v[36:39], a[176:179], v[128:131], v[224:227]\n"
"s_waitcnt vmcnt(10) lgkmcnt(0)\n"
"s_barrier\n"
"v_exp_f32 v124, v124\n"
"v_mfma_f32_16x16x32_bf16 v[36:39], a[180:183], v[132:135], v[36:39]\n"
"s_add_u32 m0, s94, 0\n"
"v_exp_f32 v125, v125\n"
"v_mfma_f32_16x16x32_bf16 v[36:39], a[184:187], v[136:139], v[36:39]\n"
"buffer_load_dwordx4 v250, s[56:59], s90 offen lds\n"
"v_mfma_f32_16x16x32_bf16 v[36:39], a[188:191], v[140:143], v[36:39]\n"
"s_add_u32 m0, s94, 896\n"
"v_exp_f32 v126, v126\n"
"v_mfma_f32_16x16x32_bf16 v[44:47], a[176:179], v[144:147], v[228:231]\n"
"buffer_load_dwordx4 v250, s[56:59], s90 offen offset:128 lds\n"
"v_mfma_f32_16x16x32_bf16 v[44:47], a[180:183], v[148:151], v[44:47]\n"
"s_add_u32 m0, s94, 0\n"
"v_cvt_pk_bf16_f32 v88, v88, v89\n"
"v_cvt_pk_bf16_f32 v89, v90, v91\n"
"v_mfma_f32_16x16x32_bf16 v[44:47], a[184:187], v[152:155], v[44:47]\n"
"buffer_load_dwordx4 v250, s[56:59], s90 offen offset:2048 lds\n"
"v_mfma_f32_16x16x32_bf16 v[44:47], a[188:191], v[156:159], v[44:47]\n"
"s_add_u32 m0, s94, 896\n"
"v_cvt_pk_bf16_f32 v90, v92, v93\n"
"v_cvt_pk_bf16_f32 v91, v94, v95\n"
"v_mfma_f32_16x16x32_bf16 v[52:55], a[176:179], v[160:163], v[232:235]\n"
"buffer_load_dwordx4 v250, s[56:59], s90 offen offset:2176 lds\n"
"v_mfma_f32_16x16x32_bf16 v[52:55], a[180:183], v[164:167], v[52:55]\n"
"ds_read_b64_tr_b16 a[192:193], v245 offset:16384\n"
"v_max3_f32 v252, v252, v5, v6\n"
"v_mfma_f32_16x16x32_bf16 v[52:55], a[184:187], v[168:171], v[52:55]\n"
"v_exp_f32 v127, v127\n"
"v_mfma_f32_16x16x32_bf16 v[52:55], a[188:191], v[172:175], v[52:55]\n"
"ds_read_b64_tr_b16 a[194:195], v245 offset:20480\n"
"v_max3_f32 v252, v252, v7, v32\n"
"v_mfma_f32_16x16x32_bf16 v[60:63], a[176:179], v[176:179], v[236:239]\n"
"v_cvt_pk_bf16_f32 v120, v120, v121\n"
"v_cvt_pk_bf16_f32 v121, v122, v123\n"
"v_mfma_f32_16x16x32_bf16 v[60:63], a[180:183], v[180:183], v[60:63]\n"
"ds_read_b64_tr_b16 a[196:197], v245 offset:24576\n"
"v_max3_f32 v253, v253, v13, v14\n"
"v_mfma_f32_16x16x32_bf16 v[60:63], a[184:187], v[184:187], v[60:63]\n"
"s_add_u32 s90, s90, 16384\n"
"v_cvt_pk_bf16_f32 v122, v124, v125\n"
"v_cvt_pk_bf16_f32 v123, v126, v127\n"
"v_mfma_f32_16x16x32_bf16 v[60:63], a[188:191], v[188:191], v[60:63]\n"
"s_add_u32 m0, s93, 0\n"
"ds_read_b64_tr_b16 a[198:199], v245 offset:28672\n"
"v_max3_f32 v253, v253, v15, v40\n"
"v_mfma_f32_16x16x32_bf16 a[128:131], a[224:227], v[64:67], a[128:131]\n"
"buffer_load_dwordx4 v249, s[52:55], s88 offen lds\n"
"v_mfma_f32_16x16x32_bf16 a[128:131], a[224:227], v[96:99], a[128:131]\n"
"s_add_u32 m0, s93, 1920\n"
"ds_read_b64_tr_b16 a[200:201], v246 offset:16384\n"
"v_max3_f32 v254, v254, v21, v22\n"
"v_mfma_f32_16x16x32_bf16 a[132:135], a[224:227], v[72:75], a[132:135]\n"
"buffer_load_dwordx4 v249, s[52:55], s88 offen offset:128 lds\n"
"v_mfma_f32_16x16x32_bf16 a[132:135], a[224:227], v[104:107], a[132:135]\n"
"s_add_u32 s88, s88, 16384\n"
"s_add_u32 m0, s93, 4096\n"
"ds_read_b64_tr_b16 a[202:203], v246 offset:20480\n"
"v_max3_f32 v254, v254, v23, v48\n"
"v_mfma_f32_16x16x32_bf16 a[136:139], a[224:227], v[80:83], a[136:139]\n"
"buffer_load_dwordx4 v249, s[52:55], s89 offen lds\n"
"v_mfma_f32_16x16x32_bf16 a[136:139], a[224:227], v[112:115], a[136:139]\n"
"s_add_u32 m0, s93, 6016\n"
"ds_read_b64_tr_b16 a[204:205], v246 offset:24576\n"
"v_max3_f32 v255, v255, v29, v30\n"
"v_mfma_f32_16x16x32_bf16 a[140:143], a[224:227], v[88:91], a[140:143]\n"
"buffer_load_dwordx4 v249, s[52:55], s89 offen offset:128 lds\n"
"v_mfma_f32_16x16x32_bf16 a[140:143], a[224:227], v[120:123], a[140:143]\n"
"s_waitcnt lgkmcnt(5)\n"
"ds_read_b64_tr_b16 a[206:207], v246 offset:28672\n"
"v_max3_f32 v255, v255, v31, v56\n"
"v_mfma_f32_16x16x32_bf16 a[0:3], a[192:195], v[64:67], a[0:3]\n"
"s_waitcnt lgkmcnt(4)\n"
"v_max3_f32 v252, v252, v33, v34\n"
"v_max3_f32 v252, v252, v35, v36\n"
"v_mfma_f32_16x16x32_bf16 a[0:3], a[196:199], v[96:99], a[0:3]\n"
"ds_read_b64_tr_b16 a[208:209], v245 offset:16896\n"
"buffer_load_dword v199, v218, s[16:19], s14 offen\n"
"v_max3_f32 v253, v253, v41, v42\n"
"v_mfma_f32_16x16x32_bf16 a[4:7], a[192:195], v[72:75], a[4:7]\n"
"s_add_u32 s14, s14, 16384\n"
"v_max3_f32 v253, v253, v43, v44\n"
"v_max3_f32 v254, v254, v49, v50\n"
"v_mfma_f32_16x16x32_bf16 a[4:7], a[196:199], v[104:107], a[4:7]\n"
"ds_read_b64_tr_b16 a[210:211], v245 offset:20992\n"
"v_max3_f32 v254, v254, v51, v52\n"
"v_mfma_f32_16x16x32_bf16 a[8:11], a[192:195], v[80:83], a[8:11]\n"
"v_max3_f32 v255, v255, v57, v58\n"
"v_max3_f32 v255, v255, v59, v60\n"
"v_mfma_f32_16x16x32_bf16 a[8:11], a[196:199], v[112:115], a[8:11]\n"
"ds_read_b64_tr_b16 a[212:213], v245 offset:25088\n"
"v_max3_f32 v252, v252, v37, v38\n"
"v_mfma_f32_16x16x32_bf16 a[12:15], a[192:195], v[88:91], a[12:15]\n"
"v_max_f32 v252, v252, v39\n"
"v_max3_f32 v253, v253, v45, v46\n"
"v_mfma_f32_16x16x32_bf16 a[12:15], a[196:199], v[120:123], a[12:15]\n"
"s_waitcnt lgkmcnt(5)\n"
"ds_read_b64_tr_b16 a[214:215], v245 offset:29184\n"
"v_max_f32 v253, v253, v47\n"
"v_mfma_f32_16x16x32_bf16 a[16:19], a[200:203], v[64:67], a[16:19]\n"
"s_waitcnt lgkmcnt(4)\n"
"v_max3_f32 v254, v254, v53, v54\n"
"v_max_f32 v254, v254, v55\n"
"v_mfma_f32_16x16x32_bf16 a[16:19], a[204:207], v[96:99], a[16:19]\n"
"ds_read_b64_tr_b16 a[216:217], v246 offset:16896\n"
"v_max3_f32 v255, v255, v61, v62\n"
"v_mfma_f32_16x16x32_bf16 a[20:23], a[200:203], v[72:75], a[20:23]\n"
"v_max_f32 v255, v255, v63\n"
"v_max3_f32 v196, v252, v253, v254\n"
"v_mfma_f32_16x16x32_bf16 a[20:23], a[204:207], v[104:107], a[20:23]\n"
"ds_read_b64_tr_b16 a[218:219], v246 offset:20992\n"
"v_max_f32 v196, v196, v255\n"
"v_mfma_f32_16x16x32_bf16 a[24:27], a[200:203], v[80:83], a[24:27]\n"
"v_cmp_lt_f32 vcc, s96, v196\n"
"v_mfma_f32_16x16x32_bf16 a[24:27], a[204:207], v[112:115], a[24:27]\n"
"s_add_u32 s89, s89, 16384\n"
"ds_read_b64_tr_b16 a[220:221], v246 offset:25088\n"
"v_mfma_f32_16x16x32_bf16 a[28:31], a[200:203], v[88:91], a[28:31]\n"
"s_add_u32 m0, s87, 32768\n"
"s_cbranch_vccnz RA4_%=\n"
"RAr4_%=:\n"
"v_exp_f32 v0, v0\n"
"v_mfma_f32_16x16x32_bf16 a[28:31], a[204:207], v[120:123], a[28:31]\n"
"s_waitcnt lgkmcnt(5)\n"
"ds_read_b64_tr_b16 a[222:223], v246 offset:29184\n"
"buffer_load_dwordx4 v247, s[60:63], s84 offen lds\n"
"v_mfma_f32_16x16x32_bf16 a[32:35], a[208:211], v[64:67], a[32:35]\n"
"s_waitcnt lgkmcnt(4)\n"
"v_exp_f32 v1, v1\n"
"v_mfma_f32_16x16x32_bf16 a[32:35], a[212:215], v[96:99], a[32:35]\n"
"s_add_u32 s84, s84, 32768\n"
"ds_read_b64_tr_b16 a[192:193], v245 offset:17408\n"
"v_cvt_pk_bf16_f32 v0, v0, v1\n"
"v_mfma_f32_16x16x32_bf16 a[36:39], a[208:211], v[72:75], a[36:39]\n"
"s_add_u32 m0, s87, 36864\n"
"v_exp_f32 v2, v2\n"
"v_mfma_f32_16x16x32_bf16 a[36:39], a[212:215], v[104:107], a[36:39]\n"
"ds_read_b64_tr_b16 a[194:195], v245 offset:21504\n"
"buffer_load_dwordx4 v247, s[60:63], s84 offen lds\n"
"v_mfma_f32_16x16x32_bf16 a[40:43], a[208:211], v[80:83], a[40:43]\n"
"v_exp_f32 v3, v3\n"
"v_mfma_f32_16x16x32_bf16 a[40:43], a[212:215], v[112:115], a[40:43]\n"
"s_add_u32 s84, s84, 32768\n"
"ds_read_b64_tr_b16 a[196:197], v245 offset:25600\n"
"v_cvt_pk_bf16_f32 v1, v2, v3\n"
"v_mfma_f32_16x16x32_bf16 a[44:47], a[208:211], v[88:91], a[44:47]\n"
"s_add_u32 m0, s87, 40960\n"
"v_exp_f32 v4, v4\n"
"v_mfma_f32_16x16x32_bf16 a[44:47], a[212:215], v[120:123], a[44:47]\n"
"s_waitcnt lgkmcnt(5)\n"
"ds_read_b64_tr_b16 a[198:199], v245 offset:29696\n"
"buffer_load_dwordx4 v247, s[60:63], s84 offen lds\n"
"v_mfma_f32_16x16x32_bf16 a[48:51], a[216:219], v[64:67], a[48:51]\n"
"s_waitcnt lgkmcnt(4)\n"
"v_exp_f32 v5, v5\n"
"v_mfma_f32_16x16x32_bf16 a[48:51], a[220:223], v[96:99], a[48:51]\n"
"s_add_u32 s84, s84, 32768\n"
"ds_read_b64_tr_b16 a[200:201], v246 offset:17408\n"
"v_cvt_pk_bf16_f32 v2, v4, v5\n"
"v_mfma_f32_16x16x32_bf16 a[52:55], a[216:219], v[72:75], a[52:55]\n"
"s_add_u32 m0, s87, 45056\n"
"v_exp_f32 v6, v6\n"
"v_mfma_f32_16x16x32_bf16 a[52:55], a[220:223], v[104:107], a[52:55]\n"
"ds_read_b64_tr_b16 a[202:203], v246 offset:21504\n"
"buffer_load_dwordx4 v247, s[60:63], s84 offen lds\n"
"v_mfma_f32_16x16x32_bf16 a[56:59], a[216:219], v[80:83], a[56:59]\n"
"v_exp_f32 v7, v7\n"
"v_mfma_f32_16x16x32_bf16 a[56:59], a[220:223], v[112:115], a[56:59]\n"
"s_add_u32 s84, s84, 32768\n"
"ds_read_b64_tr_b16 a[204:205], v246 offset:25600\n"
"v_cvt_pk_bf16_f32 v3, v6, v7\n"
"v_mfma_f32_16x16x32_bf16 a[60:63], a[216:219], v[88:91], a[60:63]\n"
"s_add_u32 m0, s87, 49152\n"
"v_exp_f32 v32, v32\n"
"v_mfma_f32_16x16x32_bf16 a[60:63], a[220:223], v[120:123], a[60:63]\n"
"s_waitcnt lgkmcnt(5)\n"
"ds_read_b64_tr_b16 a[206:207], v246 offset:29696\n"
"buffer_load_dwordx4 v247, s[60:63], s84 offen lds\n"
"v_mfma_f32_16x16x32_bf16 a[64:67], a[192:195], v[64:67], a[64:67]\n"
"s_waitcnt lgkmcnt(4)\n"
"v_exp_f32 v33, v33\n"
"v_mfma_f32_16x16x32_bf16 a[64:67], a[196:199], v[96:99], a[64:67]\n"
"s_add_u32 s84, s84, 32768\n"
"ds_read_b64_tr_b16 a[208:209], v245 offset:17920\n"
"v_cvt_pk_bf16_f32 v32, v32, v33\n"
"v_mfma_f32_16x16x32_bf16 a[68:71], a[192:195], v[72:75], a[68:71]\n"
"s_add_u32 m0, s87, 53248\n"
"v_exp_f32 v34, v34\n"
"v_mfma_f32_16x16x32_bf16 a[68:71], a[196:199], v[104:107], a[68:71]\n"
"ds_read_b64_tr_b16 a[210:211], v245 offset:22016\n"
"buffer_load_dwordx4 v247, s[60:63], s84 offen lds\n"
"v_mfma_f32_16x16x32_bf16 a[72:75], a[192:195], v[80:83], a[72:75]\n"
"v_exp_f32 v35, v35\n"
"v_mfma_f32_16x16x32_bf16 a[72:75], a[196:199], v[112:115], a[72:75]\n"
"s_add_u32 s84, s84, 32768\n"
"ds_read_b64_tr_b16 a[212:213], v245 offset:26112\n"
"v_cvt_pk_bf16_f32 v33, v34, v35\n"
"v_mfma_f32_16x16x32_bf16 a[76:79], a[192:195], v[88:91], a[76:79]\n"
"s_add_u32 m0, s87, 57344\n"
"v_exp_f32 v36, v36\n"
"v_mfma_f32_16x16x32_bf16 a[76:79], a[196:199], v[120:123], a[76:79]\n"
"s_waitcnt lgkmcnt(5)\n"
"ds_read_b64_tr_b16 a[214:215], v245 offset:30208\n"
"buffer_load_dwordx4 v247, s[60:63], s84 offen lds\n"
"v_mfma_f32_16x16x32_bf16 a[80:83], a[200:203], v[64:67], a[80:83]\n"
"s_waitcnt lgkmcnt(4)\n"
"v_exp_f32 v37, v37\n"
"v_mfma_f32_16x16x32_bf16 a[80:83], a[204:207], v[96:99], a[80:83]\n"
"s_add_u32 s84, s84, 32768\n"
"ds_read_b64_tr_b16 a[216:217], v246 offset:17920\n"
"v_cvt_pk_bf16_f32 v34, v36, v37\n"
"v_mfma_f32_16x16x32_bf16 a[84:87], a[200:203], v[72:75], a[84:87]\n"
"s_add_u32 m0, s87, 61440\n"
"v_exp_f32 v38, v38\n"
"v_mfma_f32_16x16x32_bf16 a[84:87], a[204:207], v[104:107], a[84:87]\n"
"ds_read_b64_tr_b16 a[218:219], v246 offset:22016\n"
"buffer_load_dwordx4 v247, s[60:63], s84 offen lds\n"
"v_mfma_f32_16x16x32_bf16 a[88:91], a[200:203], v[80:83], a[88:91]\n"
"s_add_u32 s84, s84, 32768\n"
"v_exp_f32 v39, v39\n"
"v_mfma_f32_16x16x32_bf16 a[88:91], a[204:207], v[112:115], a[88:91]\n"
"ds_read_b64_tr_b16 a[220:221], v246 offset:26112\n"
"v_cvt_pk_bf16_f32 v35, v38, v39\n"
"v_mfma_f32_16x16x32_bf16 a[92:95], a[200:203], v[88:91], a[92:95]\n"
"v_exp_f32 v8, v8\n"
"v_mfma_f32_16x16x32_bf16 a[92:95], a[204:207], v[120:123], a[92:95]\n"
"s_waitcnt lgkmcnt(5)\n"
"ds_read_b64_tr_b16 a[222:223], v246 offset:30208\n"
"v_mfma_f32_16x16x32_bf16 a[96:99], a[208:211], v[64:67], a[96:99]\n"
"s_waitcnt lgkmcnt(4)\n"
"ds_read_b128 a[160:163], v243 offset:16384\n"
"v_mfma_f32_16x16x32_bf16 a[96:99], a[212:215], v[96:99], a[96:99]\n"
"v_exp_f32 v9, v9\n"
"v_mfma_f32_16x16x32_bf16 a[100:103], a[208:211], v[72:75], a[100:103]\n"
"ds_read_b128 a[164:167], v244 offset:16384\n"
"v_cvt_pk_bf16_f32 v8, v8, v9\n"
"v_mfma_f32_16x16x32_bf16 a[100:103], a[212:215], v[104:107], a[100:103]\n"
"v_exp_f32 v10, v10\n"
"v_mfma_f32_16x16x32_bf16 a[104:107], a[208:211], v[80:83], a[104:107]\n"
"ds_read_b128 a[168:171], v243 offset:18432\n"
"v_mfma_f32_16x16x32_bf16 a[104:107], a[212:215], v[112:115], a[104:107]\n"
"v_exp_f32 v11, v11\n"
"v_mfma_f32_16x16x32_bf16 a[108:111], a[208:211], v[88:91], a[108:111]\n"
"ds_read_b128 a[172:175], v244 offset:18432\n"
"v_cvt_pk_bf16_f32 v9, v10, v11\n"
"v_mfma_f32_16x16x32_bf16 a[108:111], a[212:215], v[120:123], a[108:111]\n"
"s_waitcnt lgkmcnt(6)\n"
"v_exp_f32 v12, v12\n"
"v_mfma_f32_16x16x32_bf16 a[112:115], a[216:219], v[64:67], a[112:115]\n"
"s_waitcnt lgkmcnt(4)\n"
"v_exp_f32 v13, v13\n"
"v_mfma_f32_16x16x32_bf16 a[112:115], a[220:223], v[96:99], a[112:115]\n"
"v_exp_f32 v14, v14\n"
"v_mfma_f32_16x16x32_bf16 a[116:119], a[216:219], v[72:75], a[116:119]\n"
"buffer_load_dword v221, v213, s[64:67], s13 offen\n"
"v_exp_f32 v15, v15\n"
"v_mfma_f32_16x16x32_bf16 a[116:119], a[220:223], v[104:107], a[116:119]\n"
"s_add_u32 s13, s13, 2048\n"
"v_exp_f32 v40, v40\n"
"v_mfma_f32_16x16x32_bf16 a[120:123], a[216:219], v[80:83], a[120:123]\n"
"buffer_load_dwordx2 v[210:211], v207, s[40:43], s30 offen sc1\n"
"v_exp_f32 v41, v41\n"
"v_mfma_f32_16x16x32_bf16 a[120:123], a[220:223], v[112:115], a[120:123]\n"
"v_exp_f32 v42, v42\n"
"v_mfma_f32_16x16x32_bf16 a[124:127], a[216:219], v[88:91], a[124:127]\n"
"v_exp_f32 v43, v43\n"
"v_mfma_f32_16x16x32_bf16 a[124:127], a[220:223], v[120:123], a[124:127]\n"
"v_exp_f32 v44, v44\n"
"s_waitcnt lgkmcnt(3)\n"
"v_mfma_f32_16x16x32_bf16 v[64:67], a[160:163], v[128:131], v[224:227]\n"
"s_cmp_lg_u32 s95, 0\n"
"s_cbranch_scc1 RB4_%=\n"
"RBr4_%=:\n"
"ds_read_b128 a[176:179], v243 offset:20480\n"
"v_cvt_pk_bf16_f32 v10, v12, v13\n"
"s_waitcnt lgkmcnt(3)\n"
"v_mfma_f32_16x16x32_bf16 v[64:67], a[164:167], v[132:135], v[64:67]\n"
"s_waitcnt lgkmcnt(2)\n"
"v_exp_f32 v45, v45\n"
"v_mfma_f32_16x16x32_bf16 v[64:67], a[168:171], v[136:139], v[64:67]\n"
"s_waitcnt lgkmcnt(1)\n"
"ds_read_b128 a[180:183], v244 offset:20480\n"
"v_cvt_pk_bf16_f32 v11, v14, v15\n"
"v_mfma_f32_16x16x32_bf16 v[64:67], a[172:175], v[140:143], v[64:67]\n"
"v_exp_f32 v46, v46\n"
"v_mfma_f32_16x16x32_bf16 v[72:75], a[160:163], v[144:147], v[228:231]\n"
"ds_read_b128 a[184:187], v243 offset:22528\n"
"v_cvt_pk_bf16_f32 v40, v40, v41\n"
"v_mfma_f32_16x16x32_bf16 v[72:75], a[164:167], v[148:151], v[72:75]\n"
"v_exp_f32 v47, v47\n"
"v_mfma_f32_16x16x32_bf16 v[72:75], a[168:171], v[152:155], v[72:75]\n"
"ds_read_b128 a[188:191], v244 offset:22528\n"
"v_cvt_pk_bf16_f32 v41, v42, v43\n"
"v_mfma_f32_16x16x32_bf16 v[72:75], a[172:175], v[156:159], v[72:75]\n"
"v_exp_f32 v16, v16\n"
"v_mfma_f32_16x16x32_bf16 v[80:83], a[160:163], v[160:163], v[232:235]\n"
"v_exp_f32 v17, v17\n"
"v_mfma_f32_16x16x32_bf16 v[80:83], a[164:167], v[164:167], v[80:83]\n"
"v_exp_f32 v18, v18\n"
"v_mfma_f32_16x16x32_bf16 v[80:83], a[168:171], v[168:171], v[80:83]\n"
"v_exp_f32 v19, v19\n"
"v_mfma_f32_16x16x32_bf16 v[80:83], a[172:175], v[172:175], v[80:83]\n"
"v_exp_f32 v20, v20\n"
"v_mfma_f32_16x16x32_bf16 v[88:91], a[160:163], v[176:179], v[236:239]\n"
"v_exp_f32 v21, v21\n"
"v_mfma_f32_16x16x32_bf16 v[88:91], a[164:167], v[180:183], v[88:91]\n"
"v_exp_f32 v22, v22\n"
"v_mfma_f32_16x16x32_bf16 v[88:91], a[168:171], v[184:187], v[88:91]\n"
"v_exp_f32 v23, v23\n"
"v_mfma_f32_16x16x32_bf16 v[88:91], a[172:175], v[188:191], v[88:91]\n"
"s_waitcnt lgkmcnt(3)\n"
"v_exp_f32 v48, v48\n"
"v_mfma_f32_16x16x32_bf16 v[68:71], a[176:179], v[128:131], v[224:227]\n"
"s_waitcnt lgkmcnt(2)\n"
"ds_read_b128 a[160:163], v243 offset:24576\n"
"v_max3_f32 v252, v64, v65, v66\n"
"v_mfma_f32_16x16x32_bf16 v[68:71], a[180:183], v[132:135], v[68:71]\n"
"s_waitcnt lgkmcnt(2)\n"
"v_exp_f32 v49, v49\n"
"v_mfma_f32_16x16x32_bf16 v[68:71], a[184:187], v[136:139], v[68:71]\n"
"s_waitcnt lgkmcnt(1)\n"
"ds_read_b128 a[164:167], v244 offset:24576\n"
"v_max3_f32 v253, v72, v73, v74\n"
"v_mfma_f32_16x16x32_bf16 v[68:71], a[188:191], v[140:143], v[68:71]\n"
"v_exp_f32 v50, v50\n"
"v_mfma_f32_16x16x32_bf16 v[76:79], a[176:179], v[144:147], v[228:231]\n"
"ds_read_b128 a[168:171], v243 offset:26624\n"
"v_max3_f32 v254, v80, v81, v82\n"
"v_mfma_f32_16x16x32_bf16 v[76:79], a[180:183], v[148:151], v[76:79]\n"
"v_cvt_pk_bf16_f32 v42, v44, v45\n"
"v_cvt_pk_bf16_f32 v43, v46, v47\n"
"v_mfma_f32_16x16x32_bf16 v[76:79], a[184:187], v[152:155], v[76:79]\n"
"ds_read_b128 a[172:175], v244 offset:26624\n"
"v_max3_f32 v255, v88, v89, v90\n"
"v_mfma_f32_16x16x32_bf16 v[76:79], a[188:191], v[156:159], v[76:79]\n"
"v_exp_f32 v51, v51\n"
"v_mfma_f32_16x16x32_bf16 v[84:87], a[176:179], v[160:163], v[232:235]\n"
"v_exp_f32 v52, v52\n"
"v_mfma_f32_16x16x32_bf16 v[84:87], a[180:183], v[164:167], v[84:87]\n"
"v_exp_f32 v53, v53\n"
"v_mfma_f32_16x16x32_bf16 v[84:87], a[184:187], v[168:171], v[84:87]\n"
"v_exp_f32 v54, v54\n"
"v_mfma_f32_16x16x32_bf16 v[84:87], a[188:191], v[172:175], v[84:87]\n"
"v_exp_f32 v55, v55\n"
"v_mfma_f32_16x16x32_bf16 v[92:95], a[176:179], v[176:179], v[236:239]\n"
"v_exp_f32 v24, v24\n"
"v_mfma_f32_16x16x32_bf16 v[92:95], a[180:183], v[180:183], v[92:95]\n"
"v_exp_f32 v25, v25\n"
"v_mfma_f32_16x16x32_bf16 v[92:95], a[184:187], v[184:187], v[92:95]\n"
"v_cvt_pk_bf16_f32 v16, v16, v17\n"
"v_cvt_pk_bf16_f32 v17, v18, v19\n"
"v_mfma_f32_16x16x32_bf16 v[92:95], a[188:191], v[188:191], v[92:95]\n"
"s_waitcnt lgkmcnt(3)\n"
"v_cvt_pk_bf16_f32 v18, v20, v21\n"
"v_cvt_pk_bf16_f32 v19, v22, v23\n"
"v_mfma_f32_16x16x32_bf16 v[96:99], a[160:163], v[128:131], v[224:227]\n"
"s_waitcnt lgkmcnt(2)\n"
"ds_read_b128 a[176:179], v243 offset:28672\n"
"v_max3_f32 v252, v252, v67, v68\n"
"v_mfma_f32_16x16x32_bf16 v[96:99], a[164:167], v[132:135], v[96:99]\n"
"s_waitcnt lgkmcnt(2)\n"
"v_exp_f32 v26, v26\n"
"v_mfma_f32_16x16x32_bf16 v[96:99], a[168:171], v[136:139], v[96:99]\n"
"s_waitcnt lgkmcnt(1)\n"
"ds_read_b128 a[180:183], v244 offset:28672\n"
"v_max3_f32 v253, v253, v75, v76\n"
"v_mfma_f32_16x16x32_bf16 v[96:99], a[172:175], v[140:143], v[96:99]\n"
"v_exp_f32 v27, v27\n"
"v_mfma_f32_16x16x32_bf16 v[104:107], a[160:163], v[144:147], v[228:231]\n"
"ds_read_b128 a[184:187], v243 offset:30720\n"
"v_max3_f32 v254, v254, v83, v84\n"
"v_mfma_f32_16x16x32_bf16 v[104:107], a[164:167], v[148:151], v[104:107]\n"
"v_exp_f32 v28, v28\n"
"v_mfma_f32_16x16x32_bf16 v[104:107], a[168:171], v[152:155], v[104:107]\n"
"ds_read_b128 a[188:191], v244 offset:30720\n"
"v_max3_f32 v255, v255, v91, v92\n"
"v_mfma_f32_16x16x32_bf16 v[104:107], a[172:175], v[156:159], v[104:107]\n"
"v_exp_f32 v29, v29\n"
"v_mfma_f32_16x16x32_bf16 v[112:115], a[160:163], v[160:163], v[232:235]\n"
"v_exp_f32 v30, v30\n"
"v_mfma_f32_16x16x32_bf16 v[112:115], a[164:167], v[164:167], v[112:115]\n"
"v_cvt_pk_bf16_f32 v48, v48, v49\n"
"v_cvt_pk_bf16_f32 v49, v50, v51\n"
"v_mfma_f32_16x16x32_bf16 v[112:115], a[168:171], v[168:171], v[112:115]\n"
"v_cvt_pk_bf16_f32 v50, v52, v53\n"
"v_cvt_pk_bf16_f32 v51, v54, v55\n"
"v_mfma_f32_16x16x32_bf16 v[112:115], a[172:175], v[172:175], v[112:115]\n"
"v_exp_f32 v31, v31\n"
"v_mfma_f32_16x16x32_bf16 v[120:123], a[160:163], v[176:179], v[236:239]\n"
"v_exp_f32 v56, v56\n"
"v_mfma_f32_16x16x32_bf16 v[120:123], a[164:167], v[180:183], v[120:123]\n"
"v_exp_f32 v57, v57\n"
"v_mfma_f32_16x16x32_bf16 v[120:123], a[168:171], v[184:187], v[120:123]\n"
"v_exp_f32 v58, v58\n"
"v_mfma_f32_16x16x32_bf16 v[120:123], a[172:175], v[188:191], v[120:123]\n"
"s_waitcnt lgkmcnt(3)\n"
"v_exp_f32 v59, v59\n"
"v_mfma_f32_16x16x32_bf16 v[100:103], a[176:179], v[128:131], v[224:227]\n"
"s_waitcnt vmcnt(11) lgkmcnt(0)\n"
"s_barrier\n"
"v_exp_f32 v60, v60\n"
"v_mfma_f32_16x16x32_bf16 v[100:103], a[180:183], v[132:135], v[100:103]\n"
"s_add_u32 m0, s94, 16384\n"
"v_exp_f32 v61, v61\n"
"v_mfma_f32_16x16x32_bf16 v[100:103], a[184:187], v[136:139], v[100:103]\n"
"buffer_load_dwordx4 v250, s[56:59], s90 offen lds\n"
"v_mfma_f32_16x16x32_bf16 v[100:103], a[188:191], v[140:143], v[100:103]\n"
"s_add_u32 m0, s94, 17280\n"
"v_exp_f32 v62, v62\n"
"v_mfma_f32_16x16x32_bf16 v[108:111], a[176:179], v[144:147], v[228:231]\n"
"buffer_load_dwordx4 v250, s[56:59], s90 offen offset:128 lds\n"
"v_mfma_f32_16x16x32_bf16 v[108:111], a[180:183], v[148:151], v[108:111]\n"
"s_add_u32 m0, s94, 16384\n"
"v_cvt_pk_bf16_f32 v24, v24, v25\n"
"v_cvt_pk_bf16_f32 v25, v26, v27\n"
"v_mfma_f32_16x16x32_bf16 v[108:111], a[184:187], v[152:155], v[108:111]\n"
"buffer_load_dwordx4 v250, s[56:59], s90 offen offset:2048 lds\n"
"v_mfma_f32_16x16x32_bf16 v[108:111], a[188:191], v[156:159], v[108:111]\n"
"s_add_u32 m0, s94, 17280\n"
"v_cvt_pk_bf16_f32 v26, v28, v29\n"
"v_cvt_pk_bf16_f32 v27, v30, v31\n"
"v_mfma_f32_16x16x32_bf16 v[116:119], a[176:179], v[160:163], v[232:235]\n"
"buffer_load_dwordx4 v250, s[56:59], s90 offen offset:2176 lds\n"
"v_mfma_f32_16x16x32_bf16 v[116:119], a[180:183], v[164:167], v[116:119]\n"
"ds_read_b64_tr_b16 a[192:193], v245 offset:0\n"
"v_max3_f32 v252, v252, v69, v70\n"
"v_mfma_f32_16x16x32_bf16 v[116:119], a[184:187], v[168:171], v[116:119]\n"
"v_exp_f32 v63, v63\n"
"v_mfma_f32_16x16x32_bf16 v[116:119], a[188:191], v[172:175], v[116:119]\n"
"ds_read_b64_tr_b16 a[194:195], v245 offset:4096\n"
"v_max3_f32 v252, v252, v71, v96\n"
"v_mfma_f32_16x16x32_bf16 v[124:127], a[176:179], v[176:179], v[236:239]\n"
"v_cvt_pk_bf16_f32 v56, v56, v57\n"
"v_cvt_pk_bf16_f32 v57, v58, v59\n"
"v_mfma_f32_16x16x32_bf16 v[124:127], a[180:183], v[180:183], v[124:127]\n"
"ds_read_b64_tr_b16 a[196:197], v245 offset:8192\n"
"v_max3_f32 v253, v253, v77, v78\n"
"v_mfma_f32_16x16x32_bf16 v[124:127], a[184:187], v[184:187], v[124:127]\n"
"s_add_u32 s90, s90, 16384\n"
"v_cvt_pk_bf16_f32 v58, v60, v61\n"
"v_cvt_pk_bf16_f32 v59, v62, v63\n"
"v_mfma_f32_16x16x32_bf16 v[124:127], a[188:191], v[188:191], v[124:127]\n"
"s_add_u32 m0, s93, 16384\n"
"ds_read_b64_tr_b16 a[198:199], v245 offset:12288\n"
"v_max3_f32 v253, v253, v79, v104\n"
"v_mfma_f32_16x16x32_bf16 a[128:131], a[224:227], v[0:3], a[128:131]\n"
"buffer_load_dwordx4 v249, s[52:55], s88 offen lds\n"
"v_mfma_f32_16x16x32_bf16 a[128:131], a[224:227], v[32:35], a[128:131]\n"
"s_add_u32 m0, s93, 18304\n"
"ds_read_b64_tr_b16 a[200:201], v246 offset:0\n"
"v_max3_f32 v254, v254, v85, v86\n"
"v_mfma_f32_16x16x32_bf16 a[132:135], a[224:227], v[8:11], a[132:135]\n"
"buffer_load_dwordx4 v249, s[52:55], s88 offen offset:128 lds\n"
"v_mfma_f32_16x16x32_bf16 a[132:135], a[224:227], v[40:43], a[132:135]\n"
"s_add_u32 s88, s88, 16384\n"
"s_add_u32 m0, s93, 20480\n"
"ds_read_b64_tr_b16 a[202:203], v246 offset:4096\n"
"v_max3_f32 v254, v254, v87, v112\n"
"v_mfma_f32_16x16x32_bf16 a[136:139], a[224:227], v[16:19], a[136:139]\n"
"buffer_load_dwordx4 v249, s[52:55], s89 offen lds\n"
"v_mfma_f32_16x16x32_bf16 a[136:139], a[224:227], v[48:51], a[136:139]\n"
"s_add_u32 m0, s93, 22400\n"
"ds_read_b64_tr_b16 a[204:205], v246 offset:8192\n"
"v_max3_f32 v255, v255, v93, v94\n"
"v_mfma_f32_16x16x32_bf16 a[140:143], a[224:227], v[24:27], a[140:143]\n"
"buffer_load_dwordx4 v249, s[52:55], s89 offen offset:128 lds\n"
"v_mfma_f32_16x16x32_bf16 a[140:143], a[224:227], v[56:59], a[140:143]\n"
"s_waitcnt lgkmcnt(5)\n"
"ds_read_b64_tr_b16 a[206:207], v246 offset:12288\n"
"v_max3_f32 v255, v255, v95, v120\n"
"v_mfma_f32_16x16x32_bf16 a[0:3], a[192:195], v[0:3], a[0:3]\n"
"s_waitcnt lgkmcnt(4)\n"
"v_max3_f32 v252, v252, v97, v98\n"
"v_max3_f32 v252, v252, v99, v100\n"
"v_mfma_f32_16x16x32_bf16 a[0:3], a[196:199], v[32:35], a[0:3]\n"
"ds_read_b64_tr_b16 a[208:209], v245 offset:512\n"
"buffer_load_dword v199, v218, s[16:19], s14 offen\n"
"v_max3_f32 v253, v253, v105, v106\n"
"v_mfma_f32_16x16x32_bf16 a[4:7], a[192:195], v[8:11], a[4:7]\n"
"s_add_u32 s14, s14, 16384\n"
"v_max3_f32 v253, v253, v107, v108\n"
"v_max3_f32 v254, v254, v113, v114\n"
"v_mfma_f32_16x16x32_bf16 a[4:7], a[196:199], v[40:43], a[4:7]\n"
"ds_read_b64_tr_b16 a[210:211], v245 offset:4608\n"
"v_max3_f32 v254, v254, v115, v116\n"
"v_mfma_f32_16x16x32_bf16 a[8:11], a[192:195], v[16:19], a[8:11]\n"
"v_max3_f32 v255, v255, v121, v122\n"
"v_max3_f32 v255, v255, v123, v124\n"
"v_mfma_f32_16x16x32_bf16 a[8:11], a[196:199], v[48:51], a[8:11]\n"
"ds_read_b64_tr_b16 a[212:213], v245 offset:8704\n"
"v_max3_f32 v252, v252, v101, v102\n"
"v_mfma_f32_16x16x32_bf16 a[12:15], a[192:195], v[24:27], a[12:15]\n"
"v_max_f32 v252, v252, v103\n"
"v_max3_f32 v253, v253, v109, v110\n"
"v_mfma_f32_16x16x32_bf16 a[12:15], a[196:199], v[56:59], a[12:15]\n"
"s_waitcnt lgkmcnt(5)\n"
"ds_read_b64_tr_b16 a[214:215], v245 offset:12800\n"
"v_max_f32 v253, v253, v111\n"
"v_mfma_f32_16x16x32_bf16 a[16:19], a[200:203], v[0:3], a[16:19]\n"
"s_waitcnt lgkmcnt(4)\n"
"v_max3_f32 v254, v254, v117, v118\n"
"v_max_f32 v254, v254, v119\n"
"v_mfma_f32_16x16x32_bf16 a[16:19], a[204:207], v[32:35], a[16:19]\n"
"ds_read_b64_tr_b16 a[216:217], v246 offset:512\n"
"v_max3_f32 v255, v255, v125, v126\n"
"v_mfma_f32_16x16x32_bf16 a[20:23], a[200:203], v[8:11], a[20:23]\n"
"v_max_f32 v255, v255, v127\n"
"v_max3_f32 v196, v252, v253, v254\n"
"v_mfma_f32_16x16x32_bf16 a[20:23], a[204:207], v[40:43], a[20:23]\n"
"ds_read_b64_tr_b16 a[218:219], v246 offset:4608\n"
"v_max_f32 v196, v196, v255\n"
"v_mfma_f32_16x16x32_bf16 a[24:27], a[200:203], v[16:19], a[24:27]\n"
"v_cmp_lt_f32 vcc, s96, v196\n"
"v_mfma_f32_16x16x32_bf16 a[24:27], a[204:207], v[48:51], a[24:27]\n"
"s_add_u32 s89, s89, 16384\n"
"ds_read_b64_tr_b16 a[220:221], v246 offset:8704\n"
"v_mfma_f32_16x16x32_bf16 a[28:31], a[200:203], v[24:27], a[28:31]\n"
"s_add_u32 m0, s87, 65536\n"
"s_cbranch_vccnz RA5_%=\n"
"RAr5_%=:\n"
"v_exp_f32 v64, v64\n"
"v_mfma_f32_16x16x32_bf16 a[28:31], a[204:207], v[56:59], a[28:31]\n"
"s_waitcnt lgkmcnt(5)\n"
"ds_read_b64_tr_b16 a[222:223], v246 offset:12800\n"
"buffer_load_dwordx4 v247, s[60:63], s84 offen lds\n"
"v_mfma_f32_16x16x32_bf16 a[32:35], a[208:211], v[0:3], a[32:35]\n"
"s_waitcnt lgkmcnt(4)\n"
"v_exp_f32 v65, v65\n"
"v_mfma_f32_16x16x32_bf16 a[32:35], a[212:215], v[32:35], a[32:35]\n"
"s_add_u32 s84, s84, 32768\n"
"ds_read_b64_tr_b16 a[192:193], v245 offset:1024\n"
"v_cvt_pk_bf16_f32 v64, v64, v65\n"
"v_mfma_f32_16x16x32_bf16 a[36:39], a[208:211], v[8:11], a[36:39]\n"
"s_add_u32 m0, s87, 69632\n"
"v_exp_f32 v66, v66\n"
"v_mfma_f32_16x16x32_bf16 a[36:39], a[212:215], v[40:43], a[36:39]\n"
"ds_read_b64_tr_b16 a[194:195], v245 offset:5120\n"
"buffer_load_dwordx4 v247, s[60:63], s84 offen lds\n"
"v_mfma_f32_16x16x32_bf16 a[40:43], a[208:211], v[16:19], a[40:43]\n"
"v_exp_f32 v67, v67\n"
"v_mfma_f32_16x16x32_bf16 a[40:43], a[212:215], v[48:51], a[40:43]\n"
"s_add_u32 s84, s84, 32768\n"
"ds_read_b64_tr_b16 a[196:197], v245 offset:9216\n"
"v_cvt_pk_bf16_f32 v65, v66, v67\n"
"v_mfma_f32_16x16x32_bf16 a[44:47], a[208:211], v[24:27], a[44:47]\n"
"s_add_u32 m0, s87, 73728\n"
"v_exp_f32 v68, v68\n"
"v_mfma_f32_16x16x32_bf16 a[44:47], a[212:215], v[56:59], a[44:47]\n"
"s_waitcnt lgkmcnt(5)\n"
"ds_read_b64_tr_b16 a[198:199], v245 offset:13312\n"
"buffer_load_dwordx4 v247, s[60:63], s84 offen lds\n"
"v_mfma_f32_16x16x32_bf16 a[48:51], a[216:219], v[0:3], a[48:51]\n"
"s_waitcnt lgkmcnt(4)\n"
"v_exp_f32 v69, v69\n"
"v_mfma_f32_16x16x32_bf16 a[48:51], a[220:223], v[32:35], a[48:51]\n"
"s_add_u32 s84, s84, 32768\n"
"ds_read_b64_tr_b16 a[200:201], v246 offset:1024\n"
"v_cvt_pk_bf16_f32 v66, v68, v69\n"
"v_mfma_f32_16x16x32_bf16 a[52:55], a[216:219], v[8:11], a[52:55]\n"
"s_add_u32 m0, s87, 77824\n"
"v_exp_f32 v70, v70\n"
"v_mfma_f32_16x16x32_bf16 a[52:55], a[220:223], v[40:43], a[52:55]\n"
"ds_read_b64_tr_b16 a[202:203], v246 offset:5120\n"
"buffer_load_dwordx4 v247, s[60:63], s84 offen lds\n"
"v_mfma_f32_16x16x32_bf16 a[56:59], a[216:219], v[16:19], a[56:59]\n"
"v_exp_f32 v71, v71\n"
"v_mfma_f32_16x16x32_bf16 a[56:59], a[220:223], v[48:51], a[56:59]\n"
"s_add_u32 s84, s84, 32768\n"
"ds_read_b64_tr_b16 a[204:205], v246 offset:9216\n"
"v_cvt_pk_bf16_f32 v67, v70, v71\n"
"v_mfma_f32_16x16x32_bf16 a[60:63], a[216:219], v[24:27], a[60:63]\n"
"s_add_u32 m0, s87, 81920\n"
"v_exp_f32 v96, v96\n"
"v_mfma_f32_16x16x32_bf16 a[60:63], a[220:223], v[56:59], a[60:63]\n"
"s_waitcnt lgkmcnt(5)\n"
"ds_read_b64_tr_b16 a[206:207], v246 offset:13312\n"
"buffer_load_dwordx4 v247, s[60:63], s84 offen lds\n"
"v_mfma_f32_16x16x32_bf16 a[64:67], a[192:195], v[0:3], a[64:67]\n"
"s_waitcnt lgkmcnt(4)\n"
"v_exp_f32 v97, v97\n"
"v_mfma_f32_16x16x32_bf16 a[64:67], a[196:199], v[32:35], a[64:67]\n"
"s_add_u32 s84, s84, 32768\n"
"ds_read_b64_tr_b16 a[208:209], v245 offset:1536\n"
"v_cvt_pk_bf16_f32 v96, v96, v97\n"
"v_mfma_f32_16x16x32_bf16 a[68:71], a[192:195], v[8:11], a[68:71]\n"
"s_add_u32 m0, s87, 86016\n"
"v_exp_f32 v98, v98\n"
"v_mfma_f32_16x16x32_bf16 a[68:71], a[196:199], v[40:43], a[68:71]\n"
"ds_read_b64_tr_b16 a[210:211], v245 offset:5632\n"
"buffer_load_dwordx4 v247, s[60:63], s84 offen lds\n"
"v_mfma_f32_16x16x32_bf16 a[72:75], a[192:195], v[16:19], a[72:75]\n"
"v_exp_f32 v99, v99\n"
"v_mfma_f32_16x16x32_bf16 a[72:75], a[196:199], v[48:51], a[72:75]\n"
"s_add_u32 s84, s84, 32768\n"
"ds_read_b64_tr_b16 a[212:213], v245 offset:9728\n"
"v_cvt_pk_bf16_f32 v97, v98, v99\n"
"v_mfma_f32_16x16x32_bf16 a[76:79], a[192:195], v[24:27], a[76:79]\n"
"s_add_u32 m0, s87, 90112\n"
"v_exp_f32 v100, v100\n"
"v_mfma_f32_16x16x32_bf16 a[76:79], a[196:199], v[56:59], a[76:79]\n"
"s_waitcnt lgkmcnt(5)\n"
"ds_read_b64_tr_b16 a[214:215], v245 offset:13824\n"
"buffer_load_dwordx4 v247, s[60:63], s84 offen lds\n"
"v_mfma_f32_16x16x32_bf16 a[80:83], a[200:203], v[0:3], a[80:83]\n"
"s_waitcnt lgkmcnt(4)\n"
"v_exp_f32 v101, v101\n"
"v_mfma_f32_16x16x32_bf16 a[80:83], a[204:207], v[32:35], a[80:83]\n"
"s_add_u32 s84, s84, 32768\n"
"ds_read_b64_tr_b16 a[216:217], v246 offset:1536\n"
"v_cvt_pk_bf16_f32 v98, v100, v101\n"
"v_mfma_f32_16x16x32_bf16 a[84:87], a[200:203], v[8:11], a[84:87]\n"
"s_add_u32 m0, s87, 94208\n"
"v_exp_f32 v102, v102\n"
"v_mfma_f32_16x16x32_bf16 a[84:87], a[204:207], v[40:43], a[84:87]\n"
"ds_read_b64_tr_b16 a[218:219], v246 offset:5632\n"
"buffer_load_dwordx4 v247, s[60:63], s84 offen lds\n"
"v_mfma_f32_16x16x32_bf16 a[88:91], a[200:203], v[16:19], a[88:91]\n"
"s_add_u32 s84, s84, 32768\n"
"v_exp_f32 v103, v103\n"
"v_mfma_f32_16x16x32_bf16 a[88:91], a[204:207], v[48:51], a[88:91]\n"
"ds_read_b64_tr_b16 a[220:221], v246 offset:9728\n"
"v_cvt_pk_bf16_f32 v99, v102, v103\n"
"v_mfma_f32_16x16x32_bf16 a[92:95], a[200:203], v[24:27], a[92:95]\n"
"v_exp_f32 v72, v72\n"
"v_mfma_f32_16x16x32_bf16 a[92:95], a[204:207], v[56:59], a[92:95]\n"
"s_waitcnt lgkmcnt(5)\n"
"ds_read_b64_tr_b16 a[222:223], v246 offset:13824\n"
"v_mfma_f32_16x16x32_bf16 a[96:99], a[208:211], v[0:3], a[96:99]\n"
"s_waitcnt lgkmcnt(4)\n"
"ds_read_b128 a[160:163], v243 offset:0\n"
"v_mfma_f32_16x16x32_bf16 a[96:99], a[212:215], v[32:35], a[96:99]\n"
"v_exp_f32 v73, v73\n"
"v_mfma_f32_16x16x32_bf16 a[100:103], a[208:211], v[8:11], a[100:103]\n"
"ds_read_b128 a[164:167], v244 offset:0\n"
"v_cvt_pk_bf16_f32 v72, v72, v73\n"
"v_mfma_f32_16x16x32_bf16 a[100:103], a[212:215], v[40:43], a[100:103]\n"
"v_exp_f32 v74, v74\n"
"v_mfma_f32_16x16x32_bf16 a[104:107], a[208:211], v[16:19], a[104:107]\n"
"ds_read_b128 a[168:171], v243 offset:2048\n"
"v_mfma_f32_16x16x32_bf16 a[104:107], a[212:215], v[48:51], a[104:107]\n"
"v_exp_f32 v75, v75\n"
"v_mfma_f32_16x16x32_bf16 a[108:111], a[208:211], v[24:27], a[108:111]\n"
"ds_read_b128 a[172:175], v244 offset:2048\n"
"v_cvt_pk_bf16_f32 v73, v74, v75\n"
"v_mfma_f32_16x16x32_bf16 a[108:111], a[212:215], v[56:59], a[108:111]\n"
"s_waitcnt lgkmcnt(6)\n"
"v_exp_f32 v76, v76\n"
"v_mfma_f32_16x16x32_bf16 a[112:115], a[216:219], v[0:3], a[112:115]\n"
"s_waitcnt lgkmcnt(4)\n"
"v_exp_f32 v77, v77\n"
"v_mfma_f32_16x16x32_bf16 a[112:115], a[220:223], v[32:35], a[112:115]\n"
"v_exp_f32 v78, v78\n"
"v_mfma_f32_16x16x32_bf16 a[116:119], a[216:219], v[8:11], a[116:119]\n"
"buffer_load_dword v222, v213, s[64:67], s13 offen\n"
"v_exp_f32 v79, v79\n"
"v_mfma_f32_16x16x32_bf16 a[116:119], a[220:223], v[40:43], a[116:119]\n"
"s_add_u32 s13, s13, 2048\n"
"v_exp_f32 v104, v104\n"
"v_mfma_f32_16x16x32_bf16 a[120:123], a[216:219], v[16:19], a[120:123]\n"
"v_exp_f32 v105, v105\n"
"v_mfma_f32_16x16x32_bf16 a[120:123], a[220:223], v[48:51], a[120:123]\n"
"v_exp_f32 v106, v106\n"
"v_mfma_f32_16x16x32_bf16 a[124:127], a[216:219], v[24:27], a[124:127]\n"
"v_exp_f32 v107, v107\n"
"v_mfma_f32_16x16x32_bf16 a[124:127], a[220:223], v[56:59], a[124:127]\n"
"v_exp_f32 v108, v108\n"
"s_waitcnt lgkmcnt(3)\n"
"v_mfma_f32_16x16x32_bf16 v[0:3], a[160:163], v[128:131], v[224:227]\n"
"s_cmp_lg_u32 s95, 0\n"
"s_cbranch_scc1 RB5_%=\n"
"RBr5_%=:\n"
"ds_read_b128 a[176:179], v243 offset:4096\n"
"v_cvt_pk_bf16_f32 v74, v76, v77\n"
"s_waitcnt lgkmcnt(3)\n"
"v_mfma_f32_16x16x32_bf16 v[0:3], a[164:167], v[132:135], v[0:3]\n"
"s_waitcnt lgkmcnt(2)\n"
"v_exp_f32 v109, v109\n"
"v_mfma_f32_16x16x32_bf16 v[0:3], a[168:171], v[136:139], v[0:3]\n"
"s_waitcnt lgkmcnt(1)\n"
"ds_read_b128 a[180:183], v244 offset:4096\n"
"v_cvt_pk_bf16_f32 v75, v78, v79\n"
"v_mfma_f32_16x16x32_bf16 v[0:3], a[172:175], v[140:143], v[0:3]\n"
"v_exp_f32 v110, v110\n"
"v_mfma_f32_16x16x32_bf16 v[8:11], a[160:163], v[144:147], v[228:231]\n"
"ds_read_b128 a[184:187], v243 offset:6144\n"
"v_cvt_pk_bf16_f32 v104, v104, v105\n"
"v_mfma_f32_16x16x32_bf16 v[8:11], a[164:167], v[148:151], v[8:11]\n"
"v_exp_f32 v111, v111\n"
"v_mfma_f32_16x16x32_bf16 v[8:11], a[168:171], v[152:155], v[8:11]\n"
"ds_read_b128 a[188:191], v244 offset:6144\n"
"v_cvt_pk_bf16_f32 v105, v106, v107\n"
"v_mfma_f32_16x16x32_bf16 v[8:11], a[172:175], v[156:159], v[8:11]\n"
"v_exp_f32 v80, v80\n"
"v_mfma_f32_16x16x32_bf16 v[16:19], a[160:163], v[160:163], v[232:235]\n"
"v_exp_f32 v81, v81\n"
"v_mfma_f32_16x16x32_bf16 v[16:19], a[164:167], v[164:167], v[16:19]\n"
"v_exp_f32 v82, v82\n"
"v_mfma_f32_16x16x32_bf16 v[16:19], a[168:171], v[168:171], v[16:19]\n"
"v_exp_f32 v83, v83\n"
"v_mfma_f32_16x16x32_bf16 v[16:19], a[172:175], v[172:175], v[16:19]\n"
"v_exp_f32 v84, v84\n"
"v_mfma_f32_16x16x32_bf16 v[24:27], a[160:163], v[176:179], v[236:239]\n"
"v_exp_f32 v85, v85\n"
"v_mfma_f32_16x16x32_bf16 v[24:27], a[164:167], v[180:183], v[24:27]\n"
"v_exp_f32 v86, v86\n"
"v_mfma_f32_16x16x32_bf16 v[24:27], a[168:171], v[184:187], v[24:27]\n"
"v_exp_f32 v87, v87\n"
"v_mfma_f32_16x16x32_bf16 v[24:27], a[172:175], v[188:191], v[24:27]\n"
"s_waitcnt lgkmcnt(3)\n"
"v_exp_f32 v112, v112\n"
"v_mfma_f32_16x16x32_bf16 v[4:7], a[176:179], v[128:131], v[224:227]\n"
"s_waitcnt lgkmcnt(2)\n"
"ds_read_b128 a[160:163], v243 offset:8192\n"
"v_max3_f32 v252, v0, v1, v2\n"
"v_mfma_f32_16x16x32_bf16 v[4:7], a[180:183], v[132:135], v[4:7]\n"
"s_waitcnt lgkmcnt(2)\n"
"v_exp_f32 v113, v113\n"
"v_mfma_f32_16x16x32_bf16 v[4:7], a[184:187], v[136:139], v[4:7]\n"
"s_waitcnt lgkmcnt(1)\n"
"ds_read_b128 a[164:167], v244 offset:8192\n"
"v_max3_f32 v253, v8, v9, v10\n"
"v_mfma_f32_16x16x32_bf16 v[4:7], a[188:191], v[140:143], v[4:7]\n"
"v_exp_f32 v114, v114\n"
"v_mfma_f32_16x16x32_bf16 v[12:15], a[176:179], v[144:147], v[228:231]\n"
"ds_read_b128 a[168:171], v243 offset:10240\n"
"v_max3_f32 v254, v16, v17, v18\n"
"v_mfma_f32_16x16x32_bf16 v[12:15], a[180:183], v[148:151], v[12:15]\n"
"v_cvt_pk_bf16_f32 v106, v108, v109\n"
"v_cvt_pk_bf16_f32 v107, v110, v111\n"
"v_mfma_f32_16x16x32_bf16 v[12:15], a[184:187], v[152:155], v[12:15]\n"
"ds_read_b128 a[172:175], v244 offset:10240\n"
"v_max3_f32 v255, v24, v25, v26\n"
"v_mfma_f32_16x16x32_bf16 v[12:15], a[188:191], v[156:159], v[12:15]\n"
"v_exp_f32 v115, v115\n"
"v_mfma_f32_16x16x32_bf16 v[20:23], a[176:179], v[160:163], v[232:235]\n"
"v_exp_f32 v116, v116\n"
"v_mfma_f32_16x16x32_bf16 v[20:23], a[180:183], v[164:167], v[20:23]\n"
"v_exp_f32 v117, v117\n"
"v_mfma_f32_16x16x32_bf16 v[20:23], a[184:187], v[168:171], v[20:23]\n"
"v_exp_f32 v118, v118\n"
"v_mfma_f32_16x16x32_bf16 v[20:23], a[188:191], v[172:175], v[20:23]\n"
"v_exp_f32 v119, v119\n"
"v_mfma_f32_16x16x32_bf16 v[28:31], a[176:179], v[176:179], v[236:239]\n"
"v_exp_f32 v88, v88\n"
"v_mfma_f32_16x16x32_bf16 v[28:31], a[180:183], v[180:183], v[28:31]\n"
"v_exp_f32 v89, v89\n"
"v_mfma_f32_16x16x32_bf16 v[28:31], a[184:187], v[184:187], v[28:31]\n"
"v_cvt_pk_bf16_f32 v80, v80, v81\n"
"v_cvt_pk_bf16_f32 v81, v82, v83\n"
"v_mfma_f32_16x16x32_bf16 v[28:31], a[188:191], v[188:191], v[28:31]\n"
"s_waitcnt lgkmcnt(3)\n"
"v_cvt_pk_bf16_f32 v82, v84, v85\n"
"v_cvt_pk_bf16_f32 v83, v86, v87\n"
"v_mfma_f32_16x16x32_bf16 v[32:35], a[160:163], v[128:131], v[224:227]\n"
"s_waitcnt lgkmcnt(2)\n"
"ds_read_b128 a[176:179], v243 offset:12288\n"
"v_max3_f32 v252, v252, v3, v4\n"
"v_mfma_f32_16x16x32_bf16 v[32:35], a[164:167], v[132:135], v[32:35]\n"
"s_waitcnt lgkmcnt(2)\n"
"v_exp_f32 v90, v90\n"
"v_mfma_f32_16x16x32_bf16 v[32:35], a[168:171], v[136:139], v[32:35]\n"
"s_waitcnt lgkmcnt(1)\n"
"ds_read_b128 a[180:183], v244 offset:12288\n"
"v_max3_f32 v253, v253, v11, v12\n"
"v_mfma_f32_16x16x32_bf16 v[32:35], a[172:175], v[140:143], v[32:35]\n"
"v_exp_f32 v91, v91\n"
"v_mfma_f32_16x16x32_bf16 v[40:43], a[160:163], v[144:147], v[228:231]\n"
"ds_read_b128 a[184:187], v243 offset:14336\n"
"v_max3_f32 v254, v254, v19, v20\n"
"v_mfma_f32_16x16x32_bf16 v[40:43], a[164:167], v[148:151], v[40:43]\n"
"v_exp_f32 v92, v92\n"
"v_mfma_f32_16x16x32_bf16 v[40:43], a[168:171], v[152:155], v[40:43]\n"
"ds_read_b128 a[188:191], v244 offset:14336\n"
"v_max3_f32 v255, v255, v27, v28\n"
"v_mfma_f32_16x16x32_bf16 v[40:43], a[172:175], v[156:159], v[40:43]\n"
"v_exp_f32 v93, v93\n"
"v_mfma_f32_16x16x32_bf16 v[48:51], a[160:163], v[160:163], v[232:235]\n"
"v_exp_f32 v94, v94\n"
"s_waitcnt vmcnt(18)\n"
"v_xor_b32 v211, v210, v211\n"
"v_xor_b32 v210, 0x9e3779b9, v210\n"
"v_cmp_eq_u32 vcc, 0xe17d05ac, v211\n"
"v_mfma_f32_16x16x32_bf16 v[48:51], a[164:167], v[164:167], v[48:51]\n"
"v_cvt_pk_bf16_f32 v112, v112, v113\n"
"v_cvt_pk_bf16_f32 v113, v114, v115\n"
"v_subrev_u32 v210, s36, v210\n"
"v_cmp_gt_u32 s[98:99], 63, v210\n"
"v_mfma_f32_16x16x32_bf16 v[48:51], a[168:171], v[168:171], v[48:51]\n"
"s_and_b64 vcc, vcc, s[98:99]\n"
"s_cmp_eq_u64 vcc, -1\n"
"s_cbranch_scc0 RH_%=\n"
"RHr_%=:\n"
"s_add_u32 s30, s30, 8192\n"
"v_cvt_pk_bf16_f32 v114, v116, v117\n"
"v_cvt_pk_bf16_f32 v115, v118, v119\n"
"v_mfma_f32_16x16x32_bf16 v[48:51], a[172:175], v[172:175], v[48:51]\n"
"v_exp_f32 v95, v95\n"
"v_mfma_f32_16x16x32_bf16 v[56:59], a[160:163], v[176:179], v[236:239]\n"
"v_exp_f32 v120, v120\n"
"v_mfma_f32_16x16x32_bf16 v[56:59], a[164:167], v[180:183], v[56:59]\n"
"v_exp_f32 v121, v121\n"
"v_mfma_f32_16x16x32_bf16 v[56:59], a[168:171], v[184:187], v[56:59]\n"
"v_exp_f32 v122, v122\n"
"v_mfma_f32_16x16x32_bf16 v[56:59], a[172:175], v[188:191], v[56:59]\n"
"s_waitcnt lgkmcnt(3)\n"
"v_exp_f32 v123, v123\n"
"v_mfma_f32_16x16x32_bf16 v[36:39], a[176:179], v[128:131], v[224:227]\n"
"s_waitcnt vmcnt(10) lgkmcnt(0)\n"
"s_barrier\n"
"v_exp_f32 v124, v124\n"
"v_mfma_f32_16x16x32_bf16 v[36:39], a[180:183], v[132:135], v[36:39]\n"
"s_add_u32 m0, s94, 0\n"
"v_exp_f32 v125, v125\n"
"v_mfma_f32_16x16x32_bf16 v[36:39], a[184:187], v[136:139], v[36:39]\n"
"buffer_load_dwordx4 v250, s[56:59], s90 offen lds\n"
"v_mfma_f32_16x16x32_bf16 v[36:39], a[188:191], v[140:143], v[36:39]\n"
"s_add_u32 m0, s94, 896\n"
"v_exp_f32 v126, v126\n"
"v_mfma_f32_16x16x32_bf16 v[44:47], a[176:179], v[144:147], v[228:231]\n"
"buffer_load_dwordx4 v250, s[56:59], s90 offen offset:128 lds\n"
"v_mfma_f32_16x16x32_bf16 v[44:47], a[180:183], v[148:151], v[44:47]\n"
"s_add_u32 m0, s94, 0\n"
"v_cvt_pk_bf16_f32 v88, v88, v89\n"
"v_cvt_pk_bf16_f32 v89, v90, v91\n"
"v_mfma_f32_16x16x32_bf16 v[44:47], a[184:187], v[152:155], v[44:47]\n"
"buffer_load_dwordx4 v250, s[56:59], s90 offen offset:2048 lds\n"
"v_mfma_f32_16x16x32_bf16 v[44:47], a[188:191], v[156:159], v[44:47]\n"
"s_add_u32 m0, s94, 896\n"
"v_cvt_pk_bf16_f32 v90, v92, v93\n"
"v_cvt_pk_bf16_f32 v91, v94, v95\n"
"v_mfma_f32_16x16x32_bf16 v[52:55], a[176:179], v[160:163], v[232:235]\n"
"buffer_load_dwordx4 v250, s[56:59], s90 offen offset:2176 lds\n"
"v_mfma_f32_16x16x32_bf16 v[52:55], a[180:183], v[164:167], v[52:55]\n"
"ds_read_b64_tr_b16 a[192:193], v245 offset:16384\n"
"v_max3_f32 v252, v252, v5, v6\n"
"v_mfma_f32_16x16x32_bf16 v[52:55], a[184:187], v[168:171], v[52:55]\n"
"v_exp_f32 v127, v127\n"
"v_mfma_f32_16x16x32_bf16 v[52:55], a[188:191], v[172:175], v[52:55]\n"
"ds_read_b64_tr_b16 a[194:195], v245 offset:20480\n"
"v_max3_f32 v252, v252, v7, v32\n"
"v_mfma_f32_16x16x32_bf16 v[60:63], a[176:179], v[176:179], v[236:239]\n"
"v_cvt_pk_bf16_f32 v120, v120, v121\n"
"v_cvt_pk_bf16_f32 v121, v122, v123\n"
"v_mfma_f32_16x16x32_bf16 v[60:63], a[180:183], v[180:183], v[60:63]\n"
"ds_read_b64_tr_b16 a[196:197], v245 offset:24576\n"
"v_max3_f32 v253, v253, v13, v14\n"
"v_mfma_f32_16x16x32_bf16 v[60:63], a[184:187], v[184:187], v[60:63]\n"
"s_add_u32 s90, s90, 16384\n"
"v_cvt_pk_bf16_f32 v122, v124, v125\n"
"v_cvt_pk_bf16_f32 v123, v126, v127\n"
"v_mfma_f32_16x16x32_bf16 v[60:63], a[188:191], v[188:191], v[60:63]\n"
"s_add_u32 m0, s86, 98304\n"
"ds_read_b64_tr_b16 a[198:199], v245 offset:28672\n"
"v_max3_f32 v253, v253, v15, v40\n"
"v_mfma_f32_16x16x32_bf16 a[128:131], a[224:227], v[64:67], a[128:131]\n"
"buffer_load_dwordx4 v247, s[64:67], s85 offen lds\n"
"v_mfma_f32_16x16x32_bf16 a[128:131], a[224:227], v[96:99], a[128:131]\n"
"s_add_u32 m0, s86, 99328\n"
"ds_read_b64_tr_b16 a[200:201], v246 offset:16384\n"
"v_max3_f32 v254, v254, v21, v22\n"
"v_mfma_f32_16x16x32_bf16 a[132:135], a[224:227], v[72:75], a[132:135]\n"
"buffer_load_dwordx4 v248, s[64:67], s85 offen lds\n"
"v_mfma_f32_16x16x32_bf16 a[132:135], a[224:227], v[104:107], a[132:135]\n"
"s_add_u32 s85, s85, 131072\n"
"s_add_u32 m0, s86, 106496\n"
"ds_read_b64_tr_b16 a[202:203], v246 offset:20480\n"
"v_max3_f32 v254, v254, v23, v48\n"
"v_mfma_f32_16x16x32_bf16 a[136:139], a[224:227], v[80:83], a[136:139]\n"
"buffer_load_dwordx4 v247, s[64:67], s85 offen lds\n"
"v_mfma_f32_16x16x32_bf16 a[136:139], a[224:227], v[112:115], a[136:139]\n"
"s_add_u32 m0, s86, 107520\n"
"ds_read_b64_tr_b16 a[204:205], v246 offset:24576\n"
"v_max3_f32 v255, v255, v29, v30\n"
"v_mfma_f32_16x16x32_bf16 a[140:143], a[224:227], v[88:91], a[140:143]\n"
"buffer_load_dwordx4 v248, s[64:67], s85 offen lds\n"
"v_mfma_f32_16x16x32_bf16 a[140:143], a[224:227], v[120:123], a[140:143]\n"
"s_waitcnt lgkmcnt(5)\n"
"s_add_u32 s85, s85, 131072\n"
"ds_read_b64_tr_b16 a[206:207], v246 offset:28672\n"
"v_max3_f32 v255, v255, v31, v56\n"
"v_mfma_f32_16x16x32_bf16 a[0:3], a[192:195], v[64:67], a[0:3]\n"
"s_waitcnt lgkmcnt(4)\n"
"v_max3_f32 v252, v252, v33, v34\n"
"v_max3_f32 v252, v252, v35, v36\n"
"v_mfma_f32_16x16x32_bf16 a[0:3], a[196:199], v[96:99], a[0:3]\n"
"ds_read_b64_tr_b16 a[208:209], v245 offset:16896\n"
"buffer_load_dword v199, v218, s[16:19], s14 offen\n"
"v_max3_f32 v253, v253, v41, v42\n"
"v_mfma_f32_16x16x32_bf16 a[4:7], a[192:195], v[72:75], a[4:7]\n"
"s_add_u32 s14, s14, 16384\n"
"v_max3_f32 v253, v253, v43, v44\n"
"v_max3_f32 v254, v254, v49, v50\n"
"v_mfma_f32_16x16x32_bf16 a[4:7], a[196:199], v[104:107], a[4:7]\n"
"ds_read_b64_tr_b16 a[210:211], v245 offset:20992\n"
"v_max3_f32 v254, v254, v51, v52\n"
"v_mfma_f32_16x16x32_bf16 a[8:11], a[192:195], v[80:83], a[8:11]\n"
"v_max3_f32 v255, v255, v57, v58\n"
"v_max3_f32 v255, v255, v59, v60\n"
"v_mfma_f32_16x16x32_bf16 a[8:11], a[196:199], v[112:115], a[8:11]\n"
"ds_read_b64_tr_b16 a[212:213], v245 offset:25088\n"
"v_max3_f32 v252, v252, v37, v38\n"
"v_mfma_f32_16x16x32_bf16 a[12:15], a[192:195], v[88:91], a[12:15]\n"
"v_max_f32 v252, v252, v39\n"
"v_max3_f32 v253, v253, v45, v46\n"
"v_mfma_f32_16x16x32_bf16 a[12:15], a[196:199], v[120:123], a[12:15]\n"
"s_waitcnt lgkmcnt(5)\n"
"ds_read_b64_tr_b16 a[214:215], v245 offset:29184\n"
"v_max_f32 v253, v253, v47\n"
"v_mfma_f32_16x16x32_bf16 a[16:19], a[200:203], v[64:67], a[16:19]\n"
"s_waitcnt lgkmcnt(4)\n"
"v_max3_f32 v254, v254, v53, v54\n"
"v_max_f32 v254, v254, v55\n"
"v_mfma_f32_16x16x32_bf16 a[16:19], a[204:207], v[96:99], a[16:19]\n"
"ds_read_b64_tr_b16 a[216:217], v246 offset:16896\n"
"v_max3_f32 v255, v255, v61, v62\n"
"v_mfma_f32_16x16x32_bf16 a[20:23], a[200:203], v[72:75], a[20:23]\n"
"v_max_f32 v255, v255, v63\n"
"v_max3_f32 v196, v252, v253, v254\n"
"v_mfma_f32_16x16x32_bf16 a[20:23], a[204:207], v[104:107], a[20:23]\n"
"ds_read_b64_tr_b16 a[218:219], v246 offset:20992\n"
"v_max_f32 v196, v196, v255\n"
"v_mfma_f32_16x16x32_bf16 a[24:27], a[200:203], v[80:83], a[24:27]\n"
"v_cmp_lt_f32 vcc, s96, v196\n"
"v_mfma_f32_16x16x32_bf16 a[24:27], a[204:207], v[112:115], a[24:27]\n"
"ds_read_b64_tr_b16 a[220:221], v246 offset:25088\n"
"v_mfma_f32_16x16x32_bf16 a[28:31], a[200:203], v[88:91], a[28:31]\n"
"s_cbranch_vccnz RA6_%=\n"
"RAr6_%=:\n"
"v_exp_f32 v0, v0\n"
"v_mfma_f32_16x16x32_bf16 a[28:31], a[204:207], v[120:123], a[28:31]\n"
"s_waitcnt lgkmcnt(5)\n"
"ds_read_b64_tr_b16 a[222:223], v246 offset:29184\n"
"v_mfma_f32_16x16x32_bf16 a[32:35], a[208:211], v[64:67], a[32:35]\n"
"s_waitcnt lgkmcnt(4)\n"
"v_exp_f32 v1, v1\n"
"v_mfma_f32_16x16x32_bf16 a[32:35], a[212:215], v[96:99], a[32:35]\n"
"s_add_u32 s15, s6, 0\n"
"ds_read_b64_tr_b16 a[192:193], v245 offset:17408\n"
"v_cvt_pk_bf16_f32 v0, v0, v1\n"
"v_mfma_f32_16x16x32_bf16 a[36:39], a[208:211], v[72:75], a[36:39]\n"
"buffer_load_dwordx4 a[144:147], v251, s[40:43], s15 offen sc1\n"
"v_exp_f32 v2, v2\n"
"v_mfma_f32_16x16x32_bf16 a[36:39], a[212:215], v[104:107], a[36:39]\n"
"ds_read_b64_tr_b16 a[194:195], v245 offset:21504\n"
"v_mfma_f32_16x16x32_bf16 a[40:43], a[208:211], v[80:83], a[40:43]\n"
"v_exp_f32 v3, v3\n"
"v_mfma_f32_16x16x32_bf16 a[40:43], a[212:215], v[112:115], a[40:43]\n"
"ds_read_b64_tr_b16 a[196:197], v245 offset:25600\n"
"v_cvt_pk_bf16_f32 v1, v2, v3\n"
"v_mfma_f32_16x16x32_bf16 a[44:47], a[208:211], v[88:91], a[44:47]\n"
"buffer_load_dwordx4 a[148:151], v251, s[40:43], s15 offen offset:1024 sc1\n"
"v_exp_f32 v4, v4\n"
"v_mfma_f32_16x16x32_bf16 a[44:47], a[212:215], v[120:123], a[44:47]\n"
"s_waitcnt lgkmcnt(5)\n"
"ds_read_b64_tr_b16 a[198:199], v245 offset:29696\n"
"v_mfma_f32_16x16x32_bf16 a[48:51], a[216:219], v[64:67], a[48:51]\n"
"s_waitcnt lgkmcnt(4)\n"
"v_exp_f32 v5, v5\n"
"v_mfma_f32_16x16x32_bf16 a[48:51], a[220:223], v[96:99], a[48:51]\n"
"ds_read_b64_tr_b16 a[200:201], v246 offset:17408\n"
"v_cvt_pk_bf16_f32 v2, v4, v5\n"
"v_mfma_f32_16x16x32_bf16 a[52:55], a[216:219], v[72:75], a[52:55]\n"
"buffer_load_dwordx4 a[152:155], v251, s[40:43], s15 offen offset:2048 sc1\n"
"v_exp_f32 v6, v6\n"
"v_mfma_f32_16x16x32_bf16 a[52:55], a[220:223], v[104:107], a[52:55]\n"
"ds_read_b64_tr_b16 a[202:203], v246 offset:21504\n"
"v_mfma_f32_16x16x32_bf16 a[56:59], a[216:219], v[80:83], a[56:59]\n"
"v_exp_f32 v7, v7\n"
"v_mfma_f32_16x16x32_bf16 a[56:59], a[220:223], v[112:115], a[56:59]\n"
"ds_read_b64_tr_b16 a[204:205], v246 offset:25600\n"
"v_cvt_pk_bf16_f32 v3, v6, v7\n"
"v_mfma_f32_16x16x32_bf16 a[60:63], a[216:219], v[88:91], a[60:63]\n"
"buffer_load_dwordx4 a[156:159], v251, s[40:43], s15 offen offset:3072 sc1\n"
"v_exp_f32 v32, v32\n"
"v_mfma_f32_16x16x32_bf16 a[60:63], a[220:223], v[120:123], a[60:63]\n"
"s_waitcnt lgkmcnt(5)\n"
"ds_read_b64_tr_b16 a[206:207], v246 offset:29696\n"
"v_mfma_f32_16x16x32_bf16 a[64:67], a[192:195], v[64:67], a[64:67]\n"
"s_waitcnt lgkmcnt(4)\n"
"v_exp_f32 v33, v33\n"
"v_mfma_f32_16x16x32_bf16 a[64:67], a[196:199], v[96:99], a[64:67]\n"
"s_add_u32 s15, s6, 8192\n"
"ds_read_b64_tr_b16 a[208:209], v245 offset:17920\n"
"v_cvt_pk_bf16_f32 v32, v32, v33\n"
"v_mfma_f32_16x16x32_bf16 a[68:71], a[192:195], v[72:75], a[68:71]\n"
"buffer_load_dwordx4 a[228:231], v251, s[40:43], s15 offen sc1\n"
"v_exp_f32 v34, v34\n"
"v_mfma_f32_16x16x32_bf16 a[68:71], a[196:199], v[104:107], a[68:71]\n"
"ds_read_b64_tr_b16 a[210:211], v245 offset:22016\n"
"v_mfma_f32_16x16x32_bf16 a[72:75], a[192:195], v[80:83], a[72:75]\n"
"v_exp_f32 v35, v35\n"
"v_mfma_f32_16x16x32_bf16 a[72:75], a[196:199], v[112:115], a[72:75]\n"
"ds_read_b64_tr_b16 a[212:213], v245 offset:26112\n"
"v_cvt_pk_bf16_f32 v33, v34, v35\n"
"v_mfma_f32_16x16x32_bf16 a[76:79], a[192:195], v[88:91], a[76:79]\n"
"buffer_load_dwordx4 a[232:235], v251, s[40:43], s15 offen offset:1024 sc1\n"
"v_exp_f32 v36, v36\n"
"v_mfma_f32_16x16x32_bf16 a[76:79], a[196:199], v[120:123], a[76:79]\n"
"s_waitcnt lgkmcnt(5)\n"
"ds_read_b64_tr_b16 a[214:215], v245 offset:30208\n"
"v_mfma_f32_16x16x32_bf16 a[80:83], a[200:203], v[64:67], a[80:83]\n"
"s_waitcnt lgkmcnt(4)\n"
"v_exp_f32 v37, v37\n"
"v_mfma_f32_16x16x32_bf16 a[80:83], a[204:207], v[96:99], a[80:83]\n"
"ds_read_b64_tr_b16 a[216:217], v246 offset:17920\n"
"v_cvt_pk_bf16_f32 v34, v36, v37\n"
"v_mfma_f32_16x16x32_bf16 a[84:87], a[200:203], v[72:75], a[84:87]\n"
"buffer_load_dwordx4 a[236:239], v251, s[40:43], s15 offen offset:2048 sc1\n"
"v_exp_f32 v38, v38\n"
"v_mfma_f32_16x16x32_bf16 a[84:87], a[204:207], v[104:107], a[84:87]\n"
"ds_read_b64_tr_b16 a[218:219], v246 offset:22016\n"
"v_mfma_f32_16x16x32_bf16 a[88:91], a[200:203], v[80:83], a[88:91]\n"
"v_exp_f32 v39, v39\n"
"v_mfma_f32_16x16x32_bf16 a[88:91], a[204:207], v[112:115], a[88:91]\n"
"ds_read_b64_tr_b16 a[220:221], v246 offset:26112\n"
"v_cvt_pk_bf16_f32 v35, v38, v39\n"
"v_mfma_f32_16x16x32_bf16 a[92:95], a[200:203], v[88:91], a[92:95]\n"
"buffer_load_dwordx4 a[240:243], v251, s[40:43], s15 offen offset:3072 sc1\n"
"v_exp_f32 v8, v8\n"
"v_mfma_f32_16x16x32_bf16 a[92:95], a[204:207], v[120:123], a[92:95]\n"
"s_waitcnt lgkmcnt(5)\n"
"ds_read_b64_tr_b16 a[222:223], v246 offset:30208\n"
"v_mfma_f32_16x16x32_bf16 a[96:99], a[208:211], v[64:67], a[96:99]\n"
"s_waitcnt lgkmcnt(4)\n"
"ds_read_b128 a[160:163], v243 offset:16384\n"
"v_mfma_f32_16x16x32_bf16 a[96:99], a[212:215], v[96:99], a[96:99]\n"
"s_add_u32 s15, s6, 12288\n"
"v_exp_f32 v9, v9\n"
"v_mfma_f32_16x16x32_bf16 a[100:103], a[208:211], v[72:75], a[100:103]\n"
"ds_read_b128 a[164:167], v244 offset:16384\n"
"buffer_load_dwordx4 a[244:247], v251, s[40:43], s15 offen sc1\n"
"v_cvt_pk_bf16_f32 v8, v8, v9\n"
"v_mfma_f32_16x16x32_bf16 a[100:103], a[212:215], v[104:107], a[100:103]\n"
"v_exp_f32 v10, v10\n"
"v_mfma_f32_16x16x32_bf16 a[104:107], a[208:211], v[80:83], a[104:107]\n"
"ds_read_b128 a[168:171], v243 offset:18432\n"
"v_mfma_f32_16x16x32_bf16 a[104:107], a[212:215], v[112:115], a[104:107]\n"
"v_exp_f32 v11, v11\n"
"v_mfma_f32_16x16x32_bf16 a[108:111], a[208:211], v[88:91], a[108:111]\n"
"ds_read_b128 a[172:175], v244 offset:18432\n"
"buffer_load_dwordx4 a[248:251], v251, s[40:43], s15 offen offset:1024 sc1\n"
"v_cvt_pk_bf16_f32 v9, v10, v11\n"
"v_mfma_f32_16x16x32_bf16 a[108:111], a[212:215], v[120:123], a[108:111]\n"
"s_waitcnt lgkmcnt(6)\n"
"v_exp_f32 v12, v12\n"
"v_mfma_f32_16x16x32_bf16 a[112:115], a[216:219], v[64:67], a[112:115]\n"
"s_waitcnt lgkmcnt(4)\n"
"v_exp_f32 v13, v13\n"
"v_mfma_f32_16x16x32_bf16 a[112:115], a[220:223], v[96:99], a[112:115]\n"
"v_exp_f32 v14, v14\n"
"v_mfma_f32_16x16x32_bf16 a[116:119], a[216:219], v[72:75], a[116:119]\n"
"buffer_load_dwordx4 a[252:255], v251, s[40:43], s15 offen offset:2048 sc1\n"
"v_exp_f32 v15, v15\n"
"v_mfma_f32_16x16x32_bf16 a[116:119], a[220:223], v[104:107], a[116:119]\n"
"v_exp_f32 v40, v40\n"
"v_mfma_f32_16x16x32_bf16 a[120:123], a[216:219], v[80:83], a[120:123]\n"
"v_exp_f32 v41, v41\n"
"v_mfma_f32_16x16x32_bf16 a[120:123], a[220:223], v[112:115], a[120:123]\n"
"v_exp_f32 v42, v42\n"
"v_mfma_f32_16x16x32_bf16 a[124:127], a[216:219], v[88:91], a[124:127]\n"
"v_exp_f32 v43, v43\n"
"v_mfma_f32_16x16x32_bf16 a[124:127], a[220:223], v[120:123], a[124:127]\n"
"v_exp_f32 v44, v44\n"
"s_waitcnt lgkmcnt(3)\n"
"v_mfma_f32_16x16x32_bf16 v[64:67], a[160:163], v[128:131], v[224:227]\n"
"s_cmp_lg_u32 s95, 0\n"
"s_cbranch_scc1 RB6_%=\n"
"RBr6_%=:\n"
"ds_read_b128 a[176:179], v243 offset:20480\n"
"v_cvt_pk_bf16_f32 v10, v12, v13\n"
"s_waitcnt lgkmcnt(3)\n"
"v_mfma_f32_16x16x32_bf16 v[64:67], a[164:167], v[132:135], v[64:67]\n"
"s_waitcnt lgkmcnt(2)\n"
"v_exp_f32 v45, v45\n"
"v_mfma_f32_16x16x32_bf16 v[64:67], a[168:171], v[136:139], v[64:67]\n"
"s_waitcnt lgkmcnt(1)\n"
"ds_read_b128 a[180:183], v244 offset:20480\n"
"v_cvt_pk_bf16_f32 v11, v14, v15\n"
"v_mfma_f32_16x16x32_bf16 v[64:67], a[172:175], v[140:143], v[64:67]\n"
"v_exp_f32 v46, v46\n"
"v_mfma_f32_16x16x32_bf16 v[72:75], a[160:163], v[144:147], v[228:231]\n"
"ds_read_b128 a[184:187], v243 offset:22528\n"
"v_cvt_pk_bf16_f32 v40, v40, v41\n"
"v_mfma_f32_16x16x32_bf16 v[72:75], a[164:167], v[148:151], v[72:75]\n"
"v_exp_f32 v47, v47\n"
"v_mfma_f32_16x16x32_bf16 v[72:75], a[168:171], v[152:155], v[72:75]\n"
"ds_read_b128 a[188:191], v244 offset:22528\n"
"v_cvt_pk_bf16_f32 v41, v42, v43\n"
"v_mfma_f32_16x16x32_bf16 v[72:75], a[172:175], v[156:159], v[72:75]\n"
"v_exp_f32 v16, v16\n"
"v_mfma_f32_16x16x32_bf16 v[80:83], a[160:163], v[160:163], v[232:235]\n"
"v_exp_f32 v17, v17\n"
"v_mfma_f32_16x16x32_bf16 v[80:83], a[164:167], v[164:167], v[80:83]\n"
"v_exp_f32 v18, v18\n"
"v_mfma_f32_16x16x32_bf16 v[80:83], a[168:171], v[168:171], v[80:83]\n"
"v_exp_f32 v19, v19\n"
"v_mfma_f32_16x16x32_bf16 v[80:83], a[172:175], v[172:175], v[80:83]\n"
"v_exp_f32 v20, v20\n"
"v_mfma_f32_16x16x32_bf16 v[88:91], a[160:163], v[176:179], v[236:239]\n"
"v_exp_f32 v21, v21\n"
"v_mfma_f32_16x16x32_bf16 v[88:91], a[164:167], v[180:183], v[88:91]\n"
"v_exp_f32 v22, v22\n"
"v_mfma_f32_16x16x32_bf16 v[88:91], a[168:171], v[184:187], v[88:91]\n"
"v_exp_f32 v23, v23\n"
"v_mfma_f32_16x16x32_bf16 v[88:91], a[172:175], v[188:191], v[88:91]\n"
"s_waitcnt lgkmcnt(3)\n"
"v_exp_f32 v48, v48\n"
"v_mfma_f32_16x16x32_bf16 v[68:71], a[176:179], v[128:131], v[224:227]\n"
"s_waitcnt lgkmcnt(2)\n"
"ds_read_b128 a[160:163], v243 offset:24576\n"
"v_max3_f32 v252, v64, v65, v66\n"
"v_mfma_f32_16x16x32_bf16 v[68:71], a[180:183], v[132:135], v[68:71]\n"
"s_waitcnt lgkmcnt(2)\n"
"v_exp_f32 v49, v49\n"
"v_mfma_f32_16x16x32_bf16 v[68:71], a[184:187], v[136:139], v[68:71]\n"
"s_waitcnt lgkmcnt(1)\n"
"ds_read_b128 a[164:167], v244 offset:24576\n"
"v_max3_f32 v253, v72, v73, v74\n"
"v_mfma_f32_16x16x32_bf16 v[68:71], a[188:191], v[140:143], v[68:71]\n"
"v_exp_f32 v50, v50\n"
"v_mfma_f32_16x16x32_bf16 v[76:79], a[176:179], v[144:147], v[228:231]\n"
"ds_read_b128 a[168:171], v243 offset:26624\n"
"v_max3_f32 v254, v80, v81, v82\n"
"v_mfma_f32_16x16x32_bf16 v[76:79], a[180:183], v[148:151], v[76:79]\n"
"v_cvt_pk_bf16_f32 v42, v44, v45\n"
"v_cvt_pk_bf16_f32 v43, v46, v47\n"
"v_mfma_f32_16x16x32_bf16 v[76:79], a[184:187], v[152:155], v[76:79]\n"
"ds_read_b128 a[172:175], v244 offset:26624\n"
"v_max3_f32 v255, v88, v89, v90\n"
"v_mfma_f32_16x16x32_bf16 v[76:79], a[188:191], v[156:159], v[76:79]\n"
"v_exp_f32 v51, v51\n"
"v_mfma_f32_16x16x32_bf16 v[84:87], a[176:179], v[160:163], v[232:235]\n"
"v_exp_f32 v52, v52\n"
"v_mfma_f32_16x16x32_bf16 v[84:87], a[180:183], v[164:167], v[84:87]\n"
"v_exp_f32 v53, v53\n"
"v_mfma_f32_16x16x32_bf16 v[84:87], a[184:187], v[168:171], v[84:87]\n"
"v_exp_f32 v54, v54\n"
"v_mfma_f32_16x16x32_bf16 v[84:87], a[188:191], v[172:175], v[84:87]\n"
"v_exp_f32 v55, v55\n"
"v_mfma_f32_16x16x32_bf16 v[92:95], a[176:179], v[176:179], v[236:239]\n"
"v_exp_f32 v24, v24\n"
"v_mfma_f32_16x16x32_bf16 v[92:95], a[180:183], v[180:183], v[92:95]\n"
"v_exp_f32 v25, v25\n"
"v_mfma_f32_16x16x32_bf16 v[92:95], a[184:187], v[184:187], v[92:95]\n"
"v_cvt_pk_bf16_f32 v16, v16, v17\n"
"v_cvt_pk_bf16_f32 v17, v18, v19\n"
"v_mfma_f32_16x16x32_bf16 v[92:95], a[188:191], v[188:191], v[92:95]\n"
"s_waitcnt lgkmcnt(3)\n"
"v_cvt_pk_bf16_f32 v18, v20, v21\n"
"v_cvt_pk_bf16_f32 v19, v22, v23\n"
"v_mfma_f32_16x16x32_bf16 v[96:99], a[160:163], v[128:131], v[224:227]\n"
"s_waitcnt lgkmcnt(2)\n"
"ds_read_b128 a[176:179], v243 offset:28672\n"
"v_max3_f32 v252, v252, v67, v68\n"
"v_mfma_f32_16x16x32_bf16 v[96:99], a[164:167], v[132:135], v[96:99]\n"
"s_waitcnt lgkmcnt(2)\n"
"v_exp_f32 v26, v26\n"
"v_mfma_f32_16x16x32_bf16 v[96:99], a[168:171], v[136:139], v[96:99]\n"
"s_waitcnt lgkmcnt(1)\n"
"ds_read_b128 a[180:183], v244 offset:28672\n"
"v_max3_f32 v253, v253, v75, v76\n"
"v_mfma_f32_16x16x32_bf16 v[96:99], a[172:175], v[140:143], v[96:99]\n"
"v_exp_f32 v27, v27\n"
"v_mfma_f32_16x16x32_bf16 v[104:107], a[160:163], v[144:147], v[228:231]\n"
"ds_read_b128 a[184:187], v243 offset:30720\n"
"v_max3_f32 v254, v254, v83, v84\n"
"v_mfma_f32_16x16x32_bf16 v[104:107], a[164:167], v[148:151], v[104:107]\n"
"v_exp_f32 v28, v28\n"
"v_mfma_f32_16x16x32_bf16 v[104:107], a[168:171], v[152:155], v[104:107]\n"
"ds_read_b128 a[188:191], v244 offset:30720\n"
"v_max3_f32 v255, v255, v91, v92\n"
"v_mfma_f32_16x16x32_bf16 v[104:107], a[172:175], v[156:159], v[104:107]\n"
"v_exp_f32 v29, v29\n"
"v_mfma_f32_16x16x32_bf16 v[112:115], a[160:163], v[160:163], v[232:235]\n"
"v_exp_f32 v30, v30\n"
"v_mfma_f32_16x16x32_bf16 v[112:115], a[164:167], v[164:167], v[112:115]\n"
"v_cvt_pk_bf16_f32 v48, v48, v49\n"
"v_cvt_pk_bf16_f32 v49, v50, v51\n"
"v_mfma_f32_16x16x32_bf16 v[112:115], a[168:171], v[168:171], v[112:115]\n"
"v_cvt_pk_bf16_f32 v50, v52, v53\n"
"v_cvt_pk_bf16_f32 v51, v54, v55\n"
"v_mfma_f32_16x16x32_bf16 v[112:115], a[172:175], v[172:175], v[112:115]\n"
"v_exp_f32 v31, v31\n"
"v_mfma_f32_16x16x32_bf16 v[120:123], a[160:163], v[176:179], v[236:239]\n"
"v_exp_f32 v56, v56\n"
"v_mfma_f32_16x16x32_bf16 v[120:123], a[164:167], v[180:183], v[120:123]\n"
"v_exp_f32 v57, v57\n"
"v_mfma_f32_16x16x32_bf16 v[120:123], a[168:171], v[184:187], v[120:123]\n"
"v_exp_f32 v58, v58\n"
"v_mfma_f32_16x16x32_bf16 v[120:123], a[172:175], v[188:191], v[120:123]\n"
"s_waitcnt lgkmcnt(3)\n"
"v_exp_f32 v59, v59\n"
"v_mfma_f32_16x16x32_bf16 v[100:103], a[176:179], v[128:131], v[224:227]\n"
"s_waitcnt vmcnt(16) lgkmcnt(0)\n"
"s_barrier\n"
"v_exp_f32 v60, v60\n"
"v_mfma_f32_16x16x32_bf16 v[100:103], a[180:183], v[132:135], v[100:103]\n"
"s_add_u32 m0, s94, 16384\n"
"v_exp_f32 v61, v61\n"
"v_mfma_f32_16x16x32_bf16 v[100:103], a[184:187], v[136:139], v[100:103]\n"
"buffer_load_dwordx4 v250, s[56:59], s90 offen lds\n"
"v_mfma_f32_16x16x32_bf16 v[100:103], a[188:191], v[140:143], v[100:103]\n"
"s_add_u32 m0, s94, 17280\n"
"v_exp_f32 v62, v62\n"
"v_mfma_f32_16x16x32_bf16 v[108:111], a[176:179], v[144:147], v[228:231]\n"
"buffer_load_dwordx4 v250, s[56:59], s90 offen offset:128 lds\n"
"v_mfma_f32_16x16x32_bf16 v[108:111], a[180:183], v[148:151], v[108:111]\n"
"s_add_u32 m0, s94, 16384\n"
"v_cvt_pk_bf16_f32 v24, v24, v25\n"
"v_cvt_pk_bf16_f32 v25, v26, v27\n"
"v_mfma_f32_16x16x32_bf16 v[108:111], a[184:187], v[152:155], v[108:111]\n"
"buffer_load_dwordx4 v250, s[56:59], s90 offen offset:2048 lds\n"
"v_mfma_f32_16x16x32_bf16 v[108:111], a[188:191], v[156:159], v[108:111]\n"
"s_add_u32 m0, s94, 17280\n"
"v_cvt_pk_bf16_f32 v26, v28, v29\n"
"v_cvt_pk_bf16_f32 v27, v30, v31\n"
"v_mfma_f32_16x16x32_bf16 v[116:119], a[176:179], v[160:163], v[232:235]\n"
"buffer_load_dwordx4 v250, s[56:59], s90 offen offset:2176 lds\n"
"v_mfma_f32_16x16x32_bf16 v[116:119], a[180:183], v[164:167], v[116:119]\n"
"ds_read_b64_tr_b16 a[192:193], v245 offset:0\n"
"v_max3_f32 v252, v252, v69, v70\n"
"v_mfma_f32_16x16x32_bf16 v[116:119], a[184:187], v[168:171], v[116:119]\n"
"v_exp_f32 v63, v63\n"
"v_mfma_f32_16x16x32_bf16 v[116:119], a[188:191], v[172:175], v[116:119]\n"
"ds_read_b64_tr_b16 a[194:195], v245 offset:4096\n"
"v_max3_f32 v252, v252, v71, v96\n"
"v_mfma_f32_16x16x32_bf16 v[124:127], a[176:179], v[176:179], v[236:239]\n"
"v_cvt_pk_bf16_f32 v56, v56, v57\n"
"v_cvt_pk_bf16_f32 v57, v58, v59\n"
"v_mfma_f32_16x16x32_bf16 v[124:127], a[180:183], v[180:183], v[124:127]\n"
"ds_read_b64_tr_b16 a[196:197], v245 offset:8192\n"
"v_max3_f32 v253, v253, v77, v78\n"
"v_mfma_f32_16x16x32_bf16 v[124:127], a[184:187], v[184:187], v[124:127]\n"
"s_add_u32 s90, s90, 16384\n"
"v_cvt_pk_bf16_f32 v58, v60, v61\n"
"v_cvt_pk_bf16_f32 v59, v62, v63\n"
"v_mfma_f32_16x16x32_bf16 v[124:127], a[188:191], v[188:191], v[124:127]\n"
"s_add_u32 m0, s86, 114688\n"
"ds_read_b64_tr_b16 a[198:199], v245 offset:12288\n"
"v_max3_f32 v253, v253, v79, v104\n"
"v_mfma_f32_16x16x32_bf16 a[128:131], a[224:227], v[0:3], a[128:131]\n"
"buffer_load_dwordx4 v247, s[64:67], s85 offen lds\n"
"v_mfma_f32_16x16x32_bf16 a[128:131], a[224:227], v[32:35], a[128:131]\n"
"s_add_u32 m0, s86, 115712\n"
"ds_read_b64_tr_b16 a[200:201], v246 offset:0\n"
"v_max3_f32 v254, v254, v85, v86\n"
"v_mfma_f32_16x16x32_bf16 a[132:135], a[224:227], v[8:11], a[132:135]\n"
"buffer_load_dwordx4 v248, s[64:67], s85 offen lds\n"
"v_mfma_f32_16x16x32_bf16 a[132:135], a[224:227], v[40:43], a[132:135]\n"
"s_add_u32 s85, s85, 131072\n"
"s_add_u32 m0, s86, 122880\n"
"ds_read_b64_tr_b16 a[202:203], v246 offset:4096\n"
"v_max3_f32 v254, v254, v87, v112\n"
"v_mfma_f32_16x16x32_bf16 a[136:139], a[224:227], v[16:19], a[136:139]\n"
"buffer_load_dwordx4 v247, s[64:67], s85 offen lds\n"
"v_mfma_f32_16x16x32_bf16 a[136:139], a[224:227], v[48:51], a[136:139]\n"
"s_add_u32 m0, s86, 123904\n"
"ds_read_b64_tr_b16 a[204:205], v246 offset:8192\n"
"v_max3_f32 v255, v255, v93, v94\n"
"v_mfma_f32_16x16x32_bf16 a[140:143], a[224:227], v[24:27], a[140:143]\n"
"buffer_load_dwordx4 v248, s[64:67], s85 offen lds\n"
"v_mfma_f32_16x16x32_bf16 a[140:143], a[224:227], v[56:59], a[140:143]\n"
"s_waitcnt lgkmcnt(5)\n"
"s_add_u32 s85, s85, 131072\n"
"ds_read_b64_tr_b16 a[206:207], v246 offset:12288\n"
"v_max3_f32 v255, v255, v95, v120\n"
"v_mfma_f32_16x16x32_bf16 a[0:3], a[192:195], v[0:3], a[0:3]\n"
"s_waitcnt lgkmcnt(4)\n"
"v_max3_f32 v252, v252, v97, v98\n"
"v_max3_f32 v252, v252, v99, v100\n"
"v_mfma_f32_16x16x32_bf16 a[0:3], a[196:199], v[32:35], a[0:3]\n"
"ds_read_b64_tr_b16 a[208:209], v245 offset:512\n"
"v_max3_f32 v253, v253, v105, v106\n"
"v_mfma_f32_16x16x32_bf16 a[4:7], a[192:195], v[8:11], a[4:7]\n"
"v_max3_f32 v253, v253, v107, v108\n"
"v_max3_f32 v254, v254, v113, v114\n"
"v_mfma_f32_16x16x32_bf16 a[4:7], a[196:199], v[40:43], a[4:7]\n"
"ds_read_b64_tr_b16 a[210:211], v245 offset:4608\n"
"v_max3_f32 v254, v254, v115, v116\n"
"v_mfma_f32_16x16x32_bf16 a[8:11], a[192:195], v[16:19], a[8:11]\n"
"v_max3_f32 v255, v255, v121, v122\n"
"v_max3_f32 v255, v255, v123, v124\n"
"v_mfma_f32_16x16x32_bf16 a[8:11], a[196:199], v[48:51], a[8:11]\n"
"ds_read_b64_tr_b16 a[212:213], v245 offset:8704\n"
"v_max3_f32 v252, v252, v101, v102\n"
"v_mfma_f32_16x16x32_bf16 a[12:15], a[192:195], v[24:27], a[12:15]\n"
"v_max_f32 v252, v252, v103\n"
"v_max3_f32 v253, v253, v109, v110\n"
"v_mfma_f32_16x16x32_bf16 a[12:15], a[196:199], v[56:59], a[12:15]\n"
"s_waitcnt lgkmcnt(5)\n"
"ds_read_b64_tr_b16 a[214:215], v245 offset:12800\n"
"v_max_f32 v253, v253, v111\n"
"v_mfma_f32_16x16x32_bf16 a[16:19], a[200:203], v[0:3], a[16:19]\n"
"s_waitcnt lgkmcnt(4)\n"
"v_max3_f32 v254, v254, v117, v118\n"
"v_max_f32 v254, v254, v119\n"
"v_mfma_f32_16x16x32_bf16 a[16:19], a[204:207], v[32:35], a[16:19]\n"
"ds_read_b64_tr_b16 a[216:217], v246 offset:512\n"
"v_max3_f32 v255, v255, v125, v126\n"
"v_mfma_f32_16x16x32_bf16 a[20:23], a[200:203], v[8:11], a[20:23]\n"
"v_max_f32 v255, v255, v127\n"
"v_max3_f32 v196, v252, v253, v254\n"
"v_mfma_f32_16x16x32_bf16 a[20:23], a[204:207], v[40:43], a[20:23]\n"
"ds_read_b64_tr_b16 a[218:219], v246 offset:4608\n"
"v_max_f32 v196, v196, v255\n"
"v_mfma_f32_16x16x32_bf16 a[24:27], a[200:203], v[16:19], a[24:27]\n"
"s_add_u32 s15, s6, 4096\n"
"v_cmp_lt_f32 vcc, s96, v196\n"
"v_mfma_f32_16x16x32_bf16 a[24:27], a[204:207], v[48:51], a[24:27]\n"
"ds_read_b64_tr_b16 a[220:221], v246 offset:8704\n"
"buffer_load_dwordx4 a[176:179], v251, s[40:43], s15 offen sc1\n"
"v_mfma_f32_16x16x32_bf16 a[28:31], a[200:203], v[24:27], a[28:31]\n"
"s_cbranch_vccnz RA7_%=\n"
"RAr7_%=:\n"
"v_exp_f32 v64, v64\n"
"v_mfma_f32_16x16x32_bf16 a[28:31], a[204:207], v[56:59], a[28:31]\n"
"s_waitcnt lgkmcnt(5)\n"
"ds_read_b64_tr_b16 a[222:223], v246 offset:12800\n"
"v_mfma_f32_16x16x32_bf16 a[32:35], a[208:211], v[0:3], a[32:35]\n"
"s_waitcnt lgkmcnt(4)\n"
"buffer_load_dwordx4 a[180:183], v251, s[40:43], s15 offen offset:1024 sc1\n"
"v_exp_f32 v65, v65\n"
"v_mfma_f32_16x16x32_bf16 a[32:35], a[212:215], v[32:35], a[32:35]\n"
"ds_read_b64_tr_b16 a[192:193], v245 offset:1024\n"
"v_cvt_pk_bf16_f32 v64, v64, v65\n"
"v_mfma_f32_16x16x32_bf16 a[36:39], a[208:211], v[8:11], a[36:39]\n"
"v_exp_f32 v66, v66\n"
"v_mfma_f32_16x16x32_bf16 a[36:39], a[212:215], v[40:43], a[36:39]\n"
"ds_read_b64_tr_b16 a[194:195], v245 offset:5120\n"
"buffer_load_dwordx4 a[184:187], v251, s[40:43], s15 offen offset:2048 sc1\n"
"v_mfma_f32_16x16x32_bf16 a[40:43], a[208:211], v[16:19], a[40:43]\n"
"v_exp_f32 v67, v67\n"
"v_mfma_f32_16x16x32_bf16 a[40:43], a[212:215], v[48:51], a[40:43]\n"
"ds_read_b64_tr_b16 a[196:197], v245 offset:9216\n"
"v_cvt_pk_bf16_f32 v65, v66, v67\n"
"v_mfma_f32_16x16x32_bf16 a[44:47], a[208:211], v[24:27], a[44:47]\n"
"s_and_b32 s12, s10, 1023\n"
"buffer_load_dwordx4 a[188:191], v251, s[40:43], s15 offen offset:3072 sc1\n"
"v_exp_f32 v68, v68\n"
"v_mfma_f32_16x16x32_bf16 a[44:47], a[212:215], v[56:59], a[44:47]\n"
"s_waitcnt lgkmcnt(5)\n"
"ds_read_b64_tr_b16 a[198:199], v245 offset:13312\n"
"v_lshrrev_b32 v223, 8, v251\n"
"v_lshlrev_b32 v223, 3, v223\n"
"v_mfma_f32_16x16x32_bf16 a[48:51], a[216:219], v[0:3], a[48:51]\n"
"s_waitcnt lgkmcnt(4)\n"
"s_add_u32 s15, s6, 12288\n"
"v_exp_f32 v69, v69\n"
"v_mfma_f32_16x16x32_bf16 a[48:51], a[220:223], v[32:35], a[48:51]\n"
"ds_read_b64_tr_b16 a[200:201], v246 offset:1024\n"
"buffer_load_dwordx4 v[160:163], v251, s[40:43], s15 offen offset:3072 sc1\n"
"v_cvt_pk_bf16_f32 v66, v68, v69\n"
"v_mfma_f32_16x16x32_bf16 a[52:55], a[216:219], v[8:11], a[52:55]\n"
"v_exp_f32 v70, v70\n"
"v_mfma_f32_16x16x32_bf16 a[52:55], a[220:223], v[40:43], a[52:55]\n"
"s_add_u32 s15, s6, 28672\n"
"ds_read_b64_tr_b16 a[202:203], v246 offset:5120\n"
"v_mfma_f32_16x16x32_bf16 a[56:59], a[216:219], v[16:19], a[56:59]\n"
"buffer_load_dwordx4 a[160:163], v251, s[40:43], s15 offen sc1\n"
"v_exp_f32 v71, v71\n"
"v_mfma_f32_16x16x32_bf16 a[56:59], a[220:223], v[48:51], a[56:59]\n"
"ds_read_b64_tr_b16 a[204:205], v246 offset:9216\n"
"v_cvt_pk_bf16_f32 v67, v70, v71\n"
"v_mfma_f32_16x16x32_bf16 a[60:63], a[216:219], v[24:27], a[60:63]\n"
"v_exp_f32 v96, v96\n"
"v_mfma_f32_16x16x32_bf16 a[60:63], a[220:223], v[56:59], a[60:63]\n"
"s_waitcnt lgkmcnt(5)\n"
"ds_read_b64_tr_b16 a[206:207], v246 offset:13312\n"
"buffer_load_dwordx4 a[164:167], v251, s[40:43], s15 offen offset:1024 sc1\n"
"v_mfma_f32_16x16x32_bf16 a[64:67], a[192:195], v[0:3], a[64:67]\n"
"s_waitcnt lgkmcnt(4)\n"
"v_exp_f32 v97, v97\n"
"v_mfma_f32_16x16x32_bf16 a[64:67], a[196:199], v[32:35], a[64:67]\n"
"ds_read_b64_tr_b16 a[208:209], v245 offset:1536\n"
"v_cvt_pk_bf16_f32 v96, v96, v97\n"
"v_mfma_f32_16x16x32_bf16 a[68:71], a[192:195], v[8:11], a[68:71]\n"
"buffer_load_dwordx4 a[168:171], v251, s[40:43], s15 offen offset:2048 sc1\n"
"v_exp_f32 v98, v98\n"
"v_mfma_f32_16x16x32_bf16 a[68:71], a[196:199], v[40:43], a[68:71]\n"
"ds_read_b64_tr_b16 a[210:211], v245 offset:5632\n"
"v_mfma_f32_16x16x32_bf16 a[72:75], a[192:195], v[16:19], a[72:75]\n"
"v_exp_f32 v99, v99\n"
"v_mfma_f32_16x16x32_bf16 a[72:75], a[196:199], v[48:51], a[72:75]\n"
"ds_read_b64_tr_b16 a[212:213], v245 offset:9728\n"
"buffer_load_dwordx4 a[172:175], v251, s[40:43], s15 offen offset:3072 sc1\n"
"v_cvt_pk_bf16_f32 v97, v98, v99\n"
"v_mfma_f32_16x16x32_bf16 a[76:79], a[192:195], v[24:27], a[76:79]\n"
"v_exp_f32 v100, v100\n"
"v_mfma_f32_16x16x32_bf16 a[76:79], a[196:199], v[56:59], a[76:79]\n"
"s_waitcnt lgkmcnt(5)\n"
"s_add_u32 s15, s6, 32768\n"
"ds_read_b64_tr_b16 a[214:215], v245 offset:13824\n"
"v_mfma_f32_16x16x32_bf16 a[80:83], a[200:203], v[0:3], a[80:83]\n"
"s_waitcnt lgkmcnt(4)\n"
"buffer_load_dwordx4 v[128:131], v251, s[40:43], s15 offen sc1\n"
"v_exp_f32 v101, v101\n"
"v_mfma_f32_16x16x32_bf16 a[80:83], a[204:207], v[32:35], a[80:83]\n"
"ds_read_b64_tr_b16 a[216:217], v246 offset:1536\n"
"v_cvt_pk_bf16_f32 v98, v100, v101\n"
"v_mfma_f32_16x16x32_bf16 a[84:87], a[200:203], v[8:11], a[84:87]\n"
"v_exp_f32 v102, v102\n"
"v_mfma_f32_16x16x32_bf16 a[84:87], a[204:207], v[40:43], a[84:87]\n"
"ds_read_b64_tr_b16 a[218:219], v246 offset:5632\n"
"buffer_load_dwordx4 v[132:135], v251, s[40:43], s15 offen offset:1024 sc1\n"
"v_mfma_f32_16x16x32_bf16 a[88:91], a[200:203], v[16:19], a[88:91]\n"
"v_exp_f32 v103, v103\n"
"v_mfma_f32_16x16x32_bf16 a[88:91], a[204:207], v[48:51], a[88:91]\n"
"ds_read_b64_tr_b16 a[220:221], v246 offset:9728\n"
"v_cvt_pk_bf16_f32 v99, v102, v103\n"
"v_mfma_f32_16x16x32_bf16 a[92:95], a[200:203], v[24:27], a[92:95]\n"
"buffer_load_dwordx4 v[136:139], v251, s[40:43], s15 offen offset:2048 sc1\n"
"v_exp_f32 v72, v72\n"
"v_mfma_f32_16x16x32_bf16 a[92:95], a[204:207], v[56:59], a[92:95]\n"
"s_waitcnt lgkmcnt(5)\n"
"ds_read_b64_tr_b16 a[222:223], v246 offset:13824\n"
"v_mfma_f32_16x16x32_bf16 a[96:99], a[208:211], v[0:3], a[96:99]\n"
"s_waitcnt lgkmcnt(4)\n"
"v_mfma_f32_16x16x32_bf16 a[96:99], a[212:215], v[32:35], a[96:99]\n"
"buffer_load_dwordx4 v[140:143], v251, s[40:43], s15 offen offset:3072 sc1\n"
"v_exp_f32 v73, v73\n"
"v_mfma_f32_16x16x32_bf16 a[100:103], a[208:211], v[8:11], a[100:103]\n"
"v_cvt_pk_bf16_f32 v72, v72, v73\n"
"v_mfma_f32_16x16x32_bf16 a[100:103], a[212:215], v[40:43], a[100:103]\n"
"s_add_u32 s15, s6, 36864\n"
"v_exp_f32 v74, v74\n"
"v_mfma_f32_16x16x32_bf16 a[104:107], a[208:211], v[16:19], a[104:107]\n"
"buffer_load_dwordx4 v[144:147], v251, s[40:43], s15 offen sc1\n"
"v_mfma_f32_16x16x32_bf16 a[104:107], a[212:215], v[48:51], a[104:107]\n"
"v_exp_f32 v75, v75\n"
"v_mfma_f32_16x16x32_bf16 a[108:111], a[208:211], v[24:27], a[108:111]\n"
"v_cvt_pk_bf16_f32 v73, v74, v75\n"
"v_mfma_f32_16x16x32_bf16 a[108:111], a[212:215], v[56:59], a[108:111]\n"
"s_waitcnt lgkmcnt(2)\n"
"buffer_load_dwordx4 v[148:151], v251, s[40:43], s15 offen offset:1024 sc1\n"
"v_exp_f32 v76, v76\n"
"v_mfma_f32_16x16x32_bf16 a[112:115], a[216:219], v[0:3], a[112:115]\n"
"s_waitcnt lgkmcnt(0)\n"
"v_exp_f32 v77, v77\n"
"v_mfma_f32_16x16x32_bf16 a[112:115], a[220:223], v[32:35], a[112:115]\n"
"v_exp_f32 v78, v78\n"
"v_mfma_f32_16x16x32_bf16 a[116:119], a[216:219], v[8:11], a[116:119]\n"
"buffer_load_dwordx4 v[152:155], v251, s[40:43], s15 offen offset:2048 sc1\n"
"v_exp_f32 v79, v79\n"
"v_mfma_f32_16x16x32_bf16 a[116:119], a[220:223], v[40:43], a[116:119]\n"
"v_exp_f32 v104, v104\n"
"v_mfma_f32_16x16x32_bf16 a[120:123], a[216:219], v[16:19], a[120:123]\n"
"v_exp_f32 v105, v105\n"
"v_mfma_f32_16x16x32_bf16 a[120:123], a[220:223], v[48:51], a[120:123]\n"
"buffer_load_dwordx4 v[156:159], v251, s[40:43], s15 offen offset:3072 sc1\n"
"v_exp_f32 v106, v106\n"
"v_mfma_f32_16x16x32_bf16 a[124:127], a[216:219], v[24:27], a[124:127]\n"
"v_exp_f32 v107, v107\n"
"v_mfma_f32_16x16x32_bf16 a[124:127], a[220:223], v[56:59], a[124:127]\n"
"v_exp_f32 v108, v108\n"
"s_cmp_lg_u32 s95, 0\n"
"s_cbranch_scc1 RB7_%=\n"
"RBr7_%=:\n"
"v_cvt_pk_bf16_f32 v74, v76, v77\n"
"s_add_u32 s15, s6, 16384\n"
"v_exp_f32 v109, v109\n"
"buffer_load_dwordx4 v[32:35], v251, s[40:43], s15 offen sc1\n"
"v_cvt_pk_bf16_f32 v75, v78, v79\n"
"v_exp_f32 v110, v110\n"
"buffer_load_dwordx4 v[36:39], v251, s[40:43], s15 offen offset:1024 sc1\n"
"v_cvt_pk_bf16_f32 v104, v104, v105\n"
"v_exp_f32 v111, v111\n"
"buffer_load_dwordx4 v[40:43], v251, s[40:43], s15 offen offset:2048 sc1\n"
"v_cvt_pk_bf16_f32 v105, v106, v107\n"
"v_exp_f32 v80, v80\n"
"buffer_load_dwordx4 v[44:47], v251, s[40:43], s15 offen offset:3072 sc1\n"
"v_exp_f32 v81, v81\n"
"s_add_u32 s15, s6, 20480\n"
"v_exp_f32 v82, v82\n"
"buffer_load_dwordx4 v[48:51], v251, s[40:43], s15 offen sc1\n"
"v_exp_f32 v83, v83\n"
"v_exp_f32 v84, v84\n"
"buffer_load_dwordx4 v[52:55], v251, s[40:43], s15 offen offset:1024 sc1\n"
"v_exp_f32 v85, v85\n"
"v_exp_f32 v86, v86\n"
"buffer_load_dwordx4 v[56:59], v251, s[40:43], s15 offen offset:2048 sc1\n"
"v_exp_f32 v87, v87\n"
"v_exp_f32 v112, v112\n"
"buffer_load_dwordx4 v[60:63], v251, s[40:43], s15 offen offset:3072 sc1\n"
"s_add_u32 s15, s6, 24576\n"
"v_exp_f32 v113, v113\n"
"buffer_load_dwordx4 v[224:227], v251, s[40:43], s15 offen sc1\n"
"v_exp_f32 v114, v114\n"
"buffer_load_dwordx4 v[228:231], v251, s[40:43], s15 offen offset:1024 sc1\n"
"v_cvt_pk_bf16_f32 v106, v108, v109\n"
"v_cvt_pk_bf16_f32 v107, v110, v111\n"
"buffer_load_dwordx4 v[232:235], v251, s[40:43], s15 offen offset:2048 sc1\n"
"v_exp_f32 v115, v115\n"
"buffer_load_dwordx4 v[236:239], v251, s[40:43], s15 offen offset:3072 sc1\n"
"v_exp_f32 v116, v116\n"
"v_exp_f32 v117, v117\n"
"v_exp_f32 v118, v118\n"
"v_exp_f32 v119, v119\n"
"v_exp_f32 v88, v88\n"
"v_exp_f32 v89, v89\n"
"v_cvt_pk_bf16_f32 v80, v80, v81\n"
"v_cvt_pk_bf16_f32 v81, v82, v83\n"
"v_cvt_pk_bf16_f32 v82, v84, v85\n"
"v_cvt_pk_bf16_f32 v83, v86, v87\n"
"v_exp_f32 v90, v90\n"
"v_exp_f32 v91, v91\n"
"v_exp_f32 v92, v92\n"
"v_exp_f32 v93, v93\n"
"buffer_load_dwordx2 v[180:181], v223, s[76:79], s12 offen\n"
"v_exp_f32 v94, v94\n"
"buffer_load_dwordx2 v[182:183], v223, s[76:79], s12 offen offset:32\n"
"v_cvt_pk_bf16_f32 v112, v112, v113\n"
"v_cvt_pk_bf16_f32 v113, v114, v115\n"
"buffer_load_dwordx2 v[184:185], v223, s[76:79], s12 offen offset:64\n"
"v_cvt_pk_bf16_f32 v114, v116, v117\n"
"v_cvt_pk_bf16_f32 v115, v118, v119\n"
"buffer_load_dwordx2 v[186:187], v223, s[76:79], s12 offen offset:96\n"
"v_exp_f32 v95, v95\n"
"v_exp_f32 v120, v120\n"
"v_exp_f32 v121, v121\n"
"v_exp_f32 v122, v122\n"
"v_exp_f32 v123, v123\n"
"s_waitcnt vmcnt(37)\n"
"s_barrier\n"
"v_exp_f32 v124, v124\n"
"v_exp_f32 v125, v125\n"
"s_add_u32 m0, s86, 131072\n"
"s_nop 0\n"
"buffer_load_dwordx4 v247, s[64:67], s85 offen lds\n"
"v_exp_f32 v126, v126\n"
"s_add_u32 m0, s86, 132096\n"
"v_cvt_pk_bf16_f32 v88, v88, v89\n"
"v_cvt_pk_bf16_f32 v89, v90, v91\n"
"buffer_load_dwordx4 v248, s[64:67], s85 offen lds\n"
"s_add_u32 s85, s85, 131072\n"
"v_cvt_pk_bf16_f32 v90, v92, v93\n"
"v_cvt_pk_bf16_f32 v91, v94, v95\n"
"s_add_u32 m0, s86, 139264\n"
"ds_read_b64_tr_b16 a[192:193], v245 offset:16384\n"
"buffer_load_dwordx4 v247, s[64:67], s85 offen lds\n"
"v_exp_f32 v127, v127\n"
"s_add_u32 m0, s86, 140288\n"
"ds_read_b64_tr_b16 a[194:195], v245 offset:20480\n"
"buffer_load_dwordx4 v248, s[64:67], s85 offen lds\n"
"v_cvt_pk_bf16_f32 v120, v120, v121\n"
"v_cvt_pk_bf16_f32 v121, v122, v123\n"
"s_add_u32 s85, s85, 131072\n"
"ds_read_b64_tr_b16 a[196:197], v245 offset:24576\n"
"v_cvt_pk_bf16_f32 v122, v124, v125\n"
"v_cvt_pk_bf16_f32 v123, v126, v127\n"
"ds_read_b64_tr_b16 a[198:199], v245 offset:28672\n"
"v_mfma_f32_16x16x32_bf16 a[128:131], a[224:227], v[64:67], a[128:131]\n"
"v_mfma_f32_16x16x32_bf16 a[128:131], a[224:227], v[96:99], a[128:131]\n"
"ds_read_b64_tr_b16 a[200:201], v246 offset:16384\n"
"buffer_load_dwordx2 v[188:189], v206, s[68:71], s10 offen\n"
"v_mfma_f32_16x16x32_bf16 a[132:135], a[224:227], v[72:75], a[132:135]\n"
"v_mfma_f32_16x16x32_bf16 a[132:135], a[224:227], v[104:107], a[132:135]\n"
"ds_read_b64_tr_b16 a[202:203], v246 offset:20480\n"
"buffer_load_dwordx2 v[190:191], v206, s[68:71], s10 offen offset:32\n"
"v_mfma_f32_16x16x32_bf16 a[136:139], a[224:227], v[80:83], a[136:139]\n"
"v_mfma_f32_16x16x32_bf16 a[136:139], a[224:227], v[112:115], a[136:139]\n"
"ds_read_b64_tr_b16 a[204:205], v246 offset:24576\n"
"buffer_load_dwordx2 v[192:193], v206, s[68:71], s10 offen offset:64\n"
"v_mfma_f32_16x16x32_bf16 a[140:143], a[224:227], v[88:91], a[140:143]\n"
"v_accvgpr_read_b32 v200, a128\n"
"v_mfma_f32_16x16x32_bf16 a[140:143], a[224:227], v[120:123], a[140:143]\n"
"s_waitcnt lgkmcnt(5)\n"
"ds_read_b64_tr_b16 a[206:207], v246 offset:28672\n"
"buffer_load_dwordx2 v[194:195], v206, s[68:71], s10 offen offset:96\n"
"v_mfma_f32_16x16x32_bf16 a[0:3], a[192:195], v[64:67], a[0:3]\n"
"s_waitcnt lgkmcnt(4)\n"
"v_rcp_f32 v200, v200\n"
"v_mfma_f32_16x16x32_bf16 a[0:3], a[196:199], v[96:99], a[0:3]\n"
"ds_read_b64_tr_b16 a[208:209], v245 offset:16896\n"
"v_accvgpr_read_b32 v201, a132\n"
"v_mfma_f32_16x16x32_bf16 a[4:7], a[192:195], v[72:75], a[4:7]\n"
"v_accvgpr_read_b32 v202, a136\n"
"v_mfma_f32_16x16x32_bf16 a[4:7], a[196:199], v[104:107], a[4:7]\n"
"ds_read_b64_tr_b16 a[210:211], v245 offset:20992\n"
"v_accvgpr_read_b32 v203, a140\n"
"v_mfma_f32_16x16x32_bf16 a[8:11], a[192:195], v[80:83], a[8:11]\n"
"v_rcp_f32 v201, v201\n"
"v_mfma_f32_16x16x32_bf16 a[8:11], a[196:199], v[112:115], a[8:11]\n"
"ds_read_b64_tr_b16 a[212:213], v245 offset:25088\n"
"v_mfma_f32_16x16x32_bf16 a[12:15], a[192:195], v[88:91], a[12:15]\n"
"v_rcp_f32 v202, v202\n"
"v_rcp_f32 v203, v203\n"
"v_mfma_f32_16x16x32_bf16 a[12:15], a[196:199], v[120:123], a[12:15]\n"
"s_waitcnt lgkmcnt(5)\n"
"ds_read_b64_tr_b16 a[214:215], v245 offset:29184\n"
"v_mfma_f32_16x16x32_bf16 a[16:19], a[200:203], v[64:67], a[16:19]\n"
"s_waitcnt lgkmcnt(4)\n"
"v_mfma_f32_16x16x32_bf16 a[16:19], a[204:207], v[96:99], a[16:19]\n"
"ds_read_b64_tr_b16 a[216:217], v246 offset:16896\n"
"v_mfma_f32_16x16x32_bf16 a[20:23], a[200:203], v[72:75], a[20:23]\n"
"v_mfma_f32_16x16x32_bf16 a[20:23], a[204:207], v[104:107], a[20:23]\n"
"ds_read_b64_tr_b16 a[218:219], v246 offset:20992\n"
"v_mfma_f32_16x16x32_bf16 a[24:27], a[200:203], v[80:83], a[24:27]\n"
"v_mfma_f32_16x16x32_bf16 a[24:27], a[204:207], v[112:115], a[24:27]\n"
"ds_read_b64_tr_b16 a[220:221], v246 offset:25088\n"
"v_accvgpr_read_b32 v0, a0\n"
"v_mfma_f32_16x16x32_bf16 a[28:31], a[200:203], v[88:91], a[28:31]\n"
"v_accvgpr_read_b32 v1, a1\n"
"v_mul_f32 v0, v0, v200\n"
"v_mfma_f32_16x16x32_bf16 a[28:31], a[204:207], v[120:123], a[28:31]\n"
"s_waitcnt lgkmcnt(5)\n"
"ds_read_b64_tr_b16 a[222:223], v246 offset:29184\n"
"v_mul_f32 v1, v1, v200\n"
"v_mfma_f32_16x16x32_bf16 a[32:35], a[208:211], v[64:67], a[32:35]\n"
"s_waitcnt lgkmcnt(4)\n"
"v_cvt_pk_bf16_f32 v0, v0, v1\n"
"v_accvgpr_read_b32 v2, a2\n"
"v_mfma_f32_16x16x32_bf16 a[32:35], a[212:215], v[96:99], a[32:35]\n"
"ds_read_b64_tr_b16 a[192:193], v245 offset:17408\n"
"v_accvgpr_read_b32 v3, a3\n"
"v_mfma_f32_16x16x32_bf16 a[36:39], a[208:211], v[72:75], a[36:39]\n"
"v_mul_f32 v2, v2, v200\n"
"v_mul_f32 v3, v3, v200\n"
"v_mfma_f32_16x16x32_bf16 a[36:39], a[212:215], v[104:107], a[36:39]\n"
"ds_read_b64_tr_b16 a[194:195], v245 offset:21504\n"
"v_cvt_pk_bf16_f32 v1, v2, v3\n"
"v_mfma_f32_16x16x32_bf16 a[40:43], a[208:211], v[80:83], a[40:43]\n"
"v_accvgpr_read_b32 v4, a16\n"
"v_accvgpr_read_b32 v5, a17\n"
"v_mfma_f32_16x16x32_bf16 a[40:43], a[212:215], v[112:115], a[40:43]\n"
"ds_read_b64_tr_b16 a[196:197], v245 offset:25600\n"
"v_mul_f32 v4, v4, v200\n"
"v_mfma_f32_16x16x32_bf16 a[44:47], a[208:211], v[88:91], a[44:47]\n"
"v_mul_f32 v5, v5, v200\n"
"v_cvt_pk_bf16_f32 v2, v4, v5\n"
"v_mfma_f32_16x16x32_bf16 a[44:47], a[212:215], v[120:123], a[44:47]\n"
"s_waitcnt lgkmcnt(5)\n"
"ds_read_b64_tr_b16 a[198:199], v245 offset:29696\n"
"v_accvgpr_read_b32 v6, a18\n"
"v_mfma_f32_16x16x32_bf16 a[48:51], a[216:219], v[64:67], a[48:51]\n"
"s_waitcnt lgkmcnt(4)\n"
"v_accvgpr_read_b32 v7, a19\n"
"v_mul_f32 v6, v6, v200\n"
"v_mfma_f32_16x16x32_bf16 a[48:51], a[220:223], v[96:99], a[48:51]\n"
"ds_read_b64_tr_b16 a[200:201], v246 offset:17408\n"
"v_mul_f32 v7, v7, v200\n"
"v_mfma_f32_16x16x32_bf16 a[52:55], a[216:219], v[72:75], a[52:55]\n"
"v_cvt_pk_bf16_f32 v3, v6, v7\n"
"v_accvgpr_read_b32 v8, a4\n"
"v_mfma_f32_16x16x32_bf16 a[52:55], a[220:223], v[104:107], a[52:55]\n"
"ds_read_b64_tr_b16 a[202:203], v246 offset:21504\n"
"buffer_store_dwordx4 v[0:3], v251, s[40:43], s20 offen sc1\n"
"v_accvgpr_read_b32 v9, a5\n"
"v_mfma_f32_16x16x32_bf16 a[56:59], a[216:219], v[80:83], a[56:59]\n"
"v_mul_f32 v8, v8, v201\n"
"v_mul_f32 v9, v9, v201\n"
"v_mfma_f32_16x16x32_bf16 a[56:59], a[220:223], v[112:115], a[56:59]\n"
"ds_read_b64_tr_b16 a[204:205], v246 offset:25600\n"
"v_cvt_pk_bf16_f32 v8, v8, v9\n"
"v_mfma_f32_16x16x32_bf16 a[60:63], a[216:219], v[88:91], a[60:63]\n"
"v_accvgpr_read_b32 v10, a6\n"
"v_accvgpr_read_b32 v11, a7\n"
"v_mfma_f32_16x16x32_bf16 a[60:63], a[220:223], v[120:123], a[60:63]\n"
"s_waitcnt lgkmcnt(5)\n"
"ds_read_b64_tr_b16 a[206:207], v246 offset:29696\n"
"v_mul_f32 v10, v10, v201\n"
"v_mfma_f32_16x16x32_bf16 a[64:67], a[192:195], v[64:67], a[64:67]\n"
"s_waitcnt lgkmcnt(4)\n"
"v_mul_f32 v11, v11, v201\n"
"v_cvt_pk_bf16_f32 v9, v10, v11\n"
"v_mfma_f32_16x16x32_bf16 a[64:67], a[196:199], v[96:99], a[64:67]\n"
"ds_read_b64_tr_b16 a[208:209], v245 offset:17920\n"
"v_accvgpr_read_b32 v12, a20\n"
"v_mfma_f32_16x16x32_bf16 a[68:71], a[192:195], v[72:75], a[68:71]\n"
"v_accvgpr_read_b32 v13, a21\n"
"v_mul_f32 v12, v12, v201\n"
"v_mfma_f32_16x16x32_bf16 a[68:71], a[196:199], v[104:107], a[68:71]\n"
"ds_read_b64_tr_b16 a[210:211], v245 offset:22016\n"
"v_mul_f32 v13, v13, v201\n"
"v_mfma_f32_16x16x32_bf16 a[72:75], a[192:195], v[80:83], a[72:75]\n"
"v_cvt_pk_bf16_f32 v10, v12, v13\n"
"v_accvgpr_read_b32 v14, a22\n"
"v_mfma_f32_16x16x32_bf16 a[72:75], a[196:199], v[112:115], a[72:75]\n"
"ds_read_b64_tr_b16 a[212:213], v245 offset:26112\n"
"v_accvgpr_read_b32 v15, a23\n"
"v_mfma_f32_16x16x32_bf16 a[76:79], a[192:195], v[88:91], a[76:79]\n"
"v_mul_f32 v14, v14, v201\n"
"v_mul_f32 v15, v15, v201\n"
"v_mfma_f32_16x16x32_bf16 a[76:79], a[196:199], v[120:123], a[76:79]\n"
"s_waitcnt lgkmcnt(5)\n"
"ds_read_b64_tr_b16 a[214:215], v245 offset:30208\n"
"v_cvt_pk_bf16_f32 v11, v14, v15\n"
"v_mfma_f32_16x16x32_bf16 a[80:83], a[200:203], v[64:67], a[80:83]\n"
"s_waitcnt lgkmcnt(4)\n"
"buffer_store_dwordx4 v[8:11], v251, s[40:43], s21 offen sc1\n"
"v_accvgpr_read_b32 v16, a8\n"
"v_accvgpr_read_b32 v17, a9\n"
"v_mfma_f32_16x16x32_bf16 a[80:83], a[204:207], v[96:99], a[80:83]\n"
"ds_read_b64_tr_b16 a[216:217], v246 offset:17920\n"
"v_mul_f32 v16, v16, v202\n"
"v_mfma_f32_16x16x32_bf16 a[84:87], a[200:203], v[72:75], a[84:87]\n"
"v_mul_f32 v17, v17, v202\n"
"v_cvt_pk_bf16_f32 v16, v16, v17\n"
"v_mfma_f32_16x16x32_bf16 a[84:87], a[204:207], v[104:107], a[84:87]\n"
"ds_read_b64_tr_b16 a[218:219], v246 offset:22016\n"
"v_accvgpr_read_b32 v18, a10\n"
"v_mfma_f32_16x16x32_bf16 a[88:91], a[200:203], v[80:83], a[88:91]\n"
"v_accvgpr_read_b32 v19, a11\n"
"v_mul_f32 v18, v18, v202\n"
"v_mfma_f32_16x16x32_bf16 a[88:91], a[204:207], v[112:115], a[88:91]\n"
"ds_read_b64_tr_b16 a[220:221], v246 offset:26112\n"
"v_mul_f32 v19, v19, v202\n"
"v_mfma_f32_16x16x32_bf16 a[92:95], a[200:203], v[88:91], a[92:95]\n"
"v_cvt_pk_bf16_f32 v17, v18, v19\n"
"v_accvgpr_read_b32 v20, a24\n"
"v_mfma_f32_16x16x32_bf16 a[92:95], a[204:207], v[120:123], a[92:95]\n"
"s_waitcnt lgkmcnt(5)\n"
"ds_read_b64_tr_b16 a[222:223], v246 offset:30208\n"
"v_accvgpr_read_b32 v21, a25\n"
"v_mfma_f32_16x16x32_bf16 a[96:99], a[208:211], v[64:67], a[96:99]\n"
"s_waitcnt lgkmcnt(4)\n"
"v_mul_f32 v20, v20, v202\n"
"v_mul_f32 v21, v21, v202\n"
"v_mfma_f32_16x16x32_bf16 a[96:99], a[212:215], v[96:99], a[96:99]\n"
"v_cvt_pk_bf16_f32 v18, v20, v21\n"
"v_accvgpr_read_b32 v22, a26\n"
"v_mfma_f32_16x16x32_bf16 a[100:103], a[208:211], v[72:75], a[100:103]\n"
"v_accvgpr_read_b32 v23, a27\n"
"v_mul_f32 v22, v22, v202\n"
"v_mfma_f32_16x16x32_bf16 a[100:103], a[212:215], v[104:107], a[100:103]\n"
"v_mul_f32 v23, v23, v202\n"
"v_cvt_pk_bf16_f32 v19, v22, v23\n"
"v_mfma_f32_16x16x32_bf16 a[104:107], a[208:211], v[80:83], a[104:107]\n"
"buffer_store_dwordx4 v[16:19], v251, s[40:43], s22 offen sc1\n"
"v_accvgpr_read_b32 v24, a12\n"
"v_accvgpr_read_b32 v25, a13\n"
"v_mfma_f32_16x16x32_bf16 a[104:107], a[212:215], v[112:115], a[104:107]\n"
"v_mul_f32 v24, v24, v203\n"
"v_mul_f32 v25, v25, v203\n"
"v_mfma_f32_16x16x32_bf16 a[108:111], a[208:211], v[88:91], a[108:111]\n"
"v_cvt_pk_bf16_f32 v24, v24, v25\n"
"v_accvgpr_read_b32 v26, a14\n"
"v_mfma_f32_16x16x32_bf16 a[108:111], a[212:215], v[120:123], a[108:111]\n"
"s_waitcnt lgkmcnt(2)\n"
"v_accvgpr_read_b32 v27, a15\n"
"v_mul_f32 v26, v26, v203\n"
"v_mfma_f32_16x16x32_bf16 a[112:115], a[216:219], v[64:67], a[112:115]\n"
"s_waitcnt lgkmcnt(0)\n"
"v_mul_f32 v27, v27, v203\n"
"v_cvt_pk_bf16_f32 v25, v26, v27\n"
"v_mfma_f32_16x16x32_bf16 a[112:115], a[220:223], v[96:99], a[112:115]\n"
"v_accvgpr_read_b32 v28, a28\n"
"v_accvgpr_read_b32 v29, a29\n"
"v_mfma_f32_16x16x32_bf16 a[116:119], a[216:219], v[72:75], a[116:119]\n"
"v_mul_f32 v28, v28, v203\n"
"v_mul_f32 v29, v29, v203\n"
"v_mfma_f32_16x16x32_bf16 a[116:119], a[220:223], v[104:107], a[116:119]\n"
"v_cvt_pk_bf16_f32 v26, v28, v29\n"
"v_accvgpr_read_b32 v30, a30\n"
"v_mfma_f32_16x16x32_bf16 a[120:123], a[216:219], v[80:83], a[120:123]\n"
"v_accvgpr_read_b32 v31, a31\n"
"v_mul_f32 v30, v30, v203\n"
"v_mfma_f32_16x16x32_bf16 a[120:123], a[220:223], v[112:115], a[120:123]\n"
"v_mul_f32 v31, v31, v203\n"
"v_cvt_pk_bf16_f32 v27, v30, v31\n"
"v_mfma_f32_16x16x32_bf16 a[124:127], a[216:219], v[88:91], a[124:127]\n"
"buffer_store_dwordx4 v[24:27], v251, s[40:43], s23 offen sc1\n"
"v_accvgpr_read_b32 v0, a32\n"
"v_accvgpr_read_b32 v1, a33\n"
"v_mfma_f32_16x16x32_bf16 a[124:127], a[220:223], v[120:123], a[124:127]\n"
"v_mul_f32 v0, v0, v200\n"
"v_mul_f32 v1, v1, v200\n"
"s_barrier\n"
"ds_read_b64_tr_b16 v[64:65], v204 offset:0\n"
"ds_read_b64_tr_b16 v[66:67], v204 offset:2048\n"
"ds_read_b64_tr_b16 v[68:69], v205 offset:0\n"
"ds_read_b64_tr_b16 v[70:71], v205 offset:2048\n"
"ds_read_b64_tr_b16 v[72:73], v204 offset:512\n"
"ds_read_b64_tr_b16 v[74:75], v204 offset:2560\n"
"ds_read_b64_tr_b16 v[76:77], v205 offset:512\n"
"ds_read_b64_tr_b16 v[78:79], v205 offset:2560\n"
"ds_read_b64_tr_b16 v[80:81], v204 offset:4096\n"
"ds_read_b64_tr_b16 v[82:83], v204 offset:6144\n"
"ds_read_b64_tr_b16 v[84:85], v205 offset:4096\n"
"ds_read_b64_tr_b16 v[86:87], v205 offset:6144\n"
"ds_read_b64_tr_b16 v[88:89], v204 offset:4608\n"
"ds_read_b64_tr_b16 v[90:91], v204 offset:6656\n"
"ds_read_b64_tr_b16 v[92:93], v205 offset:4608\n"
"ds_read_b64_tr_b16 v[94:95], v205 offset:6656\n"
"v_cvt_pk_bf16_f32 v0, v0, v1\n"
"v_accvgpr_read_b32 v2, a34\n"
"v_accvgpr_read_b32 v3, a35\n"
"v_mul_f32 v2, v2, v200\n"
"v_mul_f32 v3, v3, v200\n"
"v_cvt_pk_bf16_f32 v1, v2, v3\n"
"v_accvgpr_read_b32 v4, a48\n"
"v_accvgpr_read_b32 v5, a49\n"
"v_mul_f32 v4, v4, v200\n"
"v_mul_f32 v5, v5, v200\n"
"v_cvt_pk_bf16_f32 v2, v4, v5\n"
"v_accvgpr_read_b32 v6, a50\n"
"v_accvgpr_read_b32 v7, a51\n"
"v_mul_f32 v6, v6, v200\n"
"v_mul_f32 v7, v7, v200\n"
"v_cvt_pk_bf16_f32 v3, v6, v7\n"
"buffer_store_dwordx4 v[0:3], v251, s[40:43], s20 offen offset:1024 sc1\n"
"v_accvgpr_read_b32 v8, a36\n"
"v_accvgpr_read_b32 v9, a37\n"
"v_pk_mul_f32 v[8:9], v[8:9], v[200:201] op_sel:[0,1] op_sel_hi:[1,1]\n"
"v_cvt_pk_bf16_f32 v8, v8, v9\n"
"v_accvgpr_read_b32 v10, a38\n"
"v_accvgpr_read_b32 v11, a39\n"
"v_pk_mul_f32 v[10:11], v[10:11], v[200:201] op_sel:[0,1] op_sel_hi:[1,1]\n"
"v_cvt_pk_bf16_f32 v9, v10, v11\n"
"v_accvgpr_read_b32 v12, a52\n"
"v_accvgpr_read_b32 v13, a53\n"
"v_pk_mul_f32 v[12:13], v[12:13], v[200:201] op_sel:[0,1] op_sel_hi:[1,1]\n"
"v_cvt_pk_bf16_f32 v10, v12, v13\n"
"v_accvgpr_read_b32 v14, a54\n"
"v_accvgpr_read_b32 v15, a55\n"
"v_pk_mul_f32 v[14:15], v[14:15], v[200:201] op_sel:[0,1] op_sel_hi:[1,1]\n"
"v_cvt_pk_bf16_f32 v11, v14, v15\n"
"buffer_store_dwordx4 v[8:11], v251, s[40:43], s21 offen offset:1024 sc1\n"
"v_accvgpr_read_b32 v16, a40\n"
"v_accvgpr_read_b32 v17, a41\n"
"v_pk_mul_f32 v[16:17], v[16:17], v[202:203] op_sel:[0,0] op_sel_hi:[1,0]\n"
"v_cvt_pk_bf16_f32 v16, v16, v17\n"
"v_accvgpr_read_b32 v18, a42\n"
"v_accvgpr_read_b32 v19, a43\n"
"v_pk_mul_f32 v[18:19], v[18:19], v[202:203] op_sel:[0,0] op_sel_hi:[1,0]\n"
"v_cvt_pk_bf16_f32 v17, v18, v19\n"
"v_accvgpr_read_b32 v20, a56\n"
"v_accvgpr_read_b32 v21, a57\n"
"v_pk_mul_f32 v[20:21], v[20:21], v[202:203] op_sel:[0,0] op_sel_hi:[1,0]\n"
"v_cvt_pk_bf16_f32 v18, v20, v21\n"
"v_accvgpr_read_b32 v22, a58\n"
"v_accvgpr_read_b32 v23, a59\n"
"v_pk_mul_f32 v[22:23], v[22:23], v[202:203] op_sel:[0,0] op_sel_hi:[1,0]\n"
"v_cvt_pk_bf16_f32 v19, v22, v23\n"
"buffer_store_dwordx4 v[16:19], v251, s[40:43], s22 offen offset:1024 sc1\n"
"v_accvgpr_read_b32 v24, a44\n"
"v_accvgpr_read_b32 v25, a45\n"
"v_pk_mul_f32 v[24:25], v[24:25], v[202:203] op_sel:[0,1] op_sel_hi:[1,1]\n"
"v_cvt_pk_bf16_f32 v24, v24, v25\n"
"v_accvgpr_read_b32 v26, a46\n"
"v_accvgpr_read_b32 v27, a47\n"
"v_mul_f32 v26, v26, v203\n"
"v_mul_f32 v27, v27, v203\n"
"v_cvt_pk_bf16_f32 v25, v26, v27\n"
"v_accvgpr_read_b32 v28, a60\n"
"v_accvgpr_read_b32 v29, a61\n"
"v_mul_f32 v28, v28, v203\n"
"v_mul_f32 v29, v29, v203\n"
"v_cvt_pk_bf16_f32 v26, v28, v29\n"
"s_waitcnt lgkmcnt(14)\n"
"v_mfma_f32_16x16x32_bf16 v[164:167], v[64:67], a[144:147], 0\n"
"s_waitcnt lgkmcnt(12)\n"
"ds_read_b64_tr_b16 v[96:97], v204 offset:8192\n"
"ds_read_b64_tr_b16 v[98:99], v204 offset:10240\n"
"v_accvgpr_read_b32 v30, a62\n"
"v_accvgpr_read_b32 v31, a63\n"
"v_mfma_f32_16x16x32_bf16 v[168:171], v[68:71], a[144:147], 0\n"
"s_waitcnt lgkmcnt(12)\n"
"ds_read_b64_tr_b16 v[100:101], v205 offset:8192\n"
"ds_read_b64_tr_b16 v[102:103], v205 offset:10240\n"
"v_mul_f32 v30, v30, v203\n"
"v_mul_f32 v31, v31, v203\n"
"v_cvt_pk_bf16_f32 v27, v30, v31\n"
"v_mfma_f32_16x16x32_bf16 v[172:175], v[72:75], a[144:147], 0\n"
"s_waitcnt lgkmcnt(12)\n"
"ds_read_b64_tr_b16 v[104:105], v204 offset:8704\n"
"ds_read_b64_tr_b16 v[106:107], v204 offset:10752\n"
"buffer_store_dwordx4 v[24:27], v251, s[40:43], s23 offen offset:1024 sc1\n"
"v_accvgpr_read_b32 v0, a64\n"
"v_accvgpr_read_b32 v1, a65\n"
"v_mfma_f32_16x16x32_bf16 v[176:179], v[76:79], a[144:147], 0\n"
"s_waitcnt lgkmcnt(12)\n"
"ds_read_b64_tr_b16 v[108:109], v205 offset:8704\n"
"ds_read_b64_tr_b16 v[110:111], v205 offset:10752\n"
"v_mul_f32 v0, v0, v200\n"
"v_mul_f32 v1, v1, v200\n"
"v_cvt_pk_bf16_f32 v0, v0, v1\n"
"v_mfma_f32_16x16x32_bf16 v[164:167], v[80:83], a[148:151], v[164:167]\n"
"s_waitcnt lgkmcnt(12)\n"
"ds_read_b64_tr_b16 v[64:65], v204 offset:12288\n"
"ds_read_b64_tr_b16 v[66:67], v204 offset:14336\n"
"v_accvgpr_read_b32 v2, a66\n"
"v_accvgpr_read_b32 v3, a67\n"
"v_mfma_f32_16x16x32_bf16 v[168:171], v[84:87], a[148:151], v[168:171]\n"
"s_waitcnt lgkmcnt(12)\n"
"ds_read_b64_tr_b16 v[68:69], v205 offset:12288\n"
"ds_read_b64_tr_b16 v[70:71], v205 offset:14336\n"
"v_mul_f32 v2, v2, v200\n"
"v_mul_f32 v3, v3, v200\n"
"v_cvt_pk_bf16_f32 v1, v2, v3\n"
"v_mfma_f32_16x16x32_bf16 v[172:175], v[88:91], a[148:151], v[172:175]\n"
"s_waitcnt lgkmcnt(12)\n"
"ds_read_b64_tr_b16 v[72:73], v204 offset:12800\n"
"ds_read_b64_tr_b16 v[74:75], v204 offset:14848\n"
"v_accvgpr_read_b32 v4, a80\n"
"v_accvgpr_read_b32 v5, a81\n"
"v_mfma_f32_16x16x32_bf16 v[176:179], v[92:95], a[148:151], v[176:179]\n"
"s_waitcnt lgkmcnt(12)\n"
"ds_read_b64_tr_b16 v[76:77], v205 offset:12800\n"
"ds_read_b64_tr_b16 v[78:79], v205 offset:14848\n"
"v_mul_f32 v4, v4, v200\n"
"v_mul_f32 v5, v5, v200\n"
"v_cvt_pk_bf16_f32 v2, v4, v5\n"
"v_mfma_f32_16x16x32_bf16 v[164:167], v[96:99], a[152:155], v[164:167]\n"
"s_waitcnt lgkmcnt(12)\n"
"ds_read_b64_tr_b16 v[80:81], v204 offset:16384\n"
"ds_read_b64_tr_b16 v[82:83], v204 offset:18432\n"
"v_accvgpr_read_b32 v6, a82\n"
"v_accvgpr_read_b32 v7, a83\n"
"v_mfma_f32_16x16x32_bf16 v[168:171], v[100:103], a[152:155], v[168:171]\n"
"s_waitcnt lgkmcnt(12)\n"
"ds_read_b64_tr_b16 v[84:85], v205 offset:16384\n"
"ds_read_b64_tr_b16 v[86:87], v205 offset:18432\n"
"v_mul_f32 v6, v6, v200\n"
"v_mul_f32 v7, v7, v200\n"
"v_cvt_pk_bf16_f32 v3, v6, v7\n"
"v_mfma_f32_16x16x32_bf16 v[172:175], v[104:107], a[152:155], v[172:175]\n"
"s_waitcnt lgkmcnt(12)\n"
"ds_read_b64_tr_b16 v[88:89], v204 offset:16896\n"
"ds_read_b64_tr_b16 v[90:91], v204 offset:18944\n"
"buffer_store_dwordx4 v[0:3], v251, s[40:43], s20 offen offset:2048 sc1\n"
"v_accvgpr_read_b32 v8, a68\n"
"v_accvgpr_read_b32 v9, a69\n"
"v_mfma_f32_16x16x32_bf16 v[176:179], v[108:111], a[152:155], v[176:179]\n"
"s_waitcnt lgkmcnt(12)\n"
"ds_read_b64_tr_b16 v[92:93], v205 offset:16896\n"
"ds_read_b64_tr_b16 v[94:95], v205 offset:18944\n"
"v_mul_f32 v8, v8, v201\n"
"v_mul_f32 v9, v9, v201\n"
"v_cvt_pk_bf16_f32 v8, v8, v9\n"
"v_mfma_f32_16x16x32_bf16 v[164:167], v[64:67], a[156:159], v[164:167]\n"
"s_waitcnt lgkmcnt(12)\n"
"ds_read_b64_tr_b16 v[96:97], v204 offset:20480\n"
"ds_read_b64_tr_b16 v[98:99], v204 offset:22528\n"
"v_accvgpr_read_b32 v10, a70\n"
"v_accvgpr_read_b32 v11, a71\n"
"v_mfma_f32_16x16x32_bf16 v[168:171], v[68:71], a[156:159], v[168:171]\n"
"s_waitcnt lgkmcnt(12)\n"
"ds_read_b64_tr_b16 v[100:101], v205 offset:20480\n"
"ds_read_b64_tr_b16 v[102:103], v205 offset:22528\n"
"v_mul_f32 v10, v10, v201\n"
"v_mul_f32 v11, v11, v201\n"
"v_cvt_pk_bf16_f32 v9, v10, v11\n"
"v_mfma_f32_16x16x32_bf16 v[172:175], v[72:75], a[156:159], v[172:175]\n"
"s_waitcnt lgkmcnt(12)\n"
"ds_read_b64_tr_b16 v[104:105], v204 offset:20992\n"
"ds_read_b64_tr_b16 v[106:107], v204 offset:23040\n"
"v_accvgpr_read_b32 v12, a84\n"
"v_accvgpr_read_b32 v13, a85\n"
"v_mfma_f32_16x16x32_bf16 v[176:179], v[76:79], a[156:159], v[176:179]\n"
"s_waitcnt vmcnt(49) lgkmcnt(12)\n"
"ds_read_b64_tr_b16 v[108:109], v205 offset:20992\n"
"ds_read_b64_tr_b16 v[110:111], v205 offset:23040\n"
"v_mul_f32 v12, v12, v201\n"
"v_mul_f32 v13, v13, v201\n"
"v_cvt_pk_bf16_f32 v10, v12, v13\n"
"v_mfma_f32_16x16x32_bf16 v[164:167], v[80:83], a[176:179], v[164:167]\n"
"s_waitcnt lgkmcnt(12)\n"
"ds_read_b64_tr_b16 v[64:65], v204 offset:24576\n"
"ds_read_b64_tr_b16 v[66:67], v204 offset:26624\n"
"v_accvgpr_read_b32 v14, a86\n"
"v_accvgpr_read_b32 v15, a87\n"
"v_mfma_f32_16x16x32_bf16 v[168:171], v[84:87], a[176:179], v[168:171]\n"
"s_waitcnt lgkmcnt(12)\n"
"ds_read_b64_tr_b16 v[68:69], v205 offset:24576\n"
"ds_read_b64_tr_b16 v[70:71], v205 offset:26624\n"
"v_mul_f32 v14, v14, v201\n"
"v_mul_f32 v15, v15, v201\n"
"v_cvt_pk_bf16_f32 v11, v14, v15\n"
"v_mfma_f32_16x16x32_bf16 v[172:175], v[88:91], a[176:179], v[172:175]\n"
"s_waitcnt lgkmcnt(12)\n"
"ds_read_b64_tr_b16 v[72:73], v204 offset:25088\n"
"ds_read_b64_tr_b16 v[74:75], v204 offset:27136\n"
"buffer_store_dwordx4 v[8:11], v251, s[40:43], s21 offen offset:2048 sc1\n"
"v_accvgpr_read_b32 v16, a72\n"
"v_accvgpr_read_b32 v17, a73\n"
"v_mfma_f32_16x16x32_bf16 v[176:179], v[92:95], a[176:179], v[176:179]\n"
"s_waitcnt vmcnt(49) lgkmcnt(12)\n"
"ds_read_b64_tr_b16 v[76:77], v205 offset:25088\n"
"ds_read_b64_tr_b16 v[78:79], v205 offset:27136\n"
"v_mul_f32 v16, v16, v202\n"
"v_mul_f32 v17, v17, v202\n"
"v_cvt_pk_bf16_f32 v16, v16, v17\n"
"v_mfma_f32_16x16x32_bf16 v[164:167], v[96:99], a[180:183], v[164:167]\n"
"s_waitcnt lgkmcnt(12)\n"
"ds_read_b64_tr_b16 v[80:81], v204 offset:28672\n"
"ds_read_b64_tr_b16 v[82:83], v204 offset:30720\n"
"v_accvgpr_read_b32 v18, a74\n"
"v_accvgpr_read_b32 v19, a75\n"
"v_mfma_f32_16x16x32_bf16 v[168:171], v[100:103], a[180:183], v[168:171]\n"
"s_waitcnt lgkmcnt(12)\n"
"ds_read_b64_tr_b16 v[84:85], v205 offset:28672\n"
"ds_read_b64_tr_b16 v[86:87], v205 offset:30720\n"
"v_mul_f32 v18, v18, v202\n"
"v_mul_f32 v19, v19, v202\n"
"v_cvt_pk_bf16_f32 v17, v18, v19\n"
"v_mfma_f32_16x16x32_bf16 v[172:175], v[104:107], a[180:183], v[172:175]\n"
"s_waitcnt lgkmcnt(12)\n"
"ds_read_b64_tr_b16 v[88:89], v204 offset:29184\n"
"ds_read_b64_tr_b16 v[90:91], v204 offset:31232\n"
"v_accvgpr_read_b32 v20, a88\n"
"v_accvgpr_read_b32 v21, a89\n"
"v_mfma_f32_16x16x32_bf16 v[176:179], v[108:111], a[180:183], v[176:179]\n"
"s_waitcnt vmcnt(48) lgkmcnt(12)\n"
"ds_read_b64_tr_b16 v[92:93], v205 offset:29184\n"
"ds_read_b64_tr_b16 v[94:95], v205 offset:31232\n"
"v_mul_f32 v20, v20, v202\n"
"v_mul_f32 v21, v21, v202\n"
"v_cvt_pk_bf16_f32 v18, v20, v21\n"
"v_mfma_f32_16x16x32_bf16 v[164:167], v[64:67], a[184:187], v[164:167]\n"
"s_waitcnt lgkmcnt(12)\n"
"ds_read_b64_tr_b16 v[96:97], v204 offset:32768\n"
"ds_read_b64_tr_b16 v[98:99], v204 offset:34816\n"
"v_accvgpr_read_b32 v22, a90\n"
"v_accvgpr_read_b32 v23, a91\n"
"v_mfma_f32_16x16x32_bf16 v[168:171], v[68:71], a[184:187], v[168:171]\n"
"s_waitcnt lgkmcnt(12)\n"
"ds_read_b64_tr_b16 v[100:101], v205 offset:32768\n"
"ds_read_b64_tr_b16 v[102:103], v205 offset:34816\n"
"v_mul_f32 v22, v22, v202\n"
"v_mul_f32 v23, v23, v202\n"
"v_cvt_pk_bf16_f32 v19, v22, v23\n"
"v_mfma_f32_16x16x32_bf16 v[172:175], v[72:75], a[184:187], v[172:175]\n"
"s_waitcnt lgkmcnt(12)\n"
"ds_read_b64_tr_b16 v[104:105], v204 offset:33280\n"
"ds_read_b64_tr_b16 v[106:107], v204 offset:35328\n"
"buffer_store_dwordx4 v[16:19], v251, s[40:43], s22 offen offset:2048 sc1\n"
"v_accvgpr_read_b32 v24, a76\n"
"v_accvgpr_read_b32 v25, a77\n"
"v_mfma_f32_16x16x32_bf16 v[176:179], v[76:79], a[184:187], v[176:179]\n"
"s_waitcnt vmcnt(48) lgkmcnt(12)\n"
"ds_read_b64_tr_b16 v[108:109], v205 offset:33280\n"
"ds_read_b64_tr_b16 v[110:111], v205 offset:35328\n"
"v_mul_f32 v24, v24, v203\n"
"v_mul_f32 v25, v25, v203\n"
"v_cvt_pk_bf16_f32 v24, v24, v25\n"
"v_mfma_f32_16x16x32_bf16 v[164:167], v[80:83], a[188:191], v[164:167]\n"
"s_waitcnt lgkmcnt(12)\n"
"ds_read_b64_tr_b16 v[64:65], v204 offset:36864\n"
"ds_read_b64_tr_b16 v[66:67], v204 offset:38912\n"
"v_accvgpr_read_b32 v26, a78\n"
"v_accvgpr_read_b32 v27, a79\n"
"v_mfma_f32_16x16x32_bf16 v[168:171], v[84:87], a[188:191], v[168:171]\n"
"s_waitcnt lgkmcnt(12)\n"
"ds_read_b64_tr_b16 v[68:69], v205 offset:36864\n"
"ds_read_b64_tr_b16 v[70:71], v205 offset:38912\n"
"v_mul_f32 v26, v26, v203\n"
"v_mul_f32 v27, v27, v203\n"
"v_cvt_pk_bf16_f32 v25, v26, v27\n"
"v_mfma_f32_16x16x32_bf16 v[172:175], v[88:91], a[188:191], v[172:175]\n"
"s_waitcnt lgkmcnt(12)\n"
"ds_read_b64_tr_b16 v[72:73], v204 offset:37376\n"
"ds_read_b64_tr_b16 v[74:75], v204 offset:39424\n"
"v_accvgpr_read_b32 v28, a92\n"
"v_accvgpr_read_b32 v29, a93\n"
"v_mfma_f32_16x16x32_bf16 v[176:179], v[92:95], a[188:191], v[176:179]\n"
"s_waitcnt lgkmcnt(12)\n"
"ds_read_b64_tr_b16 v[76:77], v205 offset:37376\n"
"ds_read_b64_tr_b16 v[78:79], v205 offset:39424\n"
"v_mul_f32 v28, v28, v203\n"
"v_mul_f32 v29, v29, v203\n"
"v_cvt_pk_bf16_f32 v26, v28, v29\n"
"v_mfma_f32_16x16x32_bf16 v[164:167], v[96:99], a[228:231], v[164:167]\n"
"s_waitcnt lgkmcnt(12)\n"
"ds_read_b64_tr_b16 v[80:81], v204 offset:40960\n"
"ds_read_b64_tr_b16 v[82:83], v204 offset:43008\n"
"v_accvgpr_read_b32 v30, a94\n"
"v_accvgpr_read_b32 v31, a95\n"
"v_mfma_f32_16x16x32_bf16 v[168:171], v[100:103], a[228:231], v[168:171]\n"
"s_waitcnt lgkmcnt(12)\n"
"ds_read_b64_tr_b16 v[84:85], v205 offset:40960\n"
"ds_read_b64_tr_b16 v[86:87], v205 offset:43008\n"
"v_mul_f32 v30, v30, v203\n"
"v_mul_f32 v31, v31, v203\n"
"v_cvt_pk_bf16_f32 v27, v30, v31\n"
"v_mfma_f32_16x16x32_bf16 v[172:175], v[104:107], a[228:231], v[172:175]\n"
"s_waitcnt lgkmcnt(12)\n"
"ds_read_b64_tr_b16 v[88:89], v204 offset:41472\n"
"ds_read_b64_tr_b16 v[90:91], v204 offset:43520\n"
"buffer_store_dwordx4 v[24:27], v251, s[40:43], s23 offen offset:2048 sc1\n"
"v_accvgpr_read_b32 v0, a96\n"
"v_accvgpr_read_b32 v1, a97\n"
"v_mfma_f32_16x16x32_bf16 v[176:179], v[108:111], a[228:231], v[176:179]\n"
"s_waitcnt lgkmcnt(12)\n"
"ds_read_b64_tr_b16 v[92:93], v205 offset:41472\n"
"ds_read_b64_tr_b16 v[94:95], v205 offset:43520\n"
"v_mul_f32 v0, v0, v200\n"
"v_mul_f32 v1, v1, v200\n"
"v_cvt_pk_bf16_f32 v0, v0, v1\n"
"v_mfma_f32_16x16x32_bf16 v[164:167], v[64:67], a[232:235], v[164:167]\n"
"s_waitcnt lgkmcnt(12)\n"
"ds_read_b64_tr_b16 v[96:97], v204 offset:45056\n"
"ds_read_b64_tr_b16 v[98:99], v204 offset:47104\n"
"v_accvgpr_read_b32 v2, a98\n"
"v_accvgpr_read_b32 v3, a99\n"
"v_mfma_f32_16x16x32_bf16 v[168:171], v[68:71], a[232:235], v[168:171]\n"
"s_waitcnt lgkmcnt(12)\n"
"ds_read_b64_tr_b16 v[100:101], v205 offset:45056\n"
"ds_read_b64_tr_b16 v[102:103], v205 offset:47104\n"
"v_mul_f32 v2, v2, v200\n"
"v_mul_f32 v3, v3, v200\n"
"v_cvt_pk_bf16_f32 v1, v2, v3\n"
"v_mfma_f32_16x16x32_bf16 v[172:175], v[72:75], a[232:235], v[172:175]\n"
"s_waitcnt lgkmcnt(12)\n"
"ds_read_b64_tr_b16 v[104:105], v204 offset:45568\n"
"ds_read_b64_tr_b16 v[106:107], v204 offset:47616\n"
"v_accvgpr_read_b32 v4, a112\n"
"v_accvgpr_read_b32 v5, a113\n"
"v_mfma_f32_16x16x32_bf16 v[176:179], v[76:79], a[232:235], v[176:179]\n"
"s_waitcnt lgkmcnt(12)\n"
"ds_read_b64_tr_b16 v[108:109], v205 offset:45568\n"
"ds_read_b64_tr_b16 v[110:111], v205 offset:47616\n"
"v_mul_f32 v4, v4, v200\n"
"v_mul_f32 v5, v5, v200\n"
"v_cvt_pk_bf16_f32 v2, v4, v5\n"
"v_mfma_f32_16x16x32_bf16 v[164:167], v[80:83], a[236:239], v[164:167]\n"
"s_waitcnt lgkmcnt(12)\n"
"ds_read_b64_tr_b16 v[64:65], v204 offset:49152\n"
"ds_read_b64_tr_b16 v[66:67], v204 offset:51200\n"
"v_accvgpr_read_b32 v6, a114\n"
"v_accvgpr_read_b32 v7, a115\n"
"v_mfma_f32_16x16x32_bf16 v[168:171], v[84:87], a[236:239], v[168:171]\n"
"s_waitcnt lgkmcnt(12)\n"
"ds_read_b64_tr_b16 v[68:69], v205 offset:49152\n"
"ds_read_b64_tr_b16 v[70:71], v205 offset:51200\n"
"v_mul_f32 v6, v6, v200\n"
"v_mul_f32 v7, v7, v200\n"
"v_cvt_pk_bf16_f32 v3, v6, v7\n"
"v_mfma_f32_16x16x32_bf16 v[172:175], v[88:91], a[236:239], v[172:175]\n"
"s_waitcnt lgkmcnt(12)\n"
"ds_read_b64_tr_b16 v[72:73], v204 offset:49664\n"
"ds_read_b64_tr_b16 v[74:75], v204 offset:51712\n"
"buffer_store_dwordx4 v[0:3], v251, s[40:43], s20 offen offset:3072 sc1\n"
"v_accvgpr_read_b32 v8, a100\n"
"v_accvgpr_read_b32 v9, a101\n"
"v_mfma_f32_16x16x32_bf16 v[176:179], v[92:95], a[236:239], v[176:179]\n"
"s_waitcnt lgkmcnt(12)\n"
"ds_read_b64_tr_b16 v[76:77], v205 offset:49664\n"
"ds_read_b64_tr_b16 v[78:79], v205 offset:51712\n"
"v_mul_f32 v8, v8, v201\n"
"v_mul_f32 v9, v9, v201\n"
"v_cvt_pk_bf16_f32 v8, v8, v9\n"
"v_mfma_f32_16x16x32_bf16 v[164:167], v[96:99], a[240:243], v[164:167]\n"
"s_waitcnt lgkmcnt(12)\n"
"ds_read_b64_tr_b16 v[80:81], v204 offset:53248\n"
"ds_read_b64_tr_b16 v[82:83], v204 offset:55296\n"
"v_accvgpr_read_b32 v10, a102\n"
"v_accvgpr_read_b32 v11, a103\n"
"v_mfma_f32_16x16x32_bf16 v[168:171], v[100:103], a[240:243], v[168:171]\n"
"s_waitcnt lgkmcnt(12)\n"
"ds_read_b64_tr_b16 v[84:85], v205 offset:53248\n"
"ds_read_b64_tr_b16 v[86:87], v205 offset:55296\n"
"v_mul_f32 v10, v10, v201\n"
"v_mul_f32 v11, v11, v201\n"
"v_cvt_pk_bf16_f32 v9, v10, v11\n"
"v_mfma_f32_16x16x32_bf16 v[172:175], v[104:107], a[240:243], v[172:175]\n"
"s_waitcnt lgkmcnt(12)\n"
"ds_read_b64_tr_b16 v[88:89], v204 offset:53760\n"
"ds_read_b64_tr_b16 v[90:91], v204 offset:55808\n"
"v_accvgpr_read_b32 v12, a116\n"
"v_accvgpr_read_b32 v13, a117\n"
"v_mov_b32 v210, s34\n"
"v_mov_b32 v211, s35\n"
"s_lshl_b32 s98, s37, 11\n"
"v_lshlrev_b32 v212, 4, v251\n"
"v_add_u32 v212, s98, v212\n"
"v_mfma_f32_16x16x32_bf16 v[176:179], v[108:111], a[240:243], v[176:179]\n"
"s_waitcnt lgkmcnt(12)\n"
"ds_read_b64_tr_b16 v[92:93], v205 offset:53760\n"
"ds_read_b64_tr_b16 v[94:95], v205 offset:55808\n"
"v_mul_f32 v12, v12, v201\n"
"v_mul_f32 v13, v13, v201\n"
"v_cvt_pk_bf16_f32 v10, v12, v13\n"
"v_mfma_f32_16x16x32_bf16 v[164:167], v[64:67], a[244:247], v[164:167]\n"
"s_waitcnt vmcnt(5)\n"
"s_barrier\n"
"s_cmp_lg_u32 s38, 0\n"
"s_cbranch_scc1 SG2_%=\n"
"s_mov_b32 exec_hi, 0\n"
"s_mov_b32 exec_lo, 0xff\n"
"buffer_store_dwordx2 v[210:211], v212, s[40:43], s31 offen sc1\n"
"s_mov_b64 exec, -1\n"
"SG2_%=:\n"
"s_add_u32 s31, s31, 8192\n"
"ds_read_b64_tr_b16 v[96:97], v204 offset:57344\n"
"ds_read_b64_tr_b16 v[98:99], v204 offset:59392\n"
"v_accvgpr_read_b32 v14, a118\n"
"v_accvgpr_read_b32 v15, a119\n"
"s_waitcnt lgkmcnt(14)\n"
"v_mfma_f32_16x16x32_bf16 v[168:171], v[68:71], a[244:247], v[168:171]\n"
"s_waitcnt lgkmcnt(12)\n"
"ds_read_b64_tr_b16 v[100:101], v205 offset:57344\n"
"ds_read_b64_tr_b16 v[102:103], v205 offset:59392\n"
"v_mul_f32 v14, v14, v201\n"
"v_mul_f32 v15, v15, v201\n"
"v_cvt_pk_bf16_f32 v11, v14, v15\n"
"v_mfma_f32_16x16x32_bf16 v[172:175], v[72:75], a[244:247], v[172:175]\n"
"s_waitcnt lgkmcnt(12)\n"
"ds_read_b64_tr_b16 v[104:105], v204 offset:57856\n"
"ds_read_b64_tr_b16 v[106:107], v204 offset:59904\n"
"buffer_store_dwordx4 v[8:11], v251, s[40:43], s21 offen offset:3072 sc1\n"
"v_accvgpr_read_b32 v16, a104\n"
"v_accvgpr_read_b32 v17, a105\n"
"v_and_b32 v207, 56, v207\n"
"s_lshl_b32 s99, s37, 6\n"
"v_add_u32 v207, s99, v207\n"
"v_mfma_f32_16x16x32_bf16 v[176:179], v[76:79], a[244:247], v[176:179]\n"
"s_waitcnt lgkmcnt(12)\n"
"ds_read_b64_tr_b16 v[108:109], v205 offset:57856\n"
"ds_read_b64_tr_b16 v[110:111], v205 offset:59904\n"
"v_mul_f32 v16, v16, v202\n"
"v_mul_f32 v17, v17, v202\n"
"v_cvt_pk_bf16_f32 v16, v16, v17\n"
"v_mfma_f32_16x16x32_bf16 v[164:167], v[80:83], a[248:251], v[164:167]\n"
"s_waitcnt lgkmcnt(12)\n"
"ds_read_b64_tr_b16 v[64:65], v204 offset:61440\n"
"ds_read_b64_tr_b16 v[66:67], v204 offset:63488\n"
"v_accvgpr_read_b32 v18, a106\n"
"v_accvgpr_read_b32 v19, a107\n"
"v_mfma_f32_16x16x32_bf16 v[168:171], v[84:87], a[248:251], v[168:171]\n"
"s_waitcnt lgkmcnt(12)\n"
"ds_read_b64_tr_b16 v[68:69], v205 offset:61440\n"
"ds_read_b64_tr_b16 v[70:71], v205 offset:63488\n"
"v_mul_f32 v18, v18, v202\n"
"v_mul_f32 v19, v19, v202\n"
"v_cvt_pk_bf16_f32 v17, v18, v19\n"
"v_mfma_f32_16x16x32_bf16 v[172:175], v[88:91], a[248:251], v[172:175]\n"
"s_waitcnt lgkmcnt(12)\n"
"ds_read_b64_tr_b16 v[72:73], v204 offset:61952\n"
"ds_read_b64_tr_b16 v[74:75], v204 offset:64000\n"
"v_accvgpr_read_b32 v20, a120\n"
"v_accvgpr_read_b32 v21, a121\n"
"v_mfma_f32_16x16x32_bf16 v[176:179], v[92:95], a[248:251], v[176:179]\n"
"s_waitcnt lgkmcnt(12)\n"
"ds_read_b64_tr_b16 v[76:77], v205 offset:61952\n"
"ds_read_b64_tr_b16 v[78:79], v205 offset:64000\n"
"v_mul_f32 v20, v20, v202\n"
"v_mul_f32 v21, v21, v202\n"
"v_cvt_pk_bf16_f32 v18, v20, v21\n"
"v_mfma_f32_16x16x32_bf16 v[164:167], v[96:99], a[252:255], v[164:167]\n"
"s_waitcnt lgkmcnt(12)\n"
"ds_read_b64_tr_b16 v[80:81], v241 offset:0\n"
"ds_read_b64_tr_b16 v[82:83], v241 offset:2048\n"
"v_accvgpr_read_b32 v22, a122\n"
"v_accvgpr_read_b32 v23, a123\n"
"v_mfma_f32_16x16x32_bf16 v[168:171], v[100:103], a[252:255], v[168:171]\n"
"s_waitcnt lgkmcnt(12)\n"
"ds_read_b64_tr_b16 v[84:85], v242 offset:0\n"
"ds_read_b64_tr_b16 v[86:87], v242 offset:2048\n"
"v_mul_f32 v22, v22, v202\n"
"v_mul_f32 v23, v23, v202\n"
"v_cvt_pk_bf16_f32 v19, v22, v23\n"
"v_mfma_f32_16x16x32_bf16 v[172:175], v[104:107], a[252:255], v[172:175]\n"
"s_waitcnt lgkmcnt(12)\n"
"ds_read_b64_tr_b16 v[88:89], v241 offset:512\n"
"ds_read_b64_tr_b16 v[90:91], v241 offset:2560\n"
"buffer_store_dwordx4 v[16:19], v251, s[40:43], s22 offen offset:3072 sc1\n"
"v_accvgpr_read_b32 v24, a108\n"
"v_accvgpr_read_b32 v25, a109\n"
"v_mfma_f32_16x16x32_bf16 v[176:179], v[108:111], a[252:255], v[176:179]\n"
"s_waitcnt lgkmcnt(12)\n"
"ds_read_b64_tr_b16 v[92:93], v242 offset:512\n"
"ds_read_b64_tr_b16 v[94:95], v242 offset:2560\n"
"v_mul_f32 v24, v24, v203\n"
"v_mul_f32 v25, v25, v203\n"
"v_cvt_pk_bf16_f32 v24, v24, v25\n"
"v_mfma_f32_16x16x32_bf16 v[164:167], v[64:67], v[160:163], v[164:167]\n"
"s_waitcnt lgkmcnt(12)\n"
"ds_read_b64_tr_b16 v[96:97], v241 offset:4096\n"
"ds_read_b64_tr_b16 v[98:99], v241 offset:6144\n"
"v_accvgpr_read_b32 v26, a110\n"
"v_accvgpr_read_b32 v27, a111\n"
"v_mfma_f32_16x16x32_bf16 v[168:171], v[68:71], v[160:163], v[168:171]\n"
"s_waitcnt lgkmcnt(12)\n"
"ds_read_b64_tr_b16 v[100:101], v242 offset:4096\n"
"ds_read_b64_tr_b16 v[102:103], v242 offset:6144\n"
"v_mul_f32 v26, v26, v203\n"
"v_mul_f32 v27, v27, v203\n"
"v_cvt_pk_bf16_f32 v25, v26, v27\n"
"v_mfma_f32_16x16x32_bf16 v[172:175], v[72:75], v[160:163], v[172:175]\n"
"s_waitcnt lgkmcnt(12)\n"
"ds_read_b64_tr_b16 v[104:105], v241 offset:4608\n"
"ds_read_b64_tr_b16 v[106:107], v241 offset:6656\n"
"v_accvgpr_read_b32 v28, a124\n"
"v_accvgpr_read_b32 v29, a125\n"
"v_mfma_f32_16x16x32_bf16 v[176:179], v[76:79], v[160:163], v[176:179]\n"
"s_waitcnt lgkmcnt(12)\n"
"ds_read_b64_tr_b16 v[108:109], v242 offset:4608\n"
"ds_read_b64_tr_b16 v[110:111], v242 offset:6656\n"
"v_mul_f32 v28, v28, v203\n"
"v_mul_f32 v29, v29, v203\n"
"v_cvt_pk_bf16_f32 v26, v28, v29\n"
"v_mfma_f32_16x16x32_bf16 v[164:167], v[80:83], v[32:35], v[164:167]\n"
"s_waitcnt lgkmcnt(12)\n"
"ds_read_b64_tr_b16 v[64:65], v241 offset:8192\n"
"ds_read_b64_tr_b16 v[66:67], v241 offset:10240\n"
"v_accvgpr_read_b32 v30, a126\n"
"v_accvgpr_read_b32 v31, a127\n"
"v_mfma_f32_16x16x32_bf16 v[168:171], v[84:87], v[32:35], v[168:171]\n"
"s_waitcnt lgkmcnt(12)\n"
"ds_read_b64_tr_b16 v[68:69], v242 offset:8192\n"
"ds_read_b64_tr_b16 v[70:71], v242 offset:10240\n"
"v_mul_f32 v30, v30, v203\n"
"v_mul_f32 v31, v31, v203\n"
"v_cvt_pk_bf16_f32 v27, v30, v31\n"
"v_mfma_f32_16x16x32_bf16 v[172:175], v[88:91], v[32:35], v[172:175]\n"
"s_waitcnt lgkmcnt(12)\n"
"ds_read_b64_tr_b16 v[72:73], v241 offset:8704\n"
"ds_read_b64_tr_b16 v[74:75], v241 offset:10752\n"
"buffer_store_dwordx4 v[24:27], v251, s[40:43], s23 offen offset:3072 sc1\n"
"v_mfma_f32_16x16x32_bf16 v[176:179], v[92:95], v[32:35], v[176:179]\n"
"s_waitcnt lgkmcnt(12)\n"
"ds_read_b64_tr_b16 v[76:77], v242 offset:8704\n"
"ds_read_b64_tr_b16 v[78:79], v242 offset:10752\n"
"v_mfma_f32_16x16x32_bf16 v[164:167], v[96:99], v[36:39], v[164:167]\n"
"s_waitcnt lgkmcnt(12)\n"
"ds_read_b64_tr_b16 v[80:81], v241 offset:12288\n"
"ds_read_b64_tr_b16 v[82:83], v241 offset:14336\n"
"v_mfma_f32_16x16x32_bf16 v[168:171], v[100:103], v[36:39], v[168:171]\n"
"s_waitcnt lgkmcnt(12)\n"
"ds_read_b64_tr_b16 v[84:85], v242 offset:12288\n"
"ds_read_b64_tr_b16 v[86:87], v242 offset:14336\n"
"v_mfma_f32_16x16x32_bf16 v[172:175], v[104:107], v[36:39], v[172:175]\n"
"s_waitcnt lgkmcnt(12)\n"
"ds_read_b64_tr_b16 v[88:89], v241 offset:12800\n"
"ds_read_b64_tr_b16 v[90:91], v241 offset:14848\n"
"v_mfma_f32_16x16x32_bf16 v[176:179], v[108:111], v[36:39], v[176:179]\n"
"s_waitcnt lgkmcnt(12)\n"
"ds_read_b64_tr_b16 v[92:93], v242 offset:12800\n"
"ds_read_b64_tr_b16 v[94:95], v242 offset:14848\n"
"v_mfma_f32_16x16x32_bf16 v[164:167], v[64:67], v[40:43], v[164:167]\n"
"s_waitcnt lgkmcnt(12)\n"
"ds_read_b64_tr_b16 v[96:97], v241 offset:16384\n"
"ds_read_b64_tr_b16 v[98:99], v241 offset:18432\n"
"v_mfma_f32_16x16x32_bf16 v[168:171], v[68:71], v[40:43], v[168:171]\n"
"s_waitcnt lgkmcnt(12)\n"
"ds_read_b64_tr_b16 v[100:101], v242 offset:16384\n"
"ds_read_b64_tr_b16 v[102:103], v242 offset:18432\n"
"v_mfma_f32_16x16x32_bf16 v[172:175], v[72:75], v[40:43], v[172:175]\n"
"s_waitcnt lgkmcnt(12)\n"
"ds_read_b64_tr_b16 v[104:105], v241 offset:16896\n"
"ds_read_b64_tr_b16 v[106:107], v241 offset:18944\n"
"v_mfma_f32_16x16x32_bf16 v[176:179], v[76:79], v[40:43], v[176:179]\n"
"s_waitcnt lgkmcnt(12)\n"
"ds_read_b64_tr_b16 v[108:109], v242 offset:16896\n"
"ds_read_b64_tr_b16 v[110:111], v242 offset:18944\n"
"v_mfma_f32_16x16x32_bf16 v[164:167], v[80:83], v[44:47], v[164:167]\n"
"s_waitcnt lgkmcnt(12)\n"
"ds_read_b64_tr_b16 v[64:65], v241 offset:20480\n"
"ds_read_b64_tr_b16 v[66:67], v241 offset:22528\n"
"v_mfma_f32_16x16x32_bf16 v[168:171], v[84:87], v[44:47], v[168:171]\n"
"s_waitcnt lgkmcnt(12)\n"
"ds_read_b64_tr_b16 v[68:69], v242 offset:20480\n"
"ds_read_b64_tr_b16 v[70:71], v242 offset:22528\n"
"v_mfma_f32_16x16x32_bf16 v[172:175], v[88:91], v[44:47], v[172:175]\n"
"s_waitcnt lgkmcnt(12)\n"
"ds_read_b64_tr_b16 v[72:73], v241 offset:20992\n"
"ds_read_b64_tr_b16 v[74:75], v241 offset:23040\n"
"v_mfma_f32_16x16x32_bf16 v[176:179], v[92:95], v[44:47], v[176:179]\n"
"s_waitcnt lgkmcnt(12)\n"
"ds_read_b64_tr_b16 v[76:77], v242 offset:20992\n"
"ds_read_b64_tr_b16 v[78:79], v242 offset:23040\n"
"v_mfma_f32_16x16x32_bf16 v[164:167], v[96:99], v[48:51], v[164:167]\n"
"s_waitcnt lgkmcnt(12)\n"
"ds_read_b64_tr_b16 v[80:81], v241 offset:24576\n"
"ds_read_b64_tr_b16 v[82:83], v241 offset:26624\n"
"v_mfma_f32_16x16x32_bf16 v[168:171], v[100:103], v[48:51], v[168:171]\n"
"s_waitcnt lgkmcnt(12)\n"
"ds_read_b64_tr_b16 v[84:85], v242 offset:24576\n"
"ds_read_b64_tr_b16 v[86:87], v242 offset:26624\n"
"v_mfma_f32_16x16x32_bf16 v[172:175], v[104:107], v[48:51], v[172:175]\n"
"s_waitcnt lgkmcnt(12)\n"
"ds_read_b64_tr_b16 v[88:89], v241 offset:25088\n"
"ds_read_b64_tr_b16 v[90:91], v241 offset:27136\n"
"v_mfma_f32_16x16x32_bf16 v[176:179], v[108:111], v[48:51], v[176:179]\n"
"s_waitcnt lgkmcnt(12)\n"
"ds_read_b64_tr_b16 v[92:93], v242 offset:25088\n"
"ds_read_b64_tr_b16 v[94:95], v242 offset:27136\n"
"v_mfma_f32_16x16x32_bf16 v[164:167], v[64:67], v[52:55], v[164:167]\n"
"s_waitcnt lgkmcnt(12)\n"
"ds_read_b64_tr_b16 v[96:97], v241 offset:28672\n"
"ds_read_b64_tr_b16 v[98:99], v241 offset:30720\n"
"v_mfma_f32_16x16x32_bf16 v[168:171], v[68:71], v[52:55], v[168:171]\n"
"s_waitcnt lgkmcnt(12)\n"
"ds_read_b64_tr_b16 v[100:101], v242 offset:28672\n"
"ds_read_b64_tr_b16 v[102:103], v242 offset:30720\n"
"v_mfma_f32_16x16x32_bf16 v[172:175], v[72:75], v[52:55], v[172:175]\n"
"s_waitcnt lgkmcnt(12)\n"
"ds_read_b64_tr_b16 v[104:105], v241 offset:29184\n"
"ds_read_b64_tr_b16 v[106:107], v241 offset:31232\n"
"v_mfma_f32_16x16x32_bf16 v[176:179], v[76:79], v[52:55], v[176:179]\n"
"s_waitcnt lgkmcnt(12)\n"
"ds_read_b64_tr_b16 v[108:109], v242 offset:29184\n"
"ds_read_b64_tr_b16 v[110:111], v242 offset:31232\n"
"v_mfma_f32_16x16x32_bf16 v[164:167], v[80:83], v[56:59], v[164:167]\n"
"s_waitcnt lgkmcnt(12)\n"
"v_mfma_f32_16x16x32_bf16 v[168:171], v[84:87], v[56:59], v[168:171]\n"
"s_waitcnt lgkmcnt(10)\n"
"v_mfma_f32_16x16x32_bf16 v[172:175], v[88:91], v[56:59], v[172:175]\n"
"s_waitcnt lgkmcnt(8)\n"
"v_mfma_f32_16x16x32_bf16 v[176:179], v[92:95], v[56:59], v[176:179]\n"
"s_waitcnt lgkmcnt(6)\n"
"v_mfma_f32_16x16x32_bf16 v[164:167], v[96:99], v[60:63], v[164:167]\n"
"s_waitcnt lgkmcnt(4)\n"
"v_mfma_f32_16x16x32_bf16 v[168:171], v[100:103], v[60:63], v[168:171]\n"
"s_waitcnt lgkmcnt(2)\n"
"v_mfma_f32_16x16x32_bf16 v[172:175], v[104:107], v[60:63], v[172:175]\n"
"s_waitcnt lgkmcnt(0)\n"
"v_mfma_f32_16x16x32_bf16 v[176:179], v[108:111], v[60:63], v[176:179]\n"
"buffer_load_dwordx2 v[214:215], v207, s[40:43], s30 offen sc1\n"
"s_xor_b32 s98, s37, 1\n"
"s_lshl_b32 s99, s37, 2\n"
"s_add_u32 s99, s99, s98\n"
"s_lshl_b32 s99, s99, 15\n"
"s_add_u32 s20, s99, s9\n"
"s_lshl_b32 s99, s98, 2\n"
"s_add_u32 s99, s99, s37\n"
"s_lshl_b32 s99, s99, 15\n"
"s_add_u32 s88, s99, s9\n"
"s_xor_b32 s98, s37, 2\n"
"s_lshl_b32 s99, s37, 2\n"
"s_add_u32 s99, s99, s98\n"
"s_lshl_b32 s99, s99, 15\n"
"s_add_u32 s21, s99, s9\n"
"s_lshl_b32 s99, s98, 2\n"
"s_add_u32 s99, s99, s37\n"
"s_lshl_b32 s99, s99, 15\n"
"s_add_u32 s89, s99, s9\n"
"s_xor_b32 s98, s37, 3\n"
"s_lshl_b32 s99, s37, 2\n"
"s_add_u32 s99, s99, s98\n"
"s_lshl_b32 s99, s99, 15\n"
"s_add_u32 s22, s99, s9\n"
"s_lshl_b32 s99, s98, 2\n"
"s_add_u32 s99, s99, s37\n"
"s_lshl_b32 s99, s99, 15\n"
"s_add_u32 s90, s99, s9\n"
"v_mov_b32 v210, s34\n"
"v_mov_b32 v211, s35\n"
"s_lshl_b32 s98, s37, 11\n"
"v_lshlrev_b32 v212, 4, v251\n"
"v_add_u32 v212, s98, v212\n"
"s_waitcnt vmcnt(0)\n"
"v_xor_b32 v215, v214, v215\n"
"v_xor_b32 v214, 0x9e3779b9, v214\n"
"v_cmp_eq_u32 vcc, 0xe17d05ac, v215\n"
"v_subrev_u32 v214, s36, v214\n"
"v_cmp_gt_u32 s[98:99], 63, v214\n"
"s_nop 0\n"
"s_and_b64 vcc, vcc, s[98:99]\n"
"s_cmp_eq_u64 vcc, -1\n"
"s_cbranch_scc0 RP_%=\n"
"RPr_%=:\n"
"s_add_u32 s30, s30, 8192\n"
"buffer_load_dwordx4 a[0:3], v251, s[40:43], s7 offen sc1\n"
"s_add_u32 s7, s7, 4096\n"
"buffer_load_dwordx4 a[4:7], v251, s[40:43], s7 offen sc1\n"
"s_add_u32 s7, s7, 4096\n"
"buffer_load_dwordx4 a[8:11], v251, s[40:43], s7 offen sc1\n"
"s_add_u32 s7, s7, 4096\n"
"buffer_load_dwordx4 a[12:15], v251, s[40:43], s7 offen sc1\n"
"s_add_u32 s7, s7, 4096\n"
"buffer_load_dwordx4 a[16:19], v251, s[40:43], s8 offen sc1\n"
"s_add_u32 s8, s8, 4096\n"
"buffer_load_dwordx4 a[20:23], v251, s[40:43], s8 offen sc1\n"
"s_add_u32 s8, s8, 4096\n"
"buffer_load_dwordx4 a[24:27], v251, s[40:43], s8 offen sc1\n"
"s_add_u32 s8, s8, 4096\n"
"buffer_load_dwordx4 a[28:31], v251, s[40:43], s8 offen sc1\n"
"s_add_u32 s8, s8, 4096\n"
"s_barrier\n"
"s_cmp_lg_u32 s38, 0\n"
"s_cbranch_scc1 SG3_%=\n"
"s_mov_b32 exec_hi, 0\n"
"s_mov_b32 exec_lo, 0xff\n"
"buffer_store_dwordx2 v[210:211], v212, s[40:43], s31 offen sc1\n"
"s_mov_b64 exec, -1\n"
"SG3_%=:\n"
"s_add_u32 s31, s31, 8192\n"
"ds_read_b64_tr_b16 a[128:129], v208 offset:0\n"
"ds_read_b64_tr_b16 a[130:131], v208 offset:4096\n"
"ds_read_b64_tr_b16 a[132:133], v208 offset:8192\n"
"ds_read_b64_tr_b16 a[134:135], v208 offset:12288\n"
"ds_read_b64_tr_b16 a[136:137], v208 offset:16384\n"
"ds_read_b64_tr_b16 a[138:139], v208 offset:20480\n"
"ds_read_b64_tr_b16 a[140:141], v208 offset:24576\n"
"ds_read_b64_tr_b16 a[142:143], v208 offset:28672\n"
"ds_read_b64_tr_b16 a[144:145], v209 offset:0\n"
"ds_read_b64_tr_b16 a[146:147], v209 offset:4096\n"
"ds_read_b64_tr_b16 a[148:149], v209 offset:8192\n"
"ds_read_b64_tr_b16 a[150:151], v209 offset:12288\n"
"ds_read_b64_tr_b16 a[152:153], v209 offset:16384\n"
"ds_read_b64_tr_b16 a[154:155], v209 offset:20480\n"
"ds_read_b64_tr_b16 a[156:157], v209 offset:24576\n"
"ds_read_b64_tr_b16 a[158:159], v209 offset:28672\n"
"s_waitcnt vmcnt(7) lgkmcnt(14)\n"
"v_mfma_f32_16x16x32_bf16 v[0:3], a[128:131], a[0:3], 0\n"
"s_waitcnt vmcnt(6) lgkmcnt(12)\n"
"s_add_u32 m0, s86, 147456\n"
"ds_read_b64_tr_b16 a[192:193], v208 offset:512\n"
"v_mfma_f32_16x16x32_bf16 v[0:3], a[132:135], a[4:7], v[0:3]\n"
"s_waitcnt vmcnt(5) lgkmcnt(11)\n"
"buffer_load_dwordx4 v247, s[64:67], s85 offen lds\n"
"v_mfma_f32_16x16x32_bf16 v[0:3], a[136:139], a[8:11], v[0:3]\n"
"s_waitcnt vmcnt(5) lgkmcnt(9)\n"
"ds_read_b64_tr_b16 a[194:195], v208 offset:4608\n"
"ds_read_b64_tr_b16 a[196:197], v208 offset:8704\n"
"v_mfma_f32_16x16x32_bf16 v[0:3], a[140:143], a[12:15], v[0:3]\n"
"s_waitcnt vmcnt(4)\n"
"s_add_u32 m0, s86, 148480\n"
"ds_read_b64_tr_b16 a[198:199], v208 offset:12800\n"
"v_mfma_f32_16x16x32_bf16 v[32:35], a[128:131], a[16:19], 0\n"
"s_waitcnt vmcnt(3)\n"
"buffer_load_dwordx4 v248, s[64:67], s85 offen lds\n"
"v_mfma_f32_16x16x32_bf16 v[32:35], a[132:135], a[20:23], v[32:35]\n"
"s_waitcnt vmcnt(3)\n"
"s_add_u32 s85, s85, 131072\n"
"ds_read_b64_tr_b16 a[200:201], v208 offset:16896\n"
"ds_read_b64_tr_b16 a[202:203], v208 offset:20992\n"
"v_mfma_f32_16x16x32_bf16 v[32:35], a[136:139], a[24:27], v[32:35]\n"
"s_waitcnt vmcnt(2)\n"
"s_add_u32 m0, s86, 155648\n"
"ds_read_b64_tr_b16 a[204:205], v208 offset:25088\n"
"ds_read_b64_tr_b16 a[206:207], v208 offset:29184\n"
"v_mfma_f32_16x16x32_bf16 v[32:35], a[140:143], a[28:31], v[32:35]\n"
"s_waitcnt lgkmcnt(14)\n"
"buffer_load_dwordx4 v247, s[64:67], s85 offen lds\n"
"v_mfma_f32_16x16x32_bf16 v[4:7], a[144:147], a[0:3], 0\n"
"s_waitcnt lgkmcnt(12)\n"
"ds_read_b64_tr_b16 a[208:209], v209 offset:512\n"
"v_mfma_f32_16x16x32_bf16 v[4:7], a[148:151], a[4:7], v[4:7]\n"
"s_waitcnt lgkmcnt(11)\n"
"s_add_u32 m0, s86, 156672\n"
"ds_read_b64_tr_b16 a[210:211], v209 offset:4608\n"
"ds_read_b64_tr_b16 a[212:213], v209 offset:8704\n"
"v_mfma_f32_16x16x32_bf16 v[4:7], a[152:155], a[8:11], v[4:7]\n"
"s_waitcnt lgkmcnt(11)\n"
"buffer_load_dwordx4 v248, s[64:67], s85 offen lds\n"
"v_mfma_f32_16x16x32_bf16 v[4:7], a[156:159], a[12:15], v[4:7]\n"
"ds_read_b64_tr_b16 a[214:215], v209 offset:12800\n"
"v_mfma_f32_16x16x32_bf16 v[36:39], a[144:147], a[16:19], 0\n"
"s_sub_u32 s85, s85, 884736\n"
"ds_read_b64_tr_b16 a[216:217], v209 offset:16896\n"
"v_mfma_f32_16x16x32_bf16 v[36:39], a[148:151], a[20:23], v[36:39]\n"
"buffer_load_dwordx4 a[32:35], v251, s[40:43], s7 offen sc1\n"
"v_mfma_f32_16x16x32_bf16 v[36:39], a[152:155], a[24:27], v[36:39]\n"
"ds_read_b64_tr_b16 a[218:219], v209 offset:20992\n"
"ds_read_b64_tr_b16 a[220:221], v209 offset:25088\n"
"v_mfma_f32_16x16x32_bf16 v[36:39], a[156:159], a[28:31], v[36:39]\n"
"s_waitcnt lgkmcnt(13)\n"
"s_add_u32 s7, s7, 4096\n"
"ds_read_b64_tr_b16 a[222:223], v209 offset:29184\n"
"v_mfma_f32_16x16x32_bf16 v[8:11], a[192:195], a[0:3], 0\n"
"s_waitcnt lgkmcnt(12)\n"
"buffer_load_dwordx4 a[36:39], v251, s[40:43], s7 offen sc1\n"
"v_mfma_f32_16x16x32_bf16 v[8:11], a[196:199], a[4:7], v[8:11]\n"
"s_waitcnt lgkmcnt(10)\n"
"ds_read_b64_tr_b16 a[128:129], v208 offset:1024\n"
"v_mfma_f32_16x16x32_bf16 v[8:11], a[200:203], a[8:11], v[8:11]\n"
"s_waitcnt lgkmcnt(9)\n"
"s_add_u32 s7, s7, 4096\n"
"ds_read_b64_tr_b16 a[130:131], v208 offset:5120\n"
"ds_read_b64_tr_b16 a[132:133], v208 offset:9216\n"
"v_mfma_f32_16x16x32_bf16 v[8:11], a[204:207], a[12:15], v[8:11]\n"
"buffer_load_dwordx4 a[40:43], v251, s[40:43], s7 offen sc1\n"
"v_mfma_f32_16x16x32_bf16 v[40:43], a[192:195], a[16:19], 0\n"
"ds_read_b64_tr_b16 a[134:135], v208 offset:13312\n"
"v_mfma_f32_16x16x32_bf16 v[40:43], a[196:199], a[20:23], v[40:43]\n"
"s_add_u32 s7, s7, 4096\n"
"ds_read_b64_tr_b16 a[136:137], v208 offset:17408\n"
"ds_read_b64_tr_b16 a[138:139], v208 offset:21504\n"
"v_mfma_f32_16x16x32_bf16 v[40:43], a[200:203], a[24:27], v[40:43]\n"
"buffer_load_dwordx4 a[44:47], v251, s[40:43], s7 offen sc1\n"
"v_mfma_f32_16x16x32_bf16 v[40:43], a[204:207], a[28:31], v[40:43]\n"
"s_waitcnt lgkmcnt(12)\n"
"ds_read_b64_tr_b16 a[140:141], v208 offset:25600\n"
"ds_read_b64_tr_b16 a[142:143], v208 offset:29696\n"
"v_mfma_f32_16x16x32_bf16 v[12:15], a[208:211], a[0:3], 0\n"
"s_waitcnt lgkmcnt(12)\n"
"s_sub_u32 s7, s7, 28672\n"
"ds_read_b64_tr_b16 a[144:145], v209 offset:1024\n"
"v_mfma_f32_16x16x32_bf16 v[12:15], a[212:215], a[4:7], v[12:15]\n"
"s_waitcnt lgkmcnt(11)\n"
"buffer_load_dwordx4 a[48:51], v251, s[40:43], s8 offen sc1\n"
"v_mfma_f32_16x16x32_bf16 v[12:15], a[216:219], a[8:11], v[12:15]\n"
"s_waitcnt lgkmcnt(9)\n"
"ds_read_b64_tr_b16 a[146:147], v209 offset:5120\n"
"ds_read_b64_tr_b16 a[148:149], v209 offset:9216\n"
"v_mfma_f32_16x16x32_bf16 v[12:15], a[220:223], a[12:15], v[12:15]\n"
"s_add_u32 s8, s8, 4096\n"
"ds_read_b64_tr_b16 a[150:151], v209 offset:13312\n"
"v_mfma_f32_16x16x32_bf16 v[44:47], a[208:211], a[16:19], 0\n"
"buffer_load_dwordx4 a[52:55], v251, s[40:43], s8 offen sc1\n"
"v_mfma_f32_16x16x32_bf16 v[44:47], a[212:215], a[20:23], v[44:47]\n"
"ds_read_b64_tr_b16 a[152:153], v209 offset:17408\n"
"ds_read_b64_tr_b16 a[154:155], v209 offset:21504\n"
"v_mfma_f32_16x16x32_bf16 v[44:47], a[216:219], a[24:27], v[44:47]\n"
"s_add_u32 s8, s8, 4096\n"
"ds_read_b64_tr_b16 a[156:157], v209 offset:25600\n"
"ds_read_b64_tr_b16 a[158:159], v209 offset:29696\n"
"v_mfma_f32_16x16x32_bf16 v[44:47], a[220:223], a[28:31], v[44:47]\n"
"s_waitcnt lgkmcnt(14)\n"
"buffer_load_dwordx4 a[56:59], v251, s[40:43], s8 offen sc1\n"
"v_mfma_f32_16x16x32_bf16 v[16:19], a[128:131], a[0:3], 0\n"
"s_waitcnt lgkmcnt(12)\n"
"ds_read_b64_tr_b16 a[192:193], v208 offset:1536\n"
"v_mfma_f32_16x16x32_bf16 v[16:19], a[132:135], a[4:7], v[16:19]\n"
"s_waitcnt lgkmcnt(11)\n"
"s_add_u32 s8, s8, 4096\n"
"ds_read_b64_tr_b16 a[194:195], v208 offset:5632\n"
"ds_read_b64_tr_b16 a[196:197], v208 offset:9728\n"
"v_mfma_f32_16x16x32_bf16 v[16:19], a[136:139], a[8:11], v[16:19]\n"
"s_waitcnt lgkmcnt(11)\n"
"buffer_load_dwordx4 a[60:63], v251, s[40:43], s8 offen sc1\n"
"v_mfma_f32_16x16x32_bf16 v[16:19], a[140:143], a[12:15], v[16:19]\n"
"s_sub_u32 s8, s8, 28672\n"
"ds_read_b64_tr_b16 a[198:199], v208 offset:13824\n"
"v_mfma_f32_16x16x32_bf16 v[48:51], a[128:131], a[16:19], 0\n"
"s_add_u32 m0, s86, 0\n"
"ds_read_b64_tr_b16 a[200:201], v208 offset:17920\n"
"v_mfma_f32_16x16x32_bf16 v[48:51], a[132:135], a[20:23], v[48:51]\n"
"buffer_load_dwordx4 v247, s[64:67], s85 offen lds\n"
"v_mfma_f32_16x16x32_bf16 v[48:51], a[136:139], a[24:27], v[48:51]\n"
"ds_read_b64_tr_b16 a[202:203], v208 offset:22016\n"
"ds_read_b64_tr_b16 a[204:205], v208 offset:26112\n"
"v_mfma_f32_16x16x32_bf16 v[48:51], a[140:143], a[28:31], v[48:51]\n"
"s_waitcnt lgkmcnt(13)\n"
"s_add_u32 m0, s86, 1024\n"
"ds_read_b64_tr_b16 a[206:207], v208 offset:30208\n"
"v_mfma_f32_16x16x32_bf16 v[20:23], a[144:147], a[0:3], 0\n"
"s_waitcnt lgkmcnt(12)\n"
"buffer_load_dwordx4 v248, s[64:67], s85 offen lds\n"
"v_mfma_f32_16x16x32_bf16 v[20:23], a[148:151], a[4:7], v[20:23]\n"
"s_waitcnt lgkmcnt(10)\n"
"s_add_u32 s85, s85, 131072\n"
"ds_read_b64_tr_b16 a[208:209], v209 offset:1536\n"
"v_mfma_f32_16x16x32_bf16 v[20:23], a[152:155], a[8:11], v[20:23]\n"
"s_waitcnt lgkmcnt(9)\n"
"s_add_u32 m0, s86, 8192\n"
"ds_read_b64_tr_b16 a[210:211], v209 offset:5632\n"
"ds_read_b64_tr_b16 a[212:213], v209 offset:9728\n"
"v_mfma_f32_16x16x32_bf16 v[20:23], a[156:159], a[12:15], v[20:23]\n"
"buffer_load_dwordx4 v247, s[64:67], s85 offen lds\n"
"v_mfma_f32_16x16x32_bf16 v[52:55], a[144:147], a[16:19], 0\n"
"ds_read_b64_tr_b16 a[214:215], v209 offset:13824\n"
"v_mfma_f32_16x16x32_bf16 v[52:55], a[148:151], a[20:23], v[52:55]\n"
"s_add_u32 m0, s86, 9216\n"
"ds_read_b64_tr_b16 a[216:217], v209 offset:17920\n"
"ds_read_b64_tr_b16 a[218:219], v209 offset:22016\n"
"v_mfma_f32_16x16x32_bf16 v[52:55], a[152:155], a[24:27], v[52:55]\n"
"buffer_load_dwordx4 v248, s[64:67], s85 offen lds\n"
"v_mfma_f32_16x16x32_bf16 v[52:55], a[156:159], a[28:31], v[52:55]\n"
"s_waitcnt lgkmcnt(12)\n"
"s_add_u32 s85, s85, 131072\n"
"ds_read_b64_tr_b16 a[220:221], v209 offset:26112\n"
"ds_read_b64_tr_b16 a[222:223], v209 offset:30208\n"
"v_add_u32 v208, 32768, v208\n"
"v_add_u32 v209, 32768, v209\n"
"v_mfma_f32_16x16x32_bf16 v[24:27], a[192:195], a[0:3], 0\n"
"s_waitcnt vmcnt(12)\n"
"s_barrier\n"
"s_add_u32 m0, s86, 16384\n"
"ds_read_b64_tr_b16 a[128:129], v208 offset:0\n"
"s_waitcnt lgkmcnt(13)\n"
"v_mfma_f32_16x16x32_bf16 v[24:27], a[196:199], a[4:7], v[24:27]\n"
"s_waitcnt lgkmcnt(11)\n"
"buffer_load_dwordx4 v247, s[64:67], s85 offen lds\n"
"v_mfma_f32_16x16x32_bf16 v[24:27], a[200:203], a[8:11], v[24:27]\n"
"s_waitcnt lgkmcnt(9)\n"
"ds_read_b64_tr_b16 a[130:131], v208 offset:4096\n"
"ds_read_b64_tr_b16 a[132:133], v208 offset:8192\n"
"v_mfma_f32_16x16x32_bf16 v[24:27], a[204:207], a[12:15], v[24:27]\n"
"s_add_u32 m0, s86, 17408\n"
"ds_read_b64_tr_b16 a[134:135], v208 offset:12288\n"
"v_mfma_f32_16x16x32_bf16 v[56:59], a[192:195], a[16:19], 0\n"
"buffer_load_dwordx4 v248, s[64:67], s85 offen lds\n"
"v_mfma_f32_16x16x32_bf16 v[56:59], a[196:199], a[20:23], v[56:59]\n"
"s_add_u32 s85, s85, 131072\n"
"ds_read_b64_tr_b16 a[136:137], v208 offset:16384\n"
"ds_read_b64_tr_b16 a[138:139], v208 offset:20480\n"
"v_mfma_f32_16x16x32_bf16 v[56:59], a[200:203], a[24:27], v[56:59]\n"
"s_add_u32 m0, s86, 24576\n"
"ds_read_b64_tr_b16 a[140:141], v208 offset:24576\n"
"ds_read_b64_tr_b16 a[142:143], v208 offset:28672\n"
"v_mfma_f32_16x16x32_bf16 v[56:59], a[204:207], a[28:31], v[56:59]\n"
"s_waitcnt lgkmcnt(14)\n"
"buffer_load_dwordx4 v247, s[64:67], s85 offen lds\n"
"v_mfma_f32_16x16x32_bf16 v[28:31], a[208:211], a[0:3], 0\n"
"s_waitcnt lgkmcnt(12)\n"
"ds_read_b64_tr_b16 a[144:145], v209 offset:0\n"
"v_mfma_f32_16x16x32_bf16 v[28:31], a[212:215], a[4:7], v[28:31]\n"
"s_waitcnt lgkmcnt(11)\n"
"s_add_u32 m0, s86, 25600\n"
"ds_read_b64_tr_b16 a[146:147], v209 offset:4096\n"
"ds_read_b64_tr_b16 a[148:149], v209 offset:8192\n"
"v_mfma_f32_16x16x32_bf16 v[28:31], a[216:219], a[8:11], v[28:31]\n"
"s_waitcnt lgkmcnt(11)\n"
"buffer_load_dwordx4 v248, s[64:67], s85 offen lds\n"
"v_mfma_f32_16x16x32_bf16 v[28:31], a[220:223], a[12:15], v[28:31]\n"
"s_add_u32 s85, s85, 131072\n"
"ds_read_b64_tr_b16 a[150:151], v209 offset:12288\n"
"v_mfma_f32_16x16x32_bf16 v[60:63], a[208:211], a[16:19], 0\n"
"ds_read_b64_tr_b16 a[152:153], v209 offset:16384\n"
"v_mfma_f32_16x16x32_bf16 v[60:63], a[212:215], a[20:23], v[60:63]\n"
"ds_read_b64_tr_b16 a[154:155], v209 offset:20480\n"
"v_mfma_f32_16x16x32_bf16 v[60:63], a[216:219], a[24:27], v[60:63]\n"
"ds_read_b64_tr_b16 a[156:157], v209 offset:24576\n"
"v_mfma_f32_16x16x32_bf16 v[60:63], a[220:223], a[28:31], v[60:63]\n"
"ds_read_b64_tr_b16 a[158:159], v209 offset:28672\n"
"s_waitcnt vmcnt(15) lgkmcnt(14)\n"
"v_mfma_f32_16x16x32_bf16 v[0:3], a[128:131], a[32:35], v[0:3]\n"
"s_waitcnt vmcnt(14) lgkmcnt(12)\n"
"ds_read_b64_tr_b16 a[192:193], v208 offset:512\n"
"v_mfma_f32_16x16x32_bf16 v[0:3], a[132:135], a[36:39], v[0:3]\n"
"s_waitcnt vmcnt(13) lgkmcnt(11)\n"
"ds_read_b64_tr_b16 a[194:195], v208 offset:4608\n"
"ds_read_b64_tr_b16 a[196:197], v208 offset:8704\n"
"v_mfma_f32_16x16x32_bf16 v[0:3], a[136:139], a[40:43], v[0:3]\n"
"s_waitcnt vmcnt(12) lgkmcnt(11)\n"
"ds_read_b64_tr_b16 a[198:199], v208 offset:12800\n"
"v_mfma_f32_16x16x32_bf16 v[0:3], a[140:143], a[44:47], v[0:3]\n"
"s_waitcnt vmcnt(11)\n"
"buffer_load_dwordx2 v[214:215], v207, s[40:43], s30 offen sc1\n"
"v_mfma_f32_16x16x32_bf16 v[32:35], a[128:131], a[48:51], v[32:35]\n"
"s_waitcnt vmcnt(11)\n"
"ds_read_b64_tr_b16 a[200:201], v208 offset:16896\n"
"v_mfma_f32_16x16x32_bf16 v[32:35], a[132:135], a[52:55], v[32:35]\n"
"s_waitcnt vmcnt(10)\n"
"ds_read_b64_tr_b16 a[202:203], v208 offset:20992\n"
"ds_read_b64_tr_b16 a[204:205], v208 offset:25088\n"
"v_mfma_f32_16x16x32_bf16 v[32:35], a[136:139], a[56:59], v[32:35]\n"
"s_waitcnt vmcnt(9)\n"
"buffer_load_dwordx4 a[64:67], v251, s[40:43], s7 offen offset:1024 sc1\n"
"v_mfma_f32_16x16x32_bf16 v[32:35], a[140:143], a[60:63], v[32:35]\n"
"s_waitcnt lgkmcnt(13)\n"
"ds_read_b64_tr_b16 a[206:207], v208 offset:29184\n"
"v_mfma_f32_16x16x32_bf16 v[4:7], a[144:147], a[32:35], v[4:7]\n"
"s_waitcnt lgkmcnt(12)\n"
"s_add_u32 s7, s7, 4096\n"
"ds_read_b64_tr_b16 a[208:209], v209 offset:512\n"
"v_mfma_f32_16x16x32_bf16 v[4:7], a[148:151], a[36:39], v[4:7]\n"
"s_waitcnt lgkmcnt(11)\n"
"buffer_load_dwordx4 a[68:71], v251, s[40:43], s7 offen offset:1024 sc1\n"
"v_mfma_f32_16x16x32_bf16 v[4:7], a[152:155], a[40:43], v[4:7]\n"
"s_waitcnt lgkmcnt(9)\n"
"ds_read_b64_tr_b16 a[210:211], v209 offset:4608\n"
"ds_read_b64_tr_b16 a[212:213], v209 offset:8704\n"
"v_mfma_f32_16x16x32_bf16 v[4:7], a[156:159], a[44:47], v[4:7]\n"
"ds_read_b64_tr_b16 a[214:215], v209 offset:12800\n"
"v_mfma_f32_16x16x32_bf16 v[36:39], a[144:147], a[48:51], v[36:39]\n"
"s_add_u32 s7, s7, 4096\n"
"ds_read_b64_tr_b16 a[216:217], v209 offset:16896\n"
"v_mfma_f32_16x16x32_bf16 v[36:39], a[148:151], a[52:55], v[36:39]\n"
"buffer_load_dwordx4 a[72:75], v251, s[40:43], s7 offen offset:1024 sc1\n"
"v_mfma_f32_16x16x32_bf16 v[36:39], a[152:155], a[56:59], v[36:39]\n"
"ds_read_b64_tr_b16 a[218:219], v209 offset:20992\n"
"ds_read_b64_tr_b16 a[220:221], v209 offset:25088\n"
"v_mfma_f32_16x16x32_bf16 v[36:39], a[156:159], a[60:63], v[36:39]\n"
"s_waitcnt lgkmcnt(13)\n"
"s_add_u32 s7, s7, 4096\n"
"ds_read_b64_tr_b16 a[222:223], v209 offset:29184\n"
"v_mfma_f32_16x16x32_bf16 v[8:11], a[192:195], a[32:35], v[8:11]\n"
"s_waitcnt lgkmcnt(12)\n"
"buffer_load_dwordx4 a[76:79], v251, s[40:43], s7 offen offset:1024 sc1\n"
"v_mfma_f32_16x16x32_bf16 v[8:11], a[196:199], a[36:39], v[8:11]\n"
"s_waitcnt lgkmcnt(10)\n"
"ds_read_b64_tr_b16 a[128:129], v208 offset:1024\n"
"v_mfma_f32_16x16x32_bf16 v[8:11], a[200:203], a[40:43], v[8:11]\n"
"s_waitcnt lgkmcnt(9)\n"
"ds_read_b64_tr_b16 a[130:131], v208 offset:5120\n"
"ds_read_b64_tr_b16 a[132:133], v208 offset:9216\n"
"v_mfma_f32_16x16x32_bf16 v[8:11], a[204:207], a[44:47], v[8:11]\n"
"s_add_u32 s7, s7, 4096\n"
"ds_read_b64_tr_b16 a[134:135], v208 offset:13312\n"
"v_mfma_f32_16x16x32_bf16 v[40:43], a[192:195], a[48:51], v[40:43]\n"
"buffer_load_dwordx4 a[80:83], v251, s[40:43], s8 offen offset:1024 sc1\n"
"v_mfma_f32_16x16x32_bf16 v[40:43], a[196:199], a[52:55], v[40:43]\n"
"ds_read_b64_tr_b16 a[136:137], v208 offset:17408\n"
"ds_read_b64_tr_b16 a[138:139], v208 offset:21504\n"
"v_mfma_f32_16x16x32_bf16 v[40:43], a[200:203], a[56:59], v[40:43]\n"
"s_add_u32 s8, s8, 4096\n"
"ds_read_b64_tr_b16 a[140:141], v208 offset:25600\n"
"ds_read_b64_tr_b16 a[142:143], v208 offset:29696\n"
"v_mfma_f32_16x16x32_bf16 v[40:43], a[204:207], a[60:63], v[40:43]\n"
"s_waitcnt lgkmcnt(14)\n"
"buffer_load_dwordx4 a[84:87], v251, s[40:43], s8 offen offset:1024 sc1\n"
"v_mfma_f32_16x16x32_bf16 v[12:15], a[208:211], a[32:35], v[12:15]\n"
"s_waitcnt lgkmcnt(12)\n"
"ds_read_b64_tr_b16 a[144:145], v209 offset:1024\n"
"v_mfma_f32_16x16x32_bf16 v[12:15], a[212:215], a[36:39], v[12:15]\n"
"s_waitcnt lgkmcnt(11)\n"
"ds_read_b64_tr_b16 a[146:147], v209 offset:5120\n"
"ds_read_b64_tr_b16 a[148:149], v209 offset:9216\n"
"v_mfma_f32_16x16x32_bf16 v[12:15], a[216:219], a[40:43], v[12:15]\n"
"s_waitcnt lgkmcnt(11)\n"
"s_add_u32 s8, s8, 4096\n"
"ds_read_b64_tr_b16 a[150:151], v209 offset:13312\n"
"v_mfma_f32_16x16x32_bf16 v[12:15], a[220:223], a[44:47], v[12:15]\n"
"buffer_load_dwordx4 a[88:91], v251, s[40:43], s8 offen offset:1024 sc1\n"
"v_mfma_f32_16x16x32_bf16 v[44:47], a[208:211], a[48:51], v[44:47]\n"
"ds_read_b64_tr_b16 a[152:153], v209 offset:17408\n"
"v_mfma_f32_16x16x32_bf16 v[44:47], a[212:215], a[52:55], v[44:47]\n"
"s_add_u32 s8, s8, 4096\n"
"ds_read_b64_tr_b16 a[154:155], v209 offset:21504\n"
"ds_read_b64_tr_b16 a[156:157], v209 offset:25600\n"
"v_mfma_f32_16x16x32_bf16 v[44:47], a[216:219], a[56:59], v[44:47]\n"
"buffer_load_dwordx4 a[92:95], v251, s[40:43], s8 offen offset:1024 sc1\n"
"v_mfma_f32_16x16x32_bf16 v[44:47], a[220:223], a[60:63], v[44:47]\n"
"s_waitcnt lgkmcnt(13)\n"
"ds_read_b64_tr_b16 a[158:159], v209 offset:29696\n"
"v_mfma_f32_16x16x32_bf16 v[16:19], a[128:131], a[32:35], v[16:19]\n"
"s_waitcnt lgkmcnt(12)\n"
"s_add_u32 s8, s8, 4096\n"
"ds_read_b64_tr_b16 a[192:193], v208 offset:1536\n"
"v_mfma_f32_16x16x32_bf16 v[16:19], a[132:135], a[36:39], v[16:19]\n"
"s_waitcnt lgkmcnt(11)\n"
"s_add_u32 m0, s86, 32768\n"
"ds_read_b64_tr_b16 a[194:195], v208 offset:5632\n"
"ds_read_b64_tr_b16 a[196:197], v208 offset:9728\n"
"v_mfma_f32_16x16x32_bf16 v[16:19], a[136:139], a[40:43], v[16:19]\n"
"s_waitcnt lgkmcnt(11)\n"
"buffer_load_dwordx4 v247, s[64:67], s85 offen lds\n"
"v_mfma_f32_16x16x32_bf16 v[16:19], a[140:143], a[44:47], v[16:19]\n"
"ds_read_b64_tr_b16 a[198:199], v208 offset:13824\n"
"v_mfma_f32_16x16x32_bf16 v[48:51], a[128:131], a[48:51], v[48:51]\n"
"s_add_u32 m0, s86, 33792\n"
"ds_read_b64_tr_b16 a[200:201], v208 offset:17920\n"
"v_mfma_f32_16x16x32_bf16 v[48:51], a[132:135], a[52:55], v[48:51]\n"
"buffer_load_dwordx4 v248, s[64:67], s85 offen lds\n"
"v_mfma_f32_16x16x32_bf16 v[48:51], a[136:139], a[56:59], v[48:51]\n"
"ds_read_b64_tr_b16 a[202:203], v208 offset:22016\n"
"ds_read_b64_tr_b16 a[204:205], v208 offset:26112\n"
"v_mfma_f32_16x16x32_bf16 v[48:51], a[140:143], a[60:63], v[48:51]\n"
"s_waitcnt lgkmcnt(13)\n"
"s_add_u32 s85, s85, 131072\n"
"ds_read_b64_tr_b16 a[206:207], v208 offset:30208\n"
"v_mfma_f32_16x16x32_bf16 v[20:23], a[144:147], a[32:35], v[20:23]\n"
"s_waitcnt lgkmcnt(12)\n"
"s_add_u32 m0, s86, 40960\n"
"ds_read_b64_tr_b16 a[208:209], v209 offset:1536\n"
"v_mfma_f32_16x16x32_bf16 v[20:23], a[148:151], a[36:39], v[20:23]\n"
"s_waitcnt lgkmcnt(11)\n"
"buffer_load_dwordx4 v247, s[64:67], s85 offen lds\n"
"v_mfma_f32_16x16x32_bf16 v[20:23], a[152:155], a[40:43], v[20:23]\n"
"s_waitcnt lgkmcnt(9)\n"
"ds_read_b64_tr_b16 a[210:211], v209 offset:5632\n"
"ds_read_b64_tr_b16 a[212:213], v209 offset:9728\n"
"v_mfma_f32_16x16x32_bf16 v[20:23], a[156:159], a[44:47], v[20:23]\n"
"s_add_u32 m0, s86, 41984\n"
"ds_read_b64_tr_b16 a[214:215], v209 offset:13824\n"
"v_mfma_f32_16x16x32_bf16 v[52:55], a[144:147], a[48:51], v[52:55]\n"
"buffer_load_dwordx4 v248, s[64:67], s85 offen lds\n"
"v_mfma_f32_16x16x32_bf16 v[52:55], a[148:151], a[52:55], v[52:55]\n"
"ds_read_b64_tr_b16 a[216:217], v209 offset:17920\n"
"v_mfma_f32_16x16x32_bf16 v[52:55], a[152:155], a[56:59], v[52:55]\n"
"s_add_u32 s85, s85, 131072\n"
"ds_read_b64_tr_b16 a[218:219], v209 offset:22016\n"
"ds_read_b64_tr_b16 a[220:221], v209 offset:26112\n"
"v_mfma_f32_16x16x32_bf16 v[52:55], a[156:159], a[60:63], v[52:55]\n"
"s_waitcnt lgkmcnt(13)\n"
"s_add_u32 m0, s86, 49152\n"
"ds_read_b64_tr_b16 a[222:223], v209 offset:30208\n"
"v_subrev_u32 v208, 131072, v208\n"
"v_subrev_u32 v209, 131072, v209\n"
"v_mfma_f32_16x16x32_bf16 v[24:27], a[192:195], a[32:35], v[24:27]\n"
"s_waitcnt vmcnt(13)\n"
"s_barrier\n"
"buffer_load_dwordx4 v247, s[64:67], s85 offen lds\n"
"s_waitcnt lgkmcnt(12)\n"
"v_mfma_f32_16x16x32_bf16 v[24:27], a[196:199], a[36:39], v[24:27]\n"
"s_waitcnt lgkmcnt(10)\n"
"ds_read_b64_tr_b16 a[128:129], v208 offset:0\n"
"v_mfma_f32_16x16x32_bf16 v[24:27], a[200:203], a[40:43], v[24:27]\n"
"s_waitcnt lgkmcnt(9)\n"
"s_add_u32 m0, s86, 50176\n"
"ds_read_b64_tr_b16 a[130:131], v208 offset:4096\n"
"ds_read_b64_tr_b16 a[132:133], v208 offset:8192\n"
"v_mfma_f32_16x16x32_bf16 v[24:27], a[204:207], a[44:47], v[24:27]\n"
"buffer_load_dwordx4 v248, s[64:67], s85 offen lds\n"
"v_mfma_f32_16x16x32_bf16 v[56:59], a[192:195], a[48:51], v[56:59]\n"
"ds_read_b64_tr_b16 a[134:135], v208 offset:12288\n"
"v_mfma_f32_16x16x32_bf16 v[56:59], a[196:199], a[52:55], v[56:59]\n"
"s_add_u32 s85, s85, 131072\n"
"ds_read_b64_tr_b16 a[136:137], v208 offset:16384\n"
"ds_read_b64_tr_b16 a[138:139], v208 offset:20480\n"
"v_mfma_f32_16x16x32_bf16 v[56:59], a[200:203], a[56:59], v[56:59]\n"
"s_add_u32 m0, s86, 57344\n"
"ds_read_b64_tr_b16 a[140:141], v208 offset:24576\n"
"ds_read_b64_tr_b16 a[142:143], v208 offset:28672\n"
"v_mfma_f32_16x16x32_bf16 v[56:59], a[204:207], a[60:63], v[56:59]\n"
"s_waitcnt lgkmcnt(14)\n"
"buffer_load_dwordx4 v247, s[64:67], s85 offen lds\n"
"v_mfma_f32_16x16x32_bf16 v[28:31], a[208:211], a[32:35], v[28:31]\n"
"s_waitcnt lgkmcnt(12)\n"
"ds_read_b64_tr_b16 a[144:145], v209 offset:0\n"
"v_mfma_f32_16x16x32_bf16 v[28:31], a[212:215], a[36:39], v[28:31]\n"
"s_waitcnt lgkmcnt(11)\n"
"s_add_u32 m0, s86, 58368\n"
"ds_read_b64_tr_b16 a[146:147], v209 offset:4096\n"
"ds_read_b64_tr_b16 a[148:149], v209 offset:8192\n"
"v_mfma_f32_16x16x32_bf16 v[28:31], a[216:219], a[40:43], v[28:31]\n"
"s_waitcnt lgkmcnt(11)\n"
"buffer_load_dwordx4 v248, s[64:67], s85 offen lds\n"
"v_mfma_f32_16x16x32_bf16 v[28:31], a[220:223], a[44:47], v[28:31]\n"
"s_sub_u32 s85, s85, 884736\n"
"ds_read_b64_tr_b16 a[150:151], v209 offset:12288\n"
"v_mfma_f32_16x16x32_bf16 v[60:63], a[208:211], a[48:51], v[60:63]\n"
"ds_read_b64_tr_b16 a[152:153], v209 offset:16384\n"
"v_mfma_f32_16x16x32_bf16 v[60:63], a[212:215], a[52:55], v[60:63]\n"
"ds_read_b64_tr_b16 a[154:155], v209 offset:20480\n"
"v_mfma_f32_16x16x32_bf16 v[60:63], a[216:219], a[56:59], v[60:63]\n"
"ds_read_b64_tr_b16 a[156:157], v209 offset:24576\n"
"v_mfma_f32_16x16x32_bf16 v[60:63], a[220:223], a[60:63], v[60:63]\n"
"ds_read_b64_tr_b16 a[158:159], v209 offset:28672\n"
"s_waitcnt vmcnt(15) lgkmcnt(14)\n"
"v_mfma_f32_16x16x32_bf16 v[0:3], a[128:131], a[64:67], v[0:3]\n"
"s_waitcnt vmcnt(14) lgkmcnt(12)\n"
"ds_read_b64_tr_b16 a[192:193], v208 offset:512\n"
"v_mfma_f32_16x16x32_bf16 v[0:3], a[132:135], a[68:71], v[0:3]\n"
"s_waitcnt vmcnt(13) lgkmcnt(11)\n"
"ds_read_b64_tr_b16 a[194:195], v208 offset:4608\n"
"ds_read_b64_tr_b16 a[196:197], v208 offset:8704\n"
"v_mfma_f32_16x16x32_bf16 v[0:3], a[136:139], a[72:75], v[0:3]\n"
"s_waitcnt vmcnt(12) lgkmcnt(11)\n"
"ds_read_b64_tr_b16 a[198:199], v208 offset:12800\n"
"v_mfma_f32_16x16x32_bf16 v[0:3], a[140:143], a[76:79], v[0:3]\n"
"s_waitcnt vmcnt(11)\n"
"buffer_load_dwordx4 a[96:99], v251, s[40:43], s7 offen offset:1024 sc1\n"
"v_mfma_f32_16x16x32_bf16 v[32:35], a[128:131], a[80:83], v[32:35]\n"
"s_waitcnt vmcnt(11)\n"
"ds_read_b64_tr_b16 a[200:201], v208 offset:16896\n"
"v_mfma_f32_16x16x32_bf16 v[32:35], a[132:135], a[84:87], v[32:35]\n"
"s_waitcnt vmcnt(10)\n"
"s_add_u32 s7, s7, 4096\n"
"ds_read_b64_tr_b16 a[202:203], v208 offset:20992\n"
"ds_read_b64_tr_b16 a[204:205], v208 offset:25088\n"
"v_mfma_f32_16x16x32_bf16 v[32:35], a[136:139], a[88:91], v[32:35]\n"
"s_waitcnt vmcnt(9)\n"
"buffer_load_dwordx4 a[100:103], v251, s[40:43], s7 offen offset:1024 sc1\n"
"v_mfma_f32_16x16x32_bf16 v[32:35], a[140:143], a[92:95], v[32:35]\n"
"s_waitcnt lgkmcnt(13)\n"
"ds_read_b64_tr_b16 a[206:207], v208 offset:29184\n"
"v_mfma_f32_16x16x32_bf16 v[4:7], a[144:147], a[64:67], v[4:7]\n"
"s_waitcnt lgkmcnt(12)\n"
"s_add_u32 s7, s7, 4096\n"
"ds_read_b64_tr_b16 a[208:209], v209 offset:512\n"
"v_mfma_f32_16x16x32_bf16 v[4:7], a[148:151], a[68:71], v[4:7]\n"
"s_waitcnt lgkmcnt(11)\n"
"buffer_load_dwordx4 a[104:107], v251, s[40:43], s7 offen offset:1024 sc1\n"
"v_mfma_f32_16x16x32_bf16 v[4:7], a[152:155], a[72:75], v[4:7]\n"
"s_waitcnt lgkmcnt(9)\n"
"ds_read_b64_tr_b16 a[210:211], v209 offset:4608\n"
"ds_read_b64_tr_b16 a[212:213], v209 offset:8704\n"
"v_mfma_f32_16x16x32_bf16 v[4:7], a[156:159], a[76:79], v[4:7]\n"
"ds_read_b64_tr_b16 a[214:215], v209 offset:12800\n"
"v_mfma_f32_16x16x32_bf16 v[36:39], a[144:147], a[80:83], v[36:39]\n"
"s_add_u32 s7, s7, 4096\n"
"ds_read_b64_tr_b16 a[216:217], v209 offset:16896\n"
"v_mfma_f32_16x16x32_bf16 v[36:39], a[148:151], a[84:87], v[36:39]\n"
"buffer_load_dwordx4 a[108:111], v251, s[40:43], s7 offen offset:1024 sc1\n"
"v_mfma_f32_16x16x32_bf16 v[36:39], a[152:155], a[88:91], v[36:39]\n"
"ds_read_b64_tr_b16 a[218:219], v209 offset:20992\n"
"ds_read_b64_tr_b16 a[220:221], v209 offset:25088\n"
"v_mfma_f32_16x16x32_bf16 v[36:39], a[156:159], a[92:95], v[36:39]\n"
"s_waitcnt lgkmcnt(13)\n"
"s_sub_u32 s7, s7, 28672\n"
"ds_read_b64_tr_b16 a[222:223], v209 offset:29184\n"
"v_mfma_f32_16x16x32_bf16 v[8:11], a[192:195], a[64:67], v[8:11]\n"
"s_waitcnt lgkmcnt(12)\n"
"buffer_load_dwordx4 a[112:115], v251, s[40:43], s8 offen offset:1024 sc1\n"
"v_mfma_f32_16x16x32_bf16 v[8:11], a[196:199], a[68:71], v[8:11]\n"
"s_waitcnt lgkmcnt(10)\n"
"ds_read_b64_tr_b16 a[128:129], v208 offset:1024\n"
"v_mfma_f32_16x16x32_bf16 v[8:11], a[200:203], a[72:75], v[8:11]\n"
"s_waitcnt lgkmcnt(9)\n"
"ds_read_b64_tr_b16 a[130:131], v208 offset:5120\n"
"ds_read_b64_tr_b16 a[132:133], v208 offset:9216\n"
"v_mfma_f32_16x16x32_bf16 v[8:11], a[204:207], a[76:79], v[8:11]\n"
"s_add_u32 s8, s8, 4096\n"
"ds_read_b64_tr_b16 a[134:135], v208 offset:13312\n"
"v_mfma_f32_16x16x32_bf16 v[40:43], a[192:195], a[80:83], v[40:43]\n"
"buffer_load_dwordx4 a[116:119], v251, s[40:43], s8 offen offset:1024 sc1\n"
"v_mfma_f32_16x16x32_bf16 v[40:43], a[196:199], a[84:87], v[40:43]\n"
"ds_read_b64_tr_b16 a[136:137], v208 offset:17408\n"
"ds_read_b64_tr_b16 a[138:139], v208 offset:21504\n"
"v_mfma_f32_16x16x32_bf16 v[40:43], a[200:203], a[88:91], v[40:43]\n"
"s_add_u32 s8, s8, 4096\n"
"ds_read_b64_tr_b16 a[140:141], v208 offset:25600\n"
"ds_read_b64_tr_b16 a[142:143], v208 offset:29696\n"
"v_mfma_f32_16x16x32_bf16 v[40:43], a[204:207], a[92:95], v[40:43]\n"
"s_waitcnt lgkmcnt(14)\n"
"buffer_load_dwordx4 a[120:123], v251, s[40:43], s8 offen offset:1024 sc1\n"
"v_mfma_f32_16x16x32_bf16 v[12:15], a[208:211], a[64:67], v[12:15]\n"
"s_waitcnt lgkmcnt(12)\n"
"ds_read_b64_tr_b16 a[144:145], v209 offset:1024\n"
"v_mfma_f32_16x16x32_bf16 v[12:15], a[212:215], a[68:71], v[12:15]\n"
"s_waitcnt lgkmcnt(11)\n"
"ds_read_b64_tr_b16 a[146:147], v209 offset:5120\n"
"ds_read_b64_tr_b16 a[148:149], v209 offset:9216\n"
"v_mfma_f32_16x16x32_bf16 v[12:15], a[216:219], a[72:75], v[12:15]\n"
"s_waitcnt lgkmcnt(11)\n"
"s_add_u32 s8, s8, 4096\n"
"ds_read_b64_tr_b16 a[150:151], v209 offset:13312\n"
"v_mfma_f32_16x16x32_bf16 v[12:15], a[220:223], a[76:79], v[12:15]\n"
"buffer_load_dwordx4 a[124:127], v251, s[40:43], s8 offen offset:1024 sc1\n"
"v_mfma_f32_16x16x32_bf16 v[44:47], a[208:211], a[80:83], v[44:47]\n"
"s_sub_u32 s8, s8, 28672\n"
"ds_read_b64_tr_b16 a[152:153], v209 offset:17408\n"
"v_mfma_f32_16x16x32_bf16 v[44:47], a[212:215], a[84:87], v[44:47]\n"
"s_add_u32 m0, s86, 65536\n"
"ds_read_b64_tr_b16 a[154:155], v209 offset:21504\n"
"ds_read_b64_tr_b16 a[156:157], v209 offset:25600\n"
"v_mfma_f32_16x16x32_bf16 v[44:47], a[216:219], a[88:91], v[44:47]\n"
"buffer_load_dwordx4 v247, s[64:67], s85 offen lds\n"
"v_mfma_f32_16x16x32_bf16 v[44:47], a[220:223], a[92:95], v[44:47]\n"
"s_waitcnt lgkmcnt(13)\n"
"ds_read_b64_tr_b16 a[158:159], v209 offset:29696\n"
"v_mfma_f32_16x16x32_bf16 v[16:19], a[128:131], a[64:67], v[16:19]\n"
"s_waitcnt lgkmcnt(12)\n"
"ds_read_b64_tr_b16 a[192:193], v208 offset:1536\n"
"v_mfma_f32_16x16x32_bf16 v[16:19], a[132:135], a[68:71], v[16:19]\n"
"s_waitcnt lgkmcnt(11)\n"
"s_add_u32 m0, s86, 66560\n"
"ds_read_b64_tr_b16 a[194:195], v208 offset:5632\n"
"ds_read_b64_tr_b16 a[196:197], v208 offset:9728\n"
"v_mfma_f32_16x16x32_bf16 v[16:19], a[136:139], a[72:75], v[16:19]\n"
"s_waitcnt lgkmcnt(11)\n"
"buffer_load_dwordx4 v248, s[64:67], s85 offen lds\n"
"v_mfma_f32_16x16x32_bf16 v[16:19], a[140:143], a[76:79], v[16:19]\n"
"s_add_u32 s85, s85, 131072\n"
"ds_read_b64_tr_b16 a[198:199], v208 offset:13824\n"
"v_mfma_f32_16x16x32_bf16 v[48:51], a[128:131], a[80:83], v[48:51]\n"
"s_add_u32 m0, s86, 73728\n"
"ds_read_b64_tr_b16 a[200:201], v208 offset:17920\n"
"v_mfma_f32_16x16x32_bf16 v[48:51], a[132:135], a[84:87], v[48:51]\n"
"buffer_load_dwordx4 v247, s[64:67], s85 offen lds\n"
"v_mfma_f32_16x16x32_bf16 v[48:51], a[136:139], a[88:91], v[48:51]\n"
"ds_read_b64_tr_b16 a[202:203], v208 offset:22016\n"
"ds_read_b64_tr_b16 a[204:205], v208 offset:26112\n"
"v_mfma_f32_16x16x32_bf16 v[48:51], a[140:143], a[92:95], v[48:51]\n"
"s_waitcnt lgkmcnt(13)\n"
"ds_read_b64_tr_b16 a[206:207], v208 offset:30208\n"
"v_mfma_f32_16x16x32_bf16 v[20:23], a[144:147], a[64:67], v[20:23]\n"
"s_waitcnt lgkmcnt(12)\n"
"s_add_u32 m0, s86, 74752\n"
"ds_read_b64_tr_b16 a[208:209], v209 offset:1536\n"
"v_mfma_f32_16x16x32_bf16 v[20:23], a[148:151], a[68:71], v[20:23]\n"
"s_waitcnt lgkmcnt(11)\n"
"buffer_load_dwordx4 v248, s[64:67], s85 offen lds\n"
"v_mfma_f32_16x16x32_bf16 v[20:23], a[152:155], a[72:75], v[20:23]\n"
"s_waitcnt lgkmcnt(9)\n"
"s_add_u32 s85, s85, 131072\n"
"ds_read_b64_tr_b16 a[210:211], v209 offset:5632\n"
"ds_read_b64_tr_b16 a[212:213], v209 offset:9728\n"
"v_mfma_f32_16x16x32_bf16 v[20:23], a[156:159], a[76:79], v[20:23]\n"
"s_add_u32 m0, s86, 81920\n"
"ds_read_b64_tr_b16 a[214:215], v209 offset:13824\n"
"v_xor_b32 v215, v214, v215\n"
"v_xor_b32 v214, 0x9e3779b9, v214\n"
"v_cmp_eq_u32 vcc, 0xe17d05ac, v215\n"
"v_mfma_f32_16x16x32_bf16 v[52:55], a[144:147], a[80:83], v[52:55]\n"
"buffer_load_dwordx4 v247, s[64:67], s85 offen lds\n"
"v_subrev_u32 v214, s36, v214\n"
"v_cmp_gt_u32 s[98:99], 63, v214\n"
"v_mfma_f32_16x16x32_bf16 v[52:55], a[148:151], a[84:87], v[52:55]\n"
"ds_read_b64_tr_b16 a[216:217], v209 offset:17920\n"
"v_mfma_f32_16x16x32_bf16 v[52:55], a[152:155], a[88:91], v[52:55]\n"
"s_and_b64 vcc, vcc, s[98:99]\n"
"s_cmp_eq_u64 vcc, -1\n"
"s_cbranch_scc0 RO_%=\n"
"ROr_%=:\n"
"s_add_u32 s30, s30, 8192\n"
"ds_read_b64_tr_b16 a[218:219], v209 offset:22016\n"
"ds_read_b64_tr_b16 a[220:221], v209 offset:26112\n"
"v_mfma_f32_16x16x32_bf16 v[52:55], a[156:159], a[92:95], v[52:55]\n"
"s_waitcnt lgkmcnt(13)\n"
"s_add_u32 m0, s86, 82944\n"
"ds_read_b64_tr_b16 a[222:223], v209 offset:30208\n"
"v_add_u32 v208, 32768, v208\n"
"v_add_u32 v209, 32768, v209\n"
"v_mfma_f32_16x16x32_bf16 v[24:27], a[192:195], a[64:67], v[24:27]\n"
"s_waitcnt vmcnt(13)\n"
"s_barrier\n"
"buffer_load_dwordx4 v248, s[64:67], s85 offen lds\n"
"s_waitcnt lgkmcnt(12)\n"
"v_mfma_f32_16x16x32_bf16 v[24:27], a[196:199], a[68:71], v[24:27]\n"
"s_waitcnt lgkmcnt(10)\n"
"s_add_u32 s85, s85, 131072\n"
"ds_read_b64_tr_b16 a[128:129], v208 offset:0\n"
"v_and_b32 v207, 48, v251\n"
"v_lshlrev_b32 v207, 2, v207\n"
"v_mfma_f32_16x16x32_bf16 v[24:27], a[200:203], a[72:75], v[24:27]\n"
"s_waitcnt lgkmcnt(9)\n"
"s_add_u32 m0, s86, 90112\n"
"ds_read_b64_tr_b16 a[130:131], v208 offset:4096\n"
"ds_read_b64_tr_b16 a[132:133], v208 offset:8192\n"
"v_add_u32 v207, s11, v207\n"
"v_mfma_f32_16x16x32_bf16 v[24:27], a[204:207], a[76:79], v[24:27]\n"
"buffer_load_dwordx4 v247, s[64:67], s85 offen lds\n"
"v_mfma_f32_16x16x32_bf16 v[56:59], a[192:195], a[80:83], v[56:59]\n"
"ds_read_b64_tr_b16 a[134:135], v208 offset:12288\n"
"v_mfma_f32_16x16x32_bf16 v[56:59], a[196:199], a[84:87], v[56:59]\n"
"ds_read_b64_tr_b16 a[136:137], v208 offset:16384\n"
"ds_read_b64_tr_b16 a[138:139], v208 offset:20480\n"
"v_mfma_f32_16x16x32_bf16 v[56:59], a[200:203], a[88:91], v[56:59]\n"
"s_add_u32 m0, s86, 91136\n"
"ds_read_b64_tr_b16 a[140:141], v208 offset:24576\n"
"ds_read_b64_tr_b16 a[142:143], v208 offset:28672\n"
"v_mfma_f32_16x16x32_bf16 v[56:59], a[204:207], a[92:95], v[56:59]\n"
"s_waitcnt lgkmcnt(14)\n"
"buffer_load_dwordx4 v248, s[64:67], s85 offen lds\n"
"v_mfma_f32_16x16x32_bf16 v[28:31], a[208:211], a[64:67], v[28:31]\n"
"s_waitcnt lgkmcnt(12)\n"
"s_add_u32 s85, s85, 131072\n"
"ds_read_b64_tr_b16 a[144:145], v209 offset:0\n"
"v_mfma_f32_16x16x32_bf16 v[28:31], a[212:215], a[68:71], v[28:31]\n"
"s_waitcnt lgkmcnt(11)\n"
"s_add_u32 s15, s6, 40960\n"
"ds_read_b64_tr_b16 a[146:147], v209 offset:4096\n"
"ds_read_b64_tr_b16 a[148:149], v209 offset:8192\n"
"v_mfma_f32_16x16x32_bf16 v[28:31], a[216:219], a[72:75], v[28:31]\n"
"s_waitcnt lgkmcnt(11)\n"
"buffer_load_dwordx4 a[176:179], v251, s[40:43], s15 offen sc1\n"
"v_mfma_f32_16x16x32_bf16 v[28:31], a[220:223], a[76:79], v[28:31]\n"
"ds_read_b64_tr_b16 a[150:151], v209 offset:12288\n"
"v_mfma_f32_16x16x32_bf16 v[60:63], a[208:211], a[80:83], v[60:63]\n"
"ds_read_b64_tr_b16 a[152:153], v209 offset:16384\n"
"v_mfma_f32_16x16x32_bf16 v[60:63], a[212:215], a[84:87], v[60:63]\n"
"ds_read_b64_tr_b16 a[154:155], v209 offset:20480\n"
"v_mfma_f32_16x16x32_bf16 v[60:63], a[216:219], a[88:91], v[60:63]\n"
"ds_read_b64_tr_b16 a[156:157], v209 offset:24576\n"
"v_mfma_f32_16x16x32_bf16 v[60:63], a[220:223], a[92:95], v[60:63]\n"
"ds_read_b64_tr_b16 a[158:159], v209 offset:28672\n"
"s_waitcnt vmcnt(16) lgkmcnt(14)\n"
"v_mfma_f32_16x16x32_bf16 v[0:3], a[128:131], a[96:99], v[0:3]\n"
"s_waitcnt vmcnt(15) lgkmcnt(12)\n"
"s_mov_b32 s13, 0x4275fdf0\n"
"ds_read_b64_tr_b16 a[192:193], v208 offset:512\n"
"v_mfma_f32_16x16x32_bf16 v[0:3], a[132:135], a[100:103], v[0:3]\n"
"s_waitcnt vmcnt(14) lgkmcnt(11)\n"
"ds_read_b64_tr_b16 a[194:195], v208 offset:4608\n"
"ds_read_b64_tr_b16 a[196:197], v208 offset:8704\n"
"v_mfma_f32_16x16x32_bf16 v[0:3], a[136:139], a[104:107], v[0:3]\n"
"s_waitcnt vmcnt(13) lgkmcnt(11)\n"
"ds_read_b64_tr_b16 a[198:199], v208 offset:12800\n"
"v_mfma_f32_16x16x32_bf16 v[0:3], a[140:143], a[108:111], v[0:3]\n"
"s_waitcnt vmcnt(12)\n"
"buffer_load_dwordx4 a[0:3], v251, s[40:43], s7 offen offset:2048 sc1\n"
"v_mfma_f32_16x16x32_bf16 v[32:35], a[128:131], a[112:115], v[32:35]\n"
"s_waitcnt vmcnt(12)\n"
"ds_read_b64_tr_b16 a[200:201], v208 offset:16896\n"
"v_mfma_f32_16x16x32_bf16 v[32:35], a[132:135], a[116:119], v[32:35]\n"
"s_waitcnt vmcnt(11)\n"
"s_add_u32 s7, s7, 4096\n"
"ds_read_b64_tr_b16 a[202:203], v208 offset:20992\n"
"ds_read_b64_tr_b16 a[204:205], v208 offset:25088\n"
"v_mfma_f32_16x16x32_bf16 v[32:35], a[136:139], a[120:123], v[32:35]\n"
"s_waitcnt vmcnt(10)\n"
"buffer_load_dwordx4 a[4:7], v251, s[40:43], s7 offen offset:2048 sc1\n"
"v_mfma_f32_16x16x32_bf16 v[32:35], a[140:143], a[124:127], v[32:35]\n"
"s_waitcnt lgkmcnt(13)\n"
"ds_read_b64_tr_b16 a[206:207], v208 offset:29184\n"
"v_mfma_f32_16x16x32_bf16 v[4:7], a[144:147], a[96:99], v[4:7]\n"
"s_waitcnt lgkmcnt(12)\n"
"s_add_u32 s7, s7, 4096\n"
"ds_read_b64_tr_b16 a[208:209], v209 offset:512\n"
"v_mfma_f32_16x16x32_bf16 v[4:7], a[148:151], a[100:103], v[4:7]\n"
"s_waitcnt lgkmcnt(11)\n"
"buffer_load_dwordx4 a[8:11], v251, s[40:43], s7 offen offset:2048 sc1\n"
"v_mfma_f32_16x16x32_bf16 v[4:7], a[152:155], a[104:107], v[4:7]\n"
"s_waitcnt lgkmcnt(9)\n"
"ds_read_b64_tr_b16 a[210:211], v209 offset:4608\n"
"ds_read_b64_tr_b16 a[212:213], v209 offset:8704\n"
"v_mfma_f32_16x16x32_bf16 v[4:7], a[156:159], a[108:111], v[4:7]\n"
"ds_read_b64_tr_b16 a[214:215], v209 offset:12800\n"
"v_mfma_f32_16x16x32_bf16 v[36:39], a[144:147], a[112:115], v[36:39]\n"
"s_add_u32 s7, s7, 4096\n"
"ds_read_b64_tr_b16 a[216:217], v209 offset:16896\n"
"v_mfma_f32_16x16x32_bf16 v[36:39], a[148:151], a[116:119], v[36:39]\n"
"buffer_load_dwordx4 a[12:15], v251, s[40:43], s7 offen offset:2048 sc1\n"
"v_mfma_f32_16x16x32_bf16 v[36:39], a[152:155], a[120:123], v[36:39]\n"
"ds_read_b64_tr_b16 a[218:219], v209 offset:20992\n"
"ds_read_b64_tr_b16 a[220:221], v209 offset:25088\n"
"v_mfma_f32_16x16x32_bf16 v[36:39], a[156:159], a[124:127], v[36:39]\n"
"s_waitcnt lgkmcnt(13)\n"
"s_add_u32 s7, s7, 4096\n"
"ds_read_b64_tr_b16 a[222:223], v209 offset:29184\n"
"v_mfma_f32_16x16x32_bf16 v[8:11], a[192:195], a[96:99], v[8:11]\n"
"s_waitcnt lgkmcnt(12)\n"
"buffer_load_dwordx4 a[16:19], v251, s[40:43], s8 offen offset:2048 sc1\n"
"v_mfma_f32_16x16x32_bf16 v[8:11], a[196:199], a[100:103], v[8:11]\n"
"s_waitcnt lgkmcnt(10)\n"
"ds_read_b64_tr_b16 a[128:129], v208 offset:1024\n"
"v_mfma_f32_16x16x32_bf16 v[8:11], a[200:203], a[104:107], v[8:11]\n"
"s_waitcnt lgkmcnt(9)\n"
"ds_read_b64_tr_b16 a[130:131], v208 offset:5120\n"
"ds_read_b64_tr_b16 a[132:133], v208 offset:9216\n"
"v_mfma_f32_16x16x32_bf16 v[8:11], a[204:207], a[108:111], v[8:11]\n"
"s_add_u32 s8, s8, 4096\n"
"ds_read_b64_tr_b16 a[134:135], v208 offset:13312\n"
"v_mfma_f32_16x16x32_bf16 v[40:43], a[192:195], a[112:115], v[40:43]\n"
"buffer_load_dwordx4 a[20:23], v251, s[40:43], s8 offen offset:2048 sc1\n"
"v_mfma_f32_16x16x32_bf16 v[40:43], a[196:199], a[116:119], v[40:43]\n"
"ds_read_b64_tr_b16 a[136:137], v208 offset:17408\n"
"ds_read_b64_tr_b16 a[138:139], v208 offset:21504\n"
"v_mfma_f32_16x16x32_bf16 v[40:43], a[200:203], a[120:123], v[40:43]\n"
"s_add_u32 s8, s8, 4096\n"
"ds_read_b64_tr_b16 a[140:141], v208 offset:25600\n"
"ds_read_b64_tr_b16 a[142:143], v208 offset:29696\n"
"v_mfma_f32_16x16x32_bf16 v[40:43], a[204:207], a[124:127], v[40:43]\n"
"s_waitcnt lgkmcnt(14)\n"
"buffer_load_dwordx4 a[24:27], v251, s[40:43], s8 offen offset:2048 sc1\n"
"v_mfma_f32_16x16x32_bf16 v[12:15], a[208:211], a[96:99], v[12:15]\n"
"s_waitcnt lgkmcnt(12)\n"
"ds_read_b64_tr_b16 a[144:145], v209 offset:1024\n"
"v_mfma_f32_16x16x32_bf16 v[12:15], a[212:215], a[100:103], v[12:15]\n"
"s_waitcnt lgkmcnt(11)\n"
"ds_read_b64_tr_b16 a[146:147], v209 offset:5120\n"
"ds_read_b64_tr_b16 a[148:149], v209 offset:9216\n"
"v_mfma_f32_16x16x32_bf16 v[12:15], a[216:219], a[104:107], v[12:15]\n"
"s_waitcnt lgkmcnt(11)\n"
"s_add_u32 s8, s8, 4096\n"
"ds_read_b64_tr_b16 a[150:151], v209 offset:13312\n"
"v_mfma_f32_16x16x32_bf16 v[12:15], a[220:223], a[108:111], v[12:15]\n"
"buffer_load_dwordx4 a[28:31], v251, s[40:43], s8 offen offset:2048 sc1\n"
"v_mfma_f32_16x16x32_bf16 v[44:47], a[208:211], a[112:115], v[44:47]\n"
"s_add_u32 s8, s8, 4096\n"
"ds_read_b64_tr_b16 a[152:153], v209 offset:17408\n"
"v_mfma_f32_16x16x32_bf16 v[44:47], a[212:215], a[116:119], v[44:47]\n"
"s_add_u32 m0, s86, 98304\n"
"ds_read_b64_tr_b16 a[154:155], v209 offset:21504\n"
"ds_read_b64_tr_b16 a[156:157], v209 offset:25600\n"
"v_mfma_f32_16x16x32_bf16 v[44:47], a[216:219], a[120:123], v[44:47]\n"
"buffer_load_dwordx4 v247, s[64:67], s85 offen lds\n"
"v_mfma_f32_16x16x32_bf16 v[44:47], a[220:223], a[124:127], v[44:47]\n"
"s_waitcnt lgkmcnt(13)\n"
"ds_read_b64_tr_b16 a[158:159], v209 offset:29696\n"
"v_mfma_f32_16x16x32_bf16 v[16:19], a[128:131], a[96:99], v[16:19]\n"
"s_waitcnt lgkmcnt(12)\n"
"ds_read_b64_tr_b16 a[192:193], v208 offset:1536\n"
"v_mfma_f32_16x16x32_bf16 v[16:19], a[132:135], a[100:103], v[16:19]\n"
"s_waitcnt lgkmcnt(11)\n"
"s_add_u32 m0, s86, 99328\n"
"ds_read_b64_tr_b16 a[194:195], v208 offset:5632\n"
"ds_read_b64_tr_b16 a[196:197], v208 offset:9728\n"
"v_mfma_f32_16x16x32_bf16 v[16:19], a[136:139], a[104:107], v[16:19]\n"
"s_waitcnt lgkmcnt(11)\n"
"buffer_load_dwordx4 v248, s[64:67], s85 offen lds\n"
"v_mfma_f32_16x16x32_bf16 v[16:19], a[140:143], a[108:111], v[16:19]\n"
"s_add_u32 s85, s85, 131072\n"
"ds_read_b64_tr_b16 a[198:199], v208 offset:13824\n"
"v_mfma_f32_16x16x32_bf16 v[48:51], a[128:131], a[112:115], v[48:51]\n"
"s_add_u32 m0, s86, 106496\n"
"ds_read_b64_tr_b16 a[200:201], v208 offset:17920\n"
"v_mfma_f32_16x16x32_bf16 v[48:51], a[132:135], a[116:119], v[48:51]\n"
"buffer_load_dwordx4 v247, s[64:67], s85 offen lds\n"
"v_mfma_f32_16x16x32_bf16 v[48:51], a[136:139], a[120:123], v[48:51]\n"
"ds_read_b64_tr_b16 a[202:203], v208 offset:22016\n"
"ds_read_b64_tr_b16 a[204:205], v208 offset:26112\n"
"v_mfma_f32_16x16x32_bf16 v[48:51], a[140:143], a[124:127], v[48:51]\n"
"s_waitcnt lgkmcnt(13)\n"
"ds_read_b64_tr_b16 a[206:207], v208 offset:30208\n"
"v_mfma_f32_16x16x32_bf16 v[20:23], a[144:147], a[96:99], v[20:23]\n"
"s_waitcnt lgkmcnt(12)\n"
"s_add_u32 m0, s86, 107520\n"
"ds_read_b64_tr_b16 a[208:209], v209 offset:1536\n"
"v_mfma_f32_16x16x32_bf16 v[20:23], a[148:151], a[100:103], v[20:23]\n"
"s_waitcnt lgkmcnt(11)\n"
"buffer_load_dwordx4 v248, s[64:67], s85 offen lds\n"
"v_mfma_f32_16x16x32_bf16 v[20:23], a[152:155], a[104:107], v[20:23]\n"
"s_waitcnt lgkmcnt(9)\n"
"s_add_u32 s85, s85, 131072\n"
"ds_read_b64_tr_b16 a[210:211], v209 offset:5632\n"
"ds_read_b64_tr_b16 a[212:213], v209 offset:9728\n"
"v_mfma_f32_16x16x32_bf16 v[20:23], a[156:159], a[108:111], v[20:23]\n"
"s_add_u32 m0, s86, 114688\n"
"ds_read_b64_tr_b16 a[214:215], v209 offset:13824\n"
"v_mfma_f32_16x16x32_bf16 v[52:55], a[144:147], a[112:115], v[52:55]\n"
"buffer_load_dwordx4 v247, s[64:67], s85 offen lds\n"
"v_mfma_f32_16x16x32_bf16 v[52:55], a[148:151], a[116:119], v[52:55]\n"
"ds_read_b64_tr_b16 a[216:217], v209 offset:17920\n"
"v_mfma_f32_16x16x32_bf16 v[52:55], a[152:155], a[120:123], v[52:55]\n"
"ds_read_b64_tr_b16 a[218:219], v209 offset:22016\n"
"ds_read_b64_tr_b16 a[220:221], v209 offset:26112\n"
"v_mfma_f32_16x16x32_bf16 v[52:55], a[156:159], a[124:127], v[52:55]\n"
"s_waitcnt lgkmcnt(13)\n"
"s_add_u32 m0, s86, 115712\n"
"ds_read_b64_tr_b16 a[222:223], v209 offset:30208\n"
"v_add_u32 v208, 32768, v208\n"
"v_add_u32 v209, 32768, v209\n"
"v_mfma_f32_16x16x32_bf16 v[24:27], a[192:195], a[96:99], v[24:27]\n"
"s_waitcnt vmcnt(14)\n"
"s_barrier\n"
"buffer_load_dwordx4 v248, s[64:67], s85 offen lds\n"
"s_waitcnt lgkmcnt(12)\n"
"v_mfma_f32_16x16x32_bf16 v[24:27], a[196:199], a[100:103], v[24:27]\n"
"s_waitcnt lgkmcnt(10)\n"
"s_add_u32 s85, s85, 131072\n"
"ds_read_b64_tr_b16 a[128:129], v208 offset:0\n"
"v_mfma_f32_16x16x32_bf16 v[24:27], a[200:203], a[104:107], v[24:27]\n"
"s_waitcnt lgkmcnt(9)\n"
"s_add_u32 m0, s86, 122880\n"
"ds_read_b64_tr_b16 a[130:131], v208 offset:4096\n"
"ds_read_b64_tr_b16 a[132:133], v208 offset:8192\n"
"v_mfma_f32_16x16x32_bf16 v[24:27], a[204:207], a[108:111], v[24:27]\n"
"buffer_load_dwordx4 v247, s[64:67], s85 offen lds\n"
"v_mfma_f32_16x16x32_bf16 v[56:59], a[192:195], a[112:115], v[56:59]\n"
"ds_read_b64_tr_b16 a[134:135], v208 offset:12288\n"
"v_mfma_f32_16x16x32_bf16 v[56:59], a[196:199], a[116:119], v[56:59]\n"
"ds_read_b64_tr_b16 a[136:137], v208 offset:16384\n"
"ds_read_b64_tr_b16 a[138:139], v208 offset:20480\n"
"v_mfma_f32_16x16x32_bf16 v[56:59], a[200:203], a[120:123], v[56:59]\n"
"s_add_u32 m0, s86, 123904\n"
"ds_read_b64_tr_b16 a[140:141], v208 offset:24576\n"
"ds_read_b64_tr_b16 a[142:143], v208 offset:28672\n"
"v_mfma_f32_16x16x32_bf16 v[56:59], a[204:207], a[124:127], v[56:59]\n"
"s_waitcnt lgkmcnt(14)\n"
"buffer_load_dwordx4 v248, s[64:67], s85 offen lds\n"
"v_mfma_f32_16x16x32_bf16 v[28:31], a[208:211], a[96:99], v[28:31]\n"
"s_waitcnt lgkmcnt(12)\n"
"s_sub_u32 s85, s85, 884736\n"
"ds_read_b64_tr_b16 a[144:145], v209 offset:0\n"
"v_mfma_f32_16x16x32_bf16 v[28:31], a[212:215], a[100:103], v[28:31]\n"
"s_waitcnt lgkmcnt(11)\n"
"s_add_u32 s15, s6, 40960\n"
"ds_read_b64_tr_b16 a[146:147], v209 offset:4096\n"
"ds_read_b64_tr_b16 a[148:149], v209 offset:8192\n"
"v_mfma_f32_16x16x32_bf16 v[28:31], a[216:219], a[104:107], v[28:31]\n"
"s_waitcnt lgkmcnt(11)\n"
"buffer_load_dwordx4 a[180:183], v251, s[40:43], s15 offen offset:1024 sc1\n"
"v_mfma_f32_16x16x32_bf16 v[28:31], a[220:223], a[108:111], v[28:31]\n"
"ds_read_b64_tr_b16 a[150:151], v209 offset:12288\n"
"v_mfma_f32_16x16x32_bf16 v[60:63], a[208:211], a[112:115], v[60:63]\n"
"ds_read_b64_tr_b16 a[152:153], v209 offset:16384\n"
"v_mfma_f32_16x16x32_bf16 v[60:63], a[212:215], a[116:119], v[60:63]\n"
"ds_read_b64_tr_b16 a[154:155], v209 offset:20480\n"
"v_mfma_f32_16x16x32_bf16 v[60:63], a[216:219], a[120:123], v[60:63]\n"
"ds_read_b64_tr_b16 a[156:157], v209 offset:24576\n"
"v_mfma_f32_16x16x32_bf16 v[60:63], a[220:223], a[124:127], v[60:63]\n"
"ds_read_b64_tr_b16 a[158:159], v209 offset:28672\n"
"s_waitcnt vmcnt(16) lgkmcnt(14)\n"
"v_mfma_f32_16x16x32_bf16 v[0:3], a[128:131], a[0:3], v[0:3]\n"
"s_waitcnt vmcnt(15) lgkmcnt(12)\n"
"ds_read_b64_tr_b16 a[192:193], v208 offset:512\n"
"v_mfma_f32_16x16x32_bf16 v[0:3], a[132:135], a[4:7], v[0:3]\n"
"s_waitcnt vmcnt(14) lgkmcnt(11)\n"
"ds_read_b64_tr_b16 a[194:195], v208 offset:4608\n"
"ds_read_b64_tr_b16 a[196:197], v208 offset:8704\n"
"v_mfma_f32_16x16x32_bf16 v[0:3], a[136:139], a[8:11], v[0:3]\n"
"s_waitcnt vmcnt(13) lgkmcnt(11)\n"
"ds_read_b64_tr_b16 a[198:199], v208 offset:12800\n"
"v_mfma_f32_16x16x32_bf16 v[0:3], a[140:143], a[12:15], v[0:3]\n"
"s_waitcnt vmcnt(12)\n"
"buffer_load_dwordx4 a[32:35], v251, s[40:43], s7 offen offset:2048 sc1\n"
"v_mfma_f32_16x16x32_bf16 v[32:35], a[128:131], a[16:19], v[32:35]\n"
"s_waitcnt vmcnt(12)\n"
"ds_read_b64_tr_b16 a[200:201], v208 offset:16896\n"
"v_mfma_f32_16x16x32_bf16 v[32:35], a[132:135], a[20:23], v[32:35]\n"
"s_waitcnt vmcnt(11)\n"
"s_add_u32 s7, s7, 4096\n"
"ds_read_b64_tr_b16 a[202:203], v208 offset:20992\n"
"ds_read_b64_tr_b16 a[204:205], v208 offset:25088\n"
"v_mfma_f32_16x16x32_bf16 v[32:35], a[136:139], a[24:27], v[32:35]\n"
"s_waitcnt vmcnt(10)\n"
"buffer_load_dwordx4 a[36:39], v251, s[40:43], s7 offen offset:2048 sc1\n"
"v_mfma_f32_16x16x32_bf16 v[32:35], a[140:143], a[28:31], v[32:35]\n"
"s_waitcnt lgkmcnt(13)\n"
"ds_read_b64_tr_b16 a[206:207], v208 offset:29184\n"
"v_mfma_f32_16x16x32_bf16 v[4:7], a[144:147], a[0:3], v[4:7]\n"
"s_waitcnt lgkmcnt(12)\n"
"s_add_u32 s7, s7, 4096\n"
"ds_read_b64_tr_b16 a[208:209], v209 offset:512\n"
"v_mfma_f32_16x16x32_bf16 v[4:7], a[148:151], a[4:7], v[4:7]\n"
"s_waitcnt lgkmcnt(11)\n"
"buffer_load_dwordx4 a[40:43], v251, s[40:43], s7 offen offset:2048 sc1\n"
"v_mfma_f32_16x16x32_bf16 v[4:7], a[152:155], a[8:11], v[4:7]\n"
"s_waitcnt lgkmcnt(9)\n"
"ds_read_b64_tr_b16 a[210:211], v209 offset:4608\n"
"ds_read_b64_tr_b16 a[212:213], v209 offset:8704\n"
"v_mfma_f32_16x16x32_bf16 v[4:7], a[156:159], a[12:15], v[4:7]\n"
"ds_read_b64_tr_b16 a[214:215], v209 offset:12800\n"
"v_mfma_f32_16x16x32_bf16 v[36:39], a[144:147], a[16:19], v[36:39]\n"
"s_add_u32 s7, s7, 4096\n"
"ds_read_b64_tr_b16 a[216:217], v209 offset:16896\n"
"v_mfma_f32_16x16x32_bf16 v[36:39], a[148:151], a[20:23], v[36:39]\n"
"buffer_load_dwordx4 a[44:47], v251, s[40:43], s7 offen offset:2048 sc1\n"
"v_mfma_f32_16x16x32_bf16 v[36:39], a[152:155], a[24:27], v[36:39]\n"
"ds_read_b64_tr_b16 a[218:219], v209 offset:20992\n"
"ds_read_b64_tr_b16 a[220:221], v209 offset:25088\n"
"v_mfma_f32_16x16x32_bf16 v[36:39], a[156:159], a[28:31], v[36:39]\n"
"s_waitcnt lgkmcnt(13)\n"
"s_sub_u32 s7, s7, 28672\n"
"ds_read_b64_tr_b16 a[222:223], v209 offset:29184\n"
"v_mfma_f32_16x16x32_bf16 v[8:11], a[192:195], a[0:3], v[8:11]\n"
"s_waitcnt lgkmcnt(12)\n"
"buffer_load_dwordx4 a[48:51], v251, s[40:43], s8 offen offset:2048 sc1\n"
"v_mfma_f32_16x16x32_bf16 v[8:11], a[196:199], a[4:7], v[8:11]\n"
"s_waitcnt lgkmcnt(10)\n"
"ds_read_b64_tr_b16 a[128:129], v208 offset:1024\n"
"v_mfma_f32_16x16x32_bf16 v[8:11], a[200:203], a[8:11], v[8:11]\n"
"s_waitcnt lgkmcnt(9)\n"
"ds_read_b64_tr_b16 a[130:131], v208 offset:5120\n"
"ds_read_b64_tr_b16 a[132:133], v208 offset:9216\n"
"v_mfma_f32_16x16x32_bf16 v[8:11], a[204:207], a[12:15], v[8:11]\n"
"s_add_u32 s8, s8, 4096\n"
"ds_read_b64_tr_b16 a[134:135], v208 offset:13312\n"
"v_mfma_f32_16x16x32_bf16 v[40:43], a[192:195], a[16:19], v[40:43]\n"
"buffer_load_dwordx4 a[52:55], v251, s[40:43], s8 offen offset:2048 sc1\n"
"v_mfma_f32_16x16x32_bf16 v[40:43], a[196:199], a[20:23], v[40:43]\n"
"ds_read_b64_tr_b16 a[136:137], v208 offset:17408\n"
"ds_read_b64_tr_b16 a[138:139], v208 offset:21504\n"
"v_mfma_f32_16x16x32_bf16 v[40:43], a[200:203], a[24:27], v[40:43]\n"
"s_add_u32 s8, s8, 4096\n"
"ds_read_b64_tr_b16 a[140:141], v208 offset:25600\n"
"ds_read_b64_tr_b16 a[142:143], v208 offset:29696\n"
"v_mfma_f32_16x16x32_bf16 v[40:43], a[204:207], a[28:31], v[40:43]\n"
"s_waitcnt lgkmcnt(14)\n"
"buffer_load_dwordx4 a[56:59], v251, s[40:43], s8 offen offset:2048 sc1\n"
"v_mfma_f32_16x16x32_bf16 v[12:15], a[208:211], a[0:3], v[12:15]\n"
"s_waitcnt lgkmcnt(12)\n"
"ds_read_b64_tr_b16 a[144:145], v209 offset:1024\n"
"v_mfma_f32_16x16x32_bf16 v[12:15], a[212:215], a[4:7], v[12:15]\n"
"s_waitcnt lgkmcnt(11)\n"
"ds_read_b64_tr_b16 a[146:147], v209 offset:5120\n"
"ds_read_b64_tr_b16 a[148:149], v209 offset:9216\n"
"v_mfma_f32_16x16x32_bf16 v[12:15], a[216:219], a[8:11], v[12:15]\n"
"s_waitcnt lgkmcnt(11)\n"
"s_add_u32 s8, s8, 4096\n"
"ds_read_b64_tr_b16 a[150:151], v209 offset:13312\n"
"v_mfma_f32_16x16x32_bf16 v[12:15], a[220:223], a[12:15], v[12:15]\n"
"buffer_load_dwordx4 a[60:63], v251, s[40:43], s8 offen offset:2048 sc1\n"
"v_mfma_f32_16x16x32_bf16 v[44:47], a[208:211], a[16:19], v[44:47]\n"
"s_sub_u32 s8, s8, 28672\n"
"ds_read_b64_tr_b16 a[152:153], v209 offset:17408\n"
"v_mfma_f32_16x16x32_bf16 v[44:47], a[212:215], a[20:23], v[44:47]\n"
"s_add_u32 m0, s86, 131072\n"
"ds_read_b64_tr_b16 a[154:155], v209 offset:21504\n"
"ds_read_b64_tr_b16 a[156:157], v209 offset:25600\n"
"v_mfma_f32_16x16x32_bf16 v[44:47], a[216:219], a[24:27], v[44:47]\n"
"buffer_load_dwordx4 v247, s[64:67], s85 offen lds\n"
"v_mfma_f32_16x16x32_bf16 v[44:47], a[220:223], a[28:31], v[44:47]\n"
"s_waitcnt lgkmcnt(13)\n"
"ds_read_b64_tr_b16 a[158:159], v209 offset:29696\n"
"v_mfma_f32_16x16x32_bf16 v[16:19], a[128:131], a[0:3], v[16:19]\n"
"s_waitcnt lgkmcnt(12)\n"
"ds_read_b64_tr_b16 a[192:193], v208 offset:1536\n"
"v_mfma_f32_16x16x32_bf16 v[16:19], a[132:135], a[4:7], v[16:19]\n"
"s_waitcnt lgkmcnt(11)\n"
"s_add_u32 m0, s86, 132096\n"
"ds_read_b64_tr_b16 a[194:195], v208 offset:5632\n"
"ds_read_b64_tr_b16 a[196:197], v208 offset:9728\n"
"v_mfma_f32_16x16x32_bf16 v[16:19], a[136:139], a[8:11], v[16:19]\n"
"s_waitcnt lgkmcnt(11)\n"
"buffer_load_dwordx4 v248, s[64:67], s85 offen lds\n"
"v_mfma_f32_16x16x32_bf16 v[16:19], a[140:143], a[12:15], v[16:19]\n"
"s_add_u32 s85, s85, 131072\n"
"ds_read_b64_tr_b16 a[198:199], v208 offset:13824\n"
"v_mfma_f32_16x16x32_bf16 v[48:51], a[128:131], a[16:19], v[48:51]\n"
"s_add_u32 m0, s86, 139264\n"
"ds_read_b64_tr_b16 a[200:201], v208 offset:17920\n"
"v_mfma_f32_16x16x32_bf16 v[48:51], a[132:135], a[20:23], v[48:51]\n"
"buffer_load_dwordx4 v247, s[64:67], s85 offen lds\n"
"v_mfma_f32_16x16x32_bf16 v[48:51], a[136:139], a[24:27], v[48:51]\n"
"ds_read_b64_tr_b16 a[202:203], v208 offset:22016\n"
"ds_read_b64_tr_b16 a[204:205], v208 offset:26112\n"
"v_mfma_f32_16x16x32_bf16 v[48:51], a[140:143], a[28:31], v[48:51]\n"
"s_waitcnt lgkmcnt(13)\n"
"ds_read_b64_tr_b16 a[206:207], v208 offset:30208\n"
"v_mfma_f32_16x16x32_bf16 v[20:23], a[144:147], a[0:3], v[20:23]\n"
"s_waitcnt lgkmcnt(12)\n"
"s_add_u32 m0, s86, 140288\n"
"ds_read_b64_tr_b16 a[208:209], v209 offset:1536\n"
"v_mfma_f32_16x16x32_bf16 v[20:23], a[148:151], a[4:7], v[20:23]\n"
"s_waitcnt lgkmcnt(11)\n"
"buffer_load_dwordx4 v248, s[64:67], s85 offen lds\n"
"v_mfma_f32_16x16x32_bf16 v[20:23], a[152:155], a[8:11], v[20:23]\n"
"s_waitcnt lgkmcnt(9)\n"
"s_add_u32 s85, s85, 131072\n"
"ds_read_b64_tr_b16 a[210:211], v209 offset:5632\n"
"ds_read_b64_tr_b16 a[212:213], v209 offset:9728\n"
"v_mfma_f32_16x16x32_bf16 v[20:23], a[156:159], a[12:15], v[20:23]\n"
"s_add_u32 m0, s86, 147456\n"
"ds_read_b64_tr_b16 a[214:215], v209 offset:13824\n"
"v_mfma_f32_16x16x32_bf16 v[52:55], a[144:147], a[16:19], v[52:55]\n"
"buffer_load_dwordx4 v247, s[64:67], s85 offen lds\n"
"v_mfma_f32_16x16x32_bf16 v[52:55], a[148:151], a[20:23], v[52:55]\n"
"ds_read_b64_tr_b16 a[216:217], v209 offset:17920\n"
"v_mfma_f32_16x16x32_bf16 v[52:55], a[152:155], a[24:27], v[52:55]\n"
"ds_read_b64_tr_b16 a[218:219], v209 offset:22016\n"
"ds_read_b64_tr_b16 a[220:221], v209 offset:26112\n"
"v_mfma_f32_16x16x32_bf16 v[52:55], a[156:159], a[28:31], v[52:55]\n"
"s_waitcnt lgkmcnt(13)\n"
"s_add_u32 m0, s86, 148480\n"
"ds_read_b64_tr_b16 a[222:223], v209 offset:30208\n"
"v_add_u32 v208, 32768, v208\n"
"v_add_u32 v209, 32768, v209\n"
"v_mfma_f32_16x16x32_bf16 v[24:27], a[192:195], a[0:3], v[24:27]\n"
"s_waitcnt vmcnt(14)\n"
"s_barrier\n"
"buffer_load_dwordx4 v248, s[64:67], s85 offen lds\n"
"s_waitcnt lgkmcnt(12)\n"
"v_mfma_f32_16x16x32_bf16 v[24:27], a[196:199], a[4:7], v[24:27]\n"
"s_waitcnt lgkmcnt(10)\n"
"s_add_u32 s85, s85, 131072\n"
"ds_read_b64_tr_b16 a[128:129], v208 offset:0\n"
"v_mfma_f32_16x16x32_bf16 v[24:27], a[200:203], a[8:11], v[24:27]\n"
"s_waitcnt lgkmcnt(9)\n"
"s_add_u32 m0, s86, 155648\n"
"ds_read_b64_tr_b16 a[130:131], v208 offset:4096\n"
"ds_read_b64_tr_b16 a[132:133], v208 offset:8192\n"
"v_mfma_f32_16x16x32_bf16 v[24:27], a[204:207], a[12:15], v[24:27]\n"
"buffer_load_dwordx4 v247, s[64:67], s85 offen lds\n"
"v_mfma_f32_16x16x32_bf16 v[56:59], a[192:195], a[16:19], v[56:59]\n"
"ds_read_b64_tr_b16 a[134:135], v208 offset:12288\n"
"v_mfma_f32_16x16x32_bf16 v[56:59], a[196:199], a[20:23], v[56:59]\n"
"ds_read_b64_tr_b16 a[136:137], v208 offset:16384\n"
"ds_read_b64_tr_b16 a[138:139], v208 offset:20480\n"
"v_mfma_f32_16x16x32_bf16 v[56:59], a[200:203], a[24:27], v[56:59]\n"
"s_add_u32 m0, s86, 156672\n"
"ds_read_b64_tr_b16 a[140:141], v208 offset:24576\n"
"ds_read_b64_tr_b16 a[142:143], v208 offset:28672\n"
"v_mfma_f32_16x16x32_bf16 v[56:59], a[204:207], a[28:31], v[56:59]\n"
"s_waitcnt lgkmcnt(14)\n"
"buffer_load_dwordx4 v248, s[64:67], s85 offen lds\n"
"v_mfma_f32_16x16x32_bf16 v[28:31], a[208:211], a[0:3], v[28:31]\n"
"s_waitcnt lgkmcnt(12)\n"
"s_add_u32 s85, s85, 131072\n"
"ds_read_b64_tr_b16 a[144:145], v209 offset:0\n"
"v_mfma_f32_16x16x32_bf16 v[28:31], a[212:215], a[4:7], v[28:31]\n"
"s_waitcnt lgkmcnt(11)\n"
"s_add_u32 s15, s6, 40960\n"
"ds_read_b64_tr_b16 a[146:147], v209 offset:4096\n"
"ds_read_b64_tr_b16 a[148:149], v209 offset:8192\n"
"v_mfma_f32_16x16x32_bf16 v[28:31], a[216:219], a[8:11], v[28:31]\n"
"s_waitcnt lgkmcnt(11)\n"
"buffer_load_dwordx4 a[184:187], v251, s[40:43], s15 offen offset:2048 sc1\n"
"v_mfma_f32_16x16x32_bf16 v[28:31], a[220:223], a[12:15], v[28:31]\n"
"ds_read_b64_tr_b16 a[150:151], v209 offset:12288\n"
"v_mfma_f32_16x16x32_bf16 v[60:63], a[208:211], a[16:19], v[60:63]\n"
"ds_read_b64_tr_b16 a[152:153], v209 offset:16384\n"
"v_mfma_f32_16x16x32_bf16 v[60:63], a[212:215], a[20:23], v[60:63]\n"
"ds_read_b64_tr_b16 a[154:155], v209 offset:20480\n"
"v_mfma_f32_16x16x32_bf16 v[60:63], a[216:219], a[24:27], v[60:63]\n"
"ds_read_b64_tr_b16 a[156:157], v209 offset:24576\n"
"v_mfma_f32_16x16x32_bf16 v[60:63], a[220:223], a[28:31], v[60:63]\n"
"ds_read_b64_tr_b16 a[158:159], v209 offset:28672\n"
"s_waitcnt vmcnt(16) lgkmcnt(14)\n"
"v_mfma_f32_16x16x32_bf16 v[0:3], a[128:131], a[32:35], v[0:3]\n"
"s_waitcnt vmcnt(15) lgkmcnt(12)\n"
"ds_read_b64_tr_b16 a[192:193], v208 offset:512\n"
"v_mfma_f32_16x16x32_bf16 v[0:3], a[132:135], a[36:39], v[0:3]\n"
"s_waitcnt vmcnt(14) lgkmcnt(11)\n"
"ds_read_b64_tr_b16 a[194:195], v208 offset:4608\n"
"ds_read_b64_tr_b16 a[196:197], v208 offset:8704\n"
"v_mfma_f32_16x16x32_bf16 v[0:3], a[136:139], a[40:43], v[0:3]\n"
"s_waitcnt vmcnt(13) lgkmcnt(11)\n"
"ds_read_b64_tr_b16 a[198:199], v208 offset:12800\n"
"v_mfma_f32_16x16x32_bf16 v[0:3], a[140:143], a[44:47], v[0:3]\n"
"s_waitcnt vmcnt(12)\n"
"buffer_load_dwordx4 a[64:67], v251, s[40:43], s7 offen offset:3072 sc1\n"
"v_mfma_f32_16x16x32_bf16 v[32:35], a[128:131], a[48:51], v[32:35]\n"
"s_waitcnt vmcnt(12)\n"
"s_add_u32 s7, s7, 4096\n"
"ds_read_b64_tr_b16 a[200:201], v208 offset:16896\n"
"ds_read_b64_tr_b16 a[202:203], v208 offset:20992\n"
"v_mfma_f32_16x16x32_bf16 v[32:35], a[132:135], a[52:55], v[32:35]\n"
"s_waitcnt vmcnt(11)\n"
"buffer_load_dwordx4 a[68:71], v251, s[40:43], s7 offen offset:3072 sc1\n"
"v_mfma_f32_16x16x32_bf16 v[32:35], a[136:139], a[56:59], v[32:35]\n"
"s_waitcnt vmcnt(11)\n"
"s_add_u32 s7, s7, 4096\n"
"ds_read_b64_tr_b16 a[204:205], v208 offset:25088\n"
"ds_read_b64_tr_b16 a[206:207], v208 offset:29184\n"
"v_mfma_f32_16x16x32_bf16 v[32:35], a[140:143], a[60:63], v[32:35]\n"
"s_waitcnt lgkmcnt(14)\n"
"buffer_load_dwordx4 a[72:75], v251, s[40:43], s7 offen offset:3072 sc1\n"
"v_mfma_f32_16x16x32_bf16 v[4:7], a[144:147], a[32:35], v[4:7]\n"
"s_waitcnt lgkmcnt(12)\n"
"ds_read_b64_tr_b16 a[208:209], v209 offset:512\n"
"v_mfma_f32_16x16x32_bf16 v[4:7], a[148:151], a[36:39], v[4:7]\n"
"s_waitcnt lgkmcnt(11)\n"
"s_add_u32 s7, s7, 4096\n"
"ds_read_b64_tr_b16 a[210:211], v209 offset:4608\n"
"ds_read_b64_tr_b16 a[212:213], v209 offset:8704\n"
"v_mfma_f32_16x16x32_bf16 v[4:7], a[152:155], a[40:43], v[4:7]\n"
"s_waitcnt lgkmcnt(11)\n"
"buffer_load_dwordx4 a[76:79], v251, s[40:43], s7 offen offset:3072 sc1\n"
"v_mfma_f32_16x16x32_bf16 v[4:7], a[156:159], a[44:47], v[4:7]\n"
"s_add_u32 s7, s7, 4096\n"
"ds_read_b64_tr_b16 a[214:215], v209 offset:12800\n"
"v_mfma_f32_16x16x32_bf16 v[36:39], a[144:147], a[48:51], v[36:39]\n"
"buffer_load_dwordx4 a[80:83], v251, s[40:43], s8 offen offset:3072 sc1\n"
"v_mfma_f32_16x16x32_bf16 v[36:39], a[148:151], a[52:55], v[36:39]\n"
"ds_read_b64_tr_b16 a[216:217], v209 offset:16896\n"
"ds_read_b64_tr_b16 a[218:219], v209 offset:20992\n"
"v_mfma_f32_16x16x32_bf16 v[36:39], a[152:155], a[56:59], v[36:39]\n"
"s_add_u32 s8, s8, 4096\n"
"ds_read_b64_tr_b16 a[220:221], v209 offset:25088\n"
"ds_read_b64_tr_b16 a[222:223], v209 offset:29184\n"
"v_mfma_f32_16x16x32_bf16 v[36:39], a[156:159], a[60:63], v[36:39]\n"
"s_waitcnt lgkmcnt(14)\n"
"buffer_load_dwordx4 a[84:87], v251, s[40:43], s8 offen offset:3072 sc1\n"
"v_mfma_f32_16x16x32_bf16 v[8:11], a[192:195], a[32:35], v[8:11]\n"
"s_waitcnt lgkmcnt(12)\n"
"s_add_u32 s8, s8, 4096\n"
"ds_read_b64_tr_b16 a[128:129], v208 offset:1024\n"
"v_mfma_f32_16x16x32_bf16 v[8:11], a[196:199], a[36:39], v[8:11]\n"
"s_waitcnt lgkmcnt(11)\n"
"buffer_load_dwordx4 a[88:91], v251, s[40:43], s8 offen offset:3072 sc1\n"
"v_mfma_f32_16x16x32_bf16 v[8:11], a[200:203], a[40:43], v[8:11]\n"
"s_waitcnt lgkmcnt(9)\n"
"ds_read_b64_tr_b16 a[130:131], v208 offset:5120\n"
"ds_read_b64_tr_b16 a[132:133], v208 offset:9216\n"
"v_mfma_f32_16x16x32_bf16 v[8:11], a[204:207], a[44:47], v[8:11]\n"
"s_add_u32 s8, s8, 4096\n"
"ds_read_b64_tr_b16 a[134:135], v208 offset:13312\n"
"v_mfma_f32_16x16x32_bf16 v[40:43], a[192:195], a[48:51], v[40:43]\n"
"buffer_load_dwordx4 a[92:95], v251, s[40:43], s8 offen offset:3072 sc1\n"
"v_mfma_f32_16x16x32_bf16 v[40:43], a[196:199], a[52:55], v[40:43]\n"
"s_add_u32 s8, s8, 4096\n"
"s_add_u32 m0, s86, 0\n"
"ds_read_b64_tr_b16 a[136:137], v208 offset:17408\n"
"ds_read_b64_tr_b16 a[138:139], v208 offset:21504\n"
"v_mfma_f32_16x16x32_bf16 v[40:43], a[200:203], a[56:59], v[40:43]\n"
"buffer_load_dwordx4 v247, s[64:67], s85 offen lds\n"
"v_mfma_f32_16x16x32_bf16 v[40:43], a[204:207], a[60:63], v[40:43]\n"
"s_waitcnt lgkmcnt(12)\n"
"ds_read_b64_tr_b16 a[140:141], v208 offset:25600\n"
"ds_read_b64_tr_b16 a[142:143], v208 offset:29696\n"
"v_mfma_f32_16x16x32_bf16 v[12:15], a[208:211], a[32:35], v[12:15]\n"
"s_waitcnt lgkmcnt(12)\n"
"s_add_u32 m0, s86, 1024\n"
"ds_read_b64_tr_b16 a[144:145], v209 offset:1024\n"
"v_mfma_f32_16x16x32_bf16 v[12:15], a[212:215], a[36:39], v[12:15]\n"
"s_waitcnt lgkmcnt(11)\n"
"buffer_load_dwordx4 v248, s[64:67], s85 offen lds\n"
"v_mfma_f32_16x16x32_bf16 v[12:15], a[216:219], a[40:43], v[12:15]\n"
"s_waitcnt lgkmcnt(9)\n"
"s_add_u32 s85, s85, 131072\n"
"s_add_u32 m0, s86, 8192\n"
"ds_read_b64_tr_b16 a[146:147], v209 offset:5120\n"
"ds_read_b64_tr_b16 a[148:149], v209 offset:9216\n"
"v_mfma_f32_16x16x32_bf16 v[12:15], a[220:223], a[44:47], v[12:15]\n"
"buffer_load_dwordx4 v247, s[64:67], s85 offen lds\n"
"v_mfma_f32_16x16x32_bf16 v[44:47], a[208:211], a[48:51], v[44:47]\n"
"ds_read_b64_tr_b16 a[150:151], v209 offset:13312\n"
"v_mfma_f32_16x16x32_bf16 v[44:47], a[212:215], a[52:55], v[44:47]\n"
"s_add_u32 m0, s86, 9216\n"
"ds_read_b64_tr_b16 a[152:153], v209 offset:17408\n"
"ds_read_b64_tr_b16 a[154:155], v209 offset:21504\n"
"v_mfma_f32_16x16x32_bf16 v[44:47], a[216:219], a[56:59], v[44:47]\n"
"buffer_load_dwordx4 v248, s[64:67], s85 offen lds\n"
"v_mfma_f32_16x16x32_bf16 v[44:47], a[220:223], a[60:63], v[44:47]\n"
"s_waitcnt lgkmcnt(12)\n"
"s_add_u32 s85, s85, 131072\n"
"s_add_u32 m0, s86, 16384\n"
"ds_read_b64_tr_b16 a[156:157], v209 offset:25600\n"
"ds_read_b64_tr_b16 a[158:159], v209 offset:29696\n"
"v_mfma_f32_16x16x32_bf16 v[16:19], a[128:131], a[32:35], v[16:19]\n"
"s_waitcnt lgkmcnt(12)\n"
"buffer_load_dwordx4 v247, s[64:67], s85 offen lds\n"
"v_mfma_f32_16x16x32_bf16 v[16:19], a[132:135], a[36:39], v[16:19]\n"
"s_waitcnt lgkmcnt(10)\n"
"s_add_u32 m0, s86, 17408\n"
"ds_read_b64_tr_b16 a[192:193], v208 offset:1536\n"
"ds_read_b64_tr_b16 a[194:195], v208 offset:5632\n"
"v_mfma_f32_16x16x32_bf16 v[16:19], a[136:139], a[40:43], v[16:19]\n"
"s_waitcnt lgkmcnt(10)\n"
"buffer_load_dwordx4 v248, s[64:67], s85 offen lds\n"
"v_mfma_f32_16x16x32_bf16 v[16:19], a[140:143], a[44:47], v[16:19]\n"
"s_add_u32 s85, s85, 131072\n"
"ds_read_b64_tr_b16 a[196:197], v208 offset:9728\n"
"ds_read_b64_tr_b16 a[198:199], v208 offset:13824\n"
"v_mfma_f32_16x16x32_bf16 v[48:51], a[128:131], a[48:51], v[48:51]\n"
"s_add_u32 m0, s86, 24576\n"
"ds_read_b64_tr_b16 a[200:201], v208 offset:17920\n"
"ds_read_b64_tr_b16 a[202:203], v208 offset:22016\n"
"v_mfma_f32_16x16x32_bf16 v[48:51], a[132:135], a[52:55], v[48:51]\n"
"buffer_load_dwordx4 v247, s[64:67], s85 offen lds\n"
"v_mfma_f32_16x16x32_bf16 v[48:51], a[136:139], a[56:59], v[48:51]\n"
"s_add_u32 m0, s86, 25600\n"
"ds_read_b64_tr_b16 a[204:205], v208 offset:26112\n"
"ds_read_b64_tr_b16 a[206:207], v208 offset:30208\n"
"v_mfma_f32_16x16x32_bf16 v[48:51], a[140:143], a[60:63], v[48:51]\n"
"s_waitcnt lgkmcnt(14)\n"
"buffer_load_dwordx4 v248, s[64:67], s85 offen lds\n"
"v_mfma_f32_16x16x32_bf16 v[20:23], a[144:147], a[32:35], v[20:23]\n"
"s_waitcnt lgkmcnt(12)\n"
"ds_read_b64_tr_b16 a[208:209], v209 offset:1536\n"
"v_mfma_f32_16x16x32_bf16 v[20:23], a[148:151], a[36:39], v[20:23]\n"
"s_waitcnt lgkmcnt(11)\n"
"s_sub_u32 s85, s85, 884736\n"
"ds_read_b64_tr_b16 a[210:211], v209 offset:5632\n"
"ds_read_b64_tr_b16 a[212:213], v209 offset:9728\n"
"v_mfma_f32_16x16x32_bf16 v[20:23], a[152:155], a[40:43], v[20:23]\n"
"s_waitcnt lgkmcnt(11)\n"
"buffer_load_dwordx4 a[96:99], v251, s[40:43], s7 offen offset:3072 sc1\n"
"v_mfma_f32_16x16x32_bf16 v[20:23], a[156:159], a[44:47], v[20:23]\n"
"s_add_u32 s7, s7, 4096\n"
"ds_read_b64_tr_b16 a[214:215], v209 offset:13824\n"
"v_mfma_f32_16x16x32_bf16 v[52:55], a[144:147], a[48:51], v[52:55]\n"
"buffer_load_dwordx4 a[100:103], v251, s[40:43], s7 offen offset:3072 sc1\n"
"v_mfma_f32_16x16x32_bf16 v[52:55], a[148:151], a[52:55], v[52:55]\n"
"ds_read_b64_tr_b16 a[216:217], v209 offset:17920\n"
"ds_read_b64_tr_b16 a[218:219], v209 offset:22016\n"
"v_mfma_f32_16x16x32_bf16 v[52:55], a[152:155], a[56:59], v[52:55]\n"
"s_add_u32 s7, s7, 4096\n"
"ds_read_b64_tr_b16 a[220:221], v209 offset:26112\n"
"ds_read_b64_tr_b16 a[222:223], v209 offset:30208\n"
"v_mfma_f32_16x16x32_bf16 v[52:55], a[156:159], a[60:63], v[52:55]\n"
"s_waitcnt lgkmcnt(14)\n"
"buffer_load_dwordx4 a[104:107], v251, s[40:43], s7 offen offset:3072 sc1\n"
"v_add_u32 v208, 32768, v208\n"
"v_add_u32 v209, 32768, v209\n"
"v_mfma_f32_16x16x32_bf16 v[24:27], a[192:195], a[32:35], v[24:27]\n"
"s_waitcnt vmcnt(20)\n"
"s_barrier\n"
"s_add_u32 s7, s7, 4096\n"
"ds_read_b64_tr_b16 a[128:129], v208 offset:1024\n"
"s_waitcnt lgkmcnt(13)\n"
"v_mfma_f32_16x16x32_bf16 v[24:27], a[196:199], a[36:39], v[24:27]\n"
"s_waitcnt lgkmcnt(11)\n"
"buffer_load_dwordx4 a[108:111], v251, s[40:43], s7 offen offset:3072 sc1\n"
"v_mfma_f32_16x16x32_bf16 v[24:27], a[200:203], a[40:43], v[24:27]\n"
"s_waitcnt lgkmcnt(9)\n"
"ds_read_b64_tr_b16 a[130:131], v208 offset:5120\n"
"ds_read_b64_tr_b16 a[132:133], v208 offset:9216\n"
"v_mfma_f32_16x16x32_bf16 v[24:27], a[204:207], a[44:47], v[24:27]\n"
"s_sub_u32 s7, s7, 28672\n"
"ds_read_b64_tr_b16 a[134:135], v208 offset:13312\n"
"v_mfma_f32_16x16x32_bf16 v[56:59], a[192:195], a[48:51], v[56:59]\n"
"buffer_load_dwordx4 a[112:115], v251, s[40:43], s8 offen offset:3072 sc1\n"
"v_mfma_f32_16x16x32_bf16 v[56:59], a[196:199], a[52:55], v[56:59]\n"
"s_add_u32 s8, s8, 4096\n"
"ds_read_b64_tr_b16 a[136:137], v208 offset:17408\n"
"ds_read_b64_tr_b16 a[138:139], v208 offset:21504\n"
"v_mfma_f32_16x16x32_bf16 v[56:59], a[200:203], a[56:59], v[56:59]\n"
"buffer_load_dwordx4 a[116:119], v251, s[40:43], s8 offen offset:3072 sc1\n"
"v_mfma_f32_16x16x32_bf16 v[56:59], a[204:207], a[60:63], v[56:59]\n"
"s_waitcnt lgkmcnt(12)\n"
"ds_read_b64_tr_b16 a[140:141], v208 offset:25600\n"
"ds_read_b64_tr_b16 a[142:143], v208 offset:29696\n"
"v_mfma_f32_16x16x32_bf16 v[28:31], a[208:211], a[32:35], v[28:31]\n"
"s_waitcnt lgkmcnt(12)\n"
"s_add_u32 s8, s8, 4096\n"
"ds_read_b64_tr_b16 a[144:145], v209 offset:1024\n"
"v_mfma_f32_16x16x32_bf16 v[28:31], a[212:215], a[36:39], v[28:31]\n"
"s_waitcnt lgkmcnt(11)\n"
"buffer_load_dwordx4 a[120:123], v251, s[40:43], s8 offen offset:3072 sc1\n"
"v_mfma_f32_16x16x32_bf16 v[28:31], a[216:219], a[40:43], v[28:31]\n"
"s_waitcnt lgkmcnt(9)\n"
"s_add_u32 s8, s8, 4096\n"
"ds_read_b64_tr_b16 a[146:147], v209 offset:5120\n"
"ds_read_b64_tr_b16 a[148:149], v209 offset:9216\n"
"v_mfma_f32_16x16x32_bf16 v[28:31], a[220:223], a[44:47], v[28:31]\n"
"buffer_load_dwordx4 a[124:127], v251, s[40:43], s8 offen offset:3072 sc1\n"
"v_mfma_f32_16x16x32_bf16 v[60:63], a[208:211], a[48:51], v[60:63]\n"
"s_sub_u32 s8, s8, 28672\n"
"ds_read_b64_tr_b16 a[150:151], v209 offset:13312\n"
"v_mfma_f32_16x16x32_bf16 v[60:63], a[212:215], a[52:55], v[60:63]\n"
"ds_read_b64_tr_b16 a[152:153], v209 offset:17408\n"
"v_mfma_f32_16x16x32_bf16 v[60:63], a[216:219], a[56:59], v[60:63]\n"
"ds_read_b64_tr_b16 a[154:155], v209 offset:21504\n"
"ds_read_b64_tr_b16 a[156:157], v209 offset:25600\n"
"v_mfma_f32_16x16x32_bf16 v[60:63], a[220:223], a[60:63], v[60:63]\n"
"ds_read_b64_tr_b16 a[158:159], v209 offset:29696\n"
"s_waitcnt vmcnt(19) lgkmcnt(14)\n"
"v_mfma_f32_16x16x32_bf16 v[48:51], a[128:131], a[80:83], v[48:51]\n"
"s_waitcnt vmcnt(18) lgkmcnt(12)\n"
"ds_read_b64_tr_b16 a[192:193], v208 offset:1536\n"
"v_mfma_f32_16x16x32_bf16 v[48:51], a[132:135], a[84:87], v[48:51]\n"
"s_waitcnt vmcnt(17) lgkmcnt(11)\n"
"s_add_u32 m0, s87, 32768\n"
"ds_read_b64_tr_b16 a[194:195], v208 offset:5632\n"
"ds_read_b64_tr_b16 a[196:197], v208 offset:9728\n"
"v_mfma_f32_16x16x32_bf16 v[48:51], a[136:139], a[88:91], v[48:51]\n"
"s_waitcnt vmcnt(16) lgkmcnt(11)\n"
"buffer_load_dwordx4 v247, s[60:63], s84 offen lds\n"
"v_mfma_f32_16x16x32_bf16 v[48:51], a[140:143], a[92:95], v[48:51]\n"
"s_add_u32 s84, s84, 32768\n"
"ds_read_b64_tr_b16 a[198:199], v208 offset:13824\n"
"v_mfma_f32_16x16x32_bf16 v[16:19], a[128:131], a[64:67], v[16:19]\n"
"s_add_u32 m0, s87, 36864\n"
"ds_read_b64_tr_b16 a[200:201], v208 offset:17920\n"
"v_mfma_f32_16x16x32_bf16 v[16:19], a[132:135], a[68:71], v[16:19]\n"
"buffer_load_dwordx4 v247, s[60:63], s84 offen lds\n"
"v_mfma_f32_16x16x32_bf16 v[16:19], a[136:139], a[72:75], v[16:19]\n"
"s_add_u32 s84, s84, 32768\n"
"ds_read_b64_tr_b16 a[202:203], v208 offset:22016\n"
"ds_read_b64_tr_b16 a[204:205], v208 offset:26112\n"
"v_mfma_f32_16x16x32_bf16 v[16:19], a[140:143], a[76:79], v[16:19]\n"
"s_waitcnt lgkmcnt(13)\n"
"s_add_u32 m0, s87, 40960\n"
"ds_read_b64_tr_b16 a[206:207], v208 offset:30208\n"
"v_mfma_f32_16x16x32_bf16 v[52:55], a[144:147], a[80:83], v[52:55]\n"
"s_waitcnt lgkmcnt(12)\n"
"buffer_load_dwordx4 v247, s[60:63], s84 offen lds\n"
"v_mfma_f32_16x16x32_bf16 v[52:55], a[148:151], a[84:87], v[52:55]\n"
"s_waitcnt lgkmcnt(10)\n"
"s_add_u32 s84, s84, 32768\n"
"ds_read_b64_tr_b16 a[208:209], v209 offset:1536\n"
"v_mfma_f32_16x16x32_bf16 v[52:55], a[152:155], a[88:91], v[52:55]\n"
"s_waitcnt lgkmcnt(9)\n"
"s_add_u32 m0, s87, 45056\n"
"ds_read_b64_tr_b16 a[210:211], v209 offset:5632\n"
"ds_read_b64_tr_b16 a[212:213], v209 offset:9728\n"
"v_mfma_f32_16x16x32_bf16 v[52:55], a[156:159], a[92:95], v[52:55]\n"
"buffer_load_dwordx4 v247, s[60:63], s84 offen lds\n"
"v_mfma_f32_16x16x32_bf16 v[20:23], a[144:147], a[64:67], v[20:23]\n"
"s_add_u32 s84, s84, 32768\n"
"ds_read_b64_tr_b16 a[214:215], v209 offset:13824\n"
"v_mfma_f32_16x16x32_bf16 v[20:23], a[148:151], a[68:71], v[20:23]\n"
"s_add_u32 m0, s87, 49152\n"
"ds_read_b64_tr_b16 a[216:217], v209 offset:17920\n"
"ds_read_b64_tr_b16 a[218:219], v209 offset:22016\n"
"v_mfma_f32_16x16x32_bf16 v[20:23], a[152:155], a[72:75], v[20:23]\n"
"buffer_load_dwordx4 v247, s[60:63], s84 offen lds\n"
"v_mfma_f32_16x16x32_bf16 v[20:23], a[156:159], a[76:79], v[20:23]\n"
"s_waitcnt lgkmcnt(12)\n"
"s_add_u32 s84, s84, 32768\n"
"ds_read_b64_tr_b16 a[220:221], v209 offset:26112\n"
"ds_read_b64_tr_b16 a[222:223], v209 offset:30208\n"
"v_subrev_u32 v216, 131072, v208\n"
"v_subrev_u32 v217, 131072, v209\n"
"v_mfma_f32_16x16x32_bf16 v[56:59], a[192:195], a[80:83], v[56:59]\n"
"s_waitcnt vmcnt(13)\n"
"s_barrier\n"
"s_add_u32 m0, s87, 53248\n"
"ds_read_b64_tr_b16 a[128:129], v216 offset:1024\n"
"s_waitcnt lgkmcnt(13)\n"
"v_mfma_f32_16x16x32_bf16 v[56:59], a[196:199], a[84:87], v[56:59]\n"
"s_waitcnt lgkmcnt(11)\n"
"buffer_load_dwordx4 v247, s[60:63], s84 offen lds\n"
"v_mfma_f32_16x16x32_bf16 v[56:59], a[200:203], a[88:91], v[56:59]\n"
"s_waitcnt lgkmcnt(9)\n"
"s_add_u32 s84, s84, 32768\n"
"ds_read_b64_tr_b16 a[130:131], v216 offset:5120\n"
"ds_read_b64_tr_b16 a[132:133], v216 offset:9216\n"
"v_mfma_f32_16x16x32_bf16 v[56:59], a[204:207], a[92:95], v[56:59]\n"
"s_add_u32 m0, s87, 57344\n"
"ds_read_b64_tr_b16 a[134:135], v216 offset:13312\n"
"v_mfma_f32_16x16x32_bf16 v[24:27], a[192:195], a[64:67], v[24:27]\n"
"buffer_load_dwordx4 v247, s[60:63], s84 offen lds\n"
"v_mfma_f32_16x16x32_bf16 v[24:27], a[196:199], a[68:71], v[24:27]\n"
"s_add_u32 s84, s84, 32768\n"
"ds_read_b64_tr_b16 a[136:137], v216 offset:17408\n"
"ds_read_b64_tr_b16 a[138:139], v216 offset:21504\n"
"v_mfma_f32_16x16x32_bf16 v[24:27], a[200:203], a[72:75], v[24:27]\n"
"s_add_u32 m0, s87, 61440\n"
"ds_read_b64_tr_b16 a[140:141], v216 offset:25600\n"
"ds_read_b64_tr_b16 a[142:143], v216 offset:29696\n"
"v_mfma_f32_16x16x32_bf16 v[24:27], a[204:207], a[76:79], v[24:27]\n"
"s_waitcnt lgkmcnt(14)\n"
"buffer_load_dwordx4 v247, s[60:63], s84 offen lds\n"
"v_mfma_f32_16x16x32_bf16 v[60:63], a[208:211], a[80:83], v[60:63]\n"
"s_waitcnt lgkmcnt(12)\n"
"s_add_u32 s84, s84, 32768\n"
"ds_read_b64_tr_b16 a[144:145], v217 offset:1024\n"
"v_mfma_f32_16x16x32_bf16 v[60:63], a[212:215], a[84:87], v[60:63]\n"
"s_waitcnt lgkmcnt(11)\n"
"s_add_u32 m0, s87, 65536\n"
"ds_read_b64_tr_b16 a[146:147], v217 offset:5120\n"
"ds_read_b64_tr_b16 a[148:149], v217 offset:9216\n"
"v_mfma_f32_16x16x32_bf16 v[60:63], a[216:219], a[88:91], v[60:63]\n"
"s_waitcnt lgkmcnt(11)\n"
"buffer_load_dwordx4 v247, s[60:63], s84 offen lds\n"
"v_mfma_f32_16x16x32_bf16 v[60:63], a[220:223], a[92:95], v[60:63]\n"
"s_add_u32 s84, s84, 32768\n"
"ds_read_b64_tr_b16 a[150:151], v217 offset:13312\n"
"v_mfma_f32_16x16x32_bf16 v[28:31], a[208:211], a[64:67], v[28:31]\n"
"s_add_u32 m0, s87, 69632\n"
"ds_read_b64_tr_b16 a[152:153], v217 offset:17408\n"
"v_mfma_f32_16x16x32_bf16 v[28:31], a[212:215], a[68:71], v[28:31]\n"
"buffer_load_dwordx4 v247, s[60:63], s84 offen lds\n"
"v_mfma_f32_16x16x32_bf16 v[28:31], a[216:219], a[72:75], v[28:31]\n"
"s_add_u32 s84, s84, 32768\n"
"ds_read_b64_tr_b16 a[154:155], v217 offset:21504\n"
"ds_read_b64_tr_b16 a[156:157], v217 offset:25600\n"
"v_mfma_f32_16x16x32_bf16 v[28:31], a[220:223], a[76:79], v[28:31]\n"
"s_waitcnt vmcnt(13) lgkmcnt(13)\n"
"s_add_u32 m0, s87, 73728\n"
"ds_read_b64_tr_b16 a[158:159], v217 offset:29696\n"
"v_mfma_f32_16x16x32_bf16 v[48:51], a[128:131], a[112:115], v[48:51]\n"
"s_waitcnt vmcnt(12) lgkmcnt(12)\n"
"buffer_load_dwordx4 v247, s[60:63], s84 offen lds\n"
"v_mfma_f32_16x16x32_bf16 v[48:51], a[132:135], a[116:119], v[48:51]\n"
"s_waitcnt vmcnt(12) lgkmcnt(10)\n"
"s_add_u32 s84, s84, 32768\n"
"ds_read_b64_tr_b16 a[192:193], v216 offset:1536\n"
"v_mfma_f32_16x16x32_bf16 v[48:51], a[136:139], a[120:123], v[48:51]\n"
"s_waitcnt vmcnt(11) lgkmcnt(9)\n"
"s_add_u32 m0, s87, 77824\n"
"ds_read_b64_tr_b16 a[194:195], v216 offset:5632\n"
"ds_read_b64_tr_b16 a[196:197], v216 offset:9728\n"
"v_mfma_f32_16x16x32_bf16 v[48:51], a[140:143], a[124:127], v[48:51]\n"
"buffer_load_dwordx4 v247, s[60:63], s84 offen lds\n"
"v_mfma_f32_16x16x32_bf16 v[16:19], a[128:131], a[96:99], v[16:19]\n"
"s_add_u32 s84, s84, 32768\n"
"ds_read_b64_tr_b16 a[198:199], v216 offset:13824\n"
"v_mfma_f32_16x16x32_bf16 v[16:19], a[132:135], a[100:103], v[16:19]\n"
"s_add_u32 m0, s87, 81920\n"
"ds_read_b64_tr_b16 a[200:201], v216 offset:17920\n"
"ds_read_b64_tr_b16 a[202:203], v216 offset:22016\n"
"v_mfma_f32_16x16x32_bf16 v[16:19], a[136:139], a[104:107], v[16:19]\n"
"buffer_load_dwordx4 v247, s[60:63], s84 offen lds\n"
"v_mfma_f32_16x16x32_bf16 v[16:19], a[140:143], a[108:111], v[16:19]\n"
"s_waitcnt lgkmcnt(12)\n"
"s_add_u32 s84, s84, 32768\n"
"ds_read_b64_tr_b16 a[204:205], v216 offset:26112\n"
"ds_read_b64_tr_b16 a[206:207], v216 offset:30208\n"
"v_mfma_f32_16x16x32_bf16 v[52:55], a[144:147], a[112:115], v[52:55]\n"
"s_waitcnt lgkmcnt(12)\n"
"s_add_u32 m0, s87, 86016\n"
"ds_read_b64_tr_b16 a[208:209], v217 offset:1536\n"
"v_cvt_pk_bf16_f32 v96, v48, v49\n"
"v_cvt_pk_bf16_f32 v97, v50, v51\n"
"v_mfma_f32_16x16x32_bf16 v[52:55], a[148:151], a[116:119], v[52:55]\n"
"s_waitcnt lgkmcnt(11)\n"
"buffer_load_dwordx4 v247, s[60:63], s84 offen lds\n"
"v_mfma_f32_16x16x32_bf16 v[52:55], a[152:155], a[120:123], v[52:55]\n"
"s_waitcnt lgkmcnt(9)\n"
"s_add_u32 s84, s84, 32768\n"
"ds_read_b64_tr_b16 a[210:211], v217 offset:5632\n"
"ds_read_b64_tr_b16 a[212:213], v217 offset:9728\n"
"v_mfma_f32_16x16x32_bf16 v[52:55], a[156:159], a[124:127], v[52:55]\n"
"s_add_u32 m0, s87, 90112\n"
"ds_read_b64_tr_b16 a[214:215], v217 offset:13824\n"
"v_cvt_pk_bf16_f32 v112, v16, v17\n"
"v_mfma_f32_16x16x32_bf16 v[20:23], a[144:147], a[96:99], v[20:23]\n"
"buffer_load_dwordx4 v247, s[60:63], s84 offen lds\n"
"v_cvt_pk_bf16_f32 v113, v18, v19\n"
"v_mfma_f32_16x16x32_bf16 v[20:23], a[148:151], a[100:103], v[20:23]\n"
"s_add_u32 s84, s84, 32768\n"
"ds_read_b64_tr_b16 a[216:217], v217 offset:17920\n"
"ds_read_b64_tr_b16 a[218:219], v217 offset:22016\n"
"v_mfma_f32_16x16x32_bf16 v[20:23], a[152:155], a[104:107], v[20:23]\n"
"s_add_u32 m0, s87, 94208\n"
"ds_read_b64_tr_b16 a[220:221], v217 offset:26112\n"
"ds_read_b64_tr_b16 a[222:223], v217 offset:30208\n"
"v_mfma_f32_16x16x32_bf16 v[20:23], a[156:159], a[108:111], v[20:23]\n"
"s_waitcnt lgkmcnt(14)\n"
"buffer_load_dwordx4 v247, s[60:63], s84 offen lds\n"
"v_mfma_f32_16x16x32_bf16 v[56:59], a[192:195], a[112:115], v[56:59]\n"
"s_waitcnt lgkmcnt(12)\n"
"s_add_u32 s84, s84, 32768\n"
"ds_read_b64_tr_b16 a[128:129], v208 offset:0\n"
"v_cvt_pk_bf16_f32 v98, v52, v53\n"
"v_cvt_pk_bf16_f32 v99, v54, v55\n"
"v_mfma_f32_16x16x32_bf16 v[56:59], a[196:199], a[116:119], v[56:59]\n"
"s_waitcnt lgkmcnt(11)\n"
"s_add_u32 m0, s87, 98304\n"
"ds_read_b64_tr_b16 a[130:131], v208 offset:4096\n"
"ds_read_b64_tr_b16 a[132:133], v208 offset:8192\n"
"buffer_store_dwordx4 v[96:99], v251, s[40:43], s90 offen sc1\n"
"v_mfma_f32_16x16x32_bf16 v[56:59], a[200:203], a[120:123], v[56:59]\n"
"s_waitcnt lgkmcnt(11)\n"
"buffer_load_dwordx4 v247, s[60:63], s84 offen lds\n"
"v_mfma_f32_16x16x32_bf16 v[56:59], a[204:207], a[124:127], v[56:59]\n"
"s_add_u32 s84, s84, 32768\n"
"ds_read_b64_tr_b16 a[134:135], v208 offset:12288\n"
"v_cvt_pk_bf16_f32 v114, v20, v21\n"
"v_mfma_f32_16x16x32_bf16 v[24:27], a[192:195], a[96:99], v[24:27]\n"
"s_add_u32 m0, s87, 102400\n"
"ds_read_b64_tr_b16 a[136:137], v208 offset:16384\n"
"v_cvt_pk_bf16_f32 v115, v22, v23\n"
"v_mfma_f32_16x16x32_bf16 v[24:27], a[196:199], a[100:103], v[24:27]\n"
"buffer_load_dwordx4 v247, s[60:63], s84 offen lds\n"
"buffer_store_dwordx4 v[112:115], v251, s[40:43], s88 offen sc1\n"
"v_mfma_f32_16x16x32_bf16 v[24:27], a[200:203], a[104:107], v[24:27]\n"
"s_add_u32 s84, s84, 32768\n"
"ds_read_b64_tr_b16 a[138:139], v208 offset:20480\n"
"ds_read_b64_tr_b16 a[140:141], v208 offset:24576\n"
"v_mfma_f32_16x16x32_bf16 v[24:27], a[204:207], a[108:111], v[24:27]\n"
"s_waitcnt lgkmcnt(13)\n"
"s_add_u32 m0, s87, 106496\n"
"ds_read_b64_tr_b16 a[142:143], v208 offset:28672\n"
"v_cvt_pk_bf16_f32 v100, v56, v57\n"
"v_mfma_f32_16x16x32_bf16 v[60:63], a[208:211], a[112:115], v[60:63]\n"
"s_waitcnt lgkmcnt(12)\n"
"buffer_load_dwordx4 v247, s[60:63], s84 offen lds\n"
"v_cvt_pk_bf16_f32 v101, v58, v59\n"
"v_mfma_f32_16x16x32_bf16 v[60:63], a[212:215], a[116:119], v[60:63]\n"
"s_waitcnt lgkmcnt(10)\n"
"s_add_u32 s84, s84, 32768\n"
"ds_read_b64_tr_b16 a[144:145], v209 offset:0\n"
"v_mfma_f32_16x16x32_bf16 v[60:63], a[216:219], a[120:123], v[60:63]\n"
"s_waitcnt lgkmcnt(9)\n"
"ds_read_b64_tr_b16 a[146:147], v209 offset:4096\n"
"ds_read_b64_tr_b16 a[148:149], v209 offset:8192\n"
"v_mfma_f32_16x16x32_bf16 v[60:63], a[220:223], a[124:127], v[60:63]\n"
"ds_read_b64_tr_b16 a[150:151], v209 offset:12288\n"
"v_cvt_pk_bf16_f32 v116, v24, v25\n"
"v_mfma_f32_16x16x32_bf16 v[28:31], a[208:211], a[96:99], v[28:31]\n"
"ds_read_b64_tr_b16 a[152:153], v209 offset:16384\n"
"v_cvt_pk_bf16_f32 v117, v26, v27\n"
"v_mfma_f32_16x16x32_bf16 v[28:31], a[212:215], a[100:103], v[28:31]\n"
"ds_read_b64_tr_b16 a[154:155], v209 offset:20480\n"
"v_mfma_f32_16x16x32_bf16 v[28:31], a[216:219], a[104:107], v[28:31]\n"
"ds_read_b64_tr_b16 a[156:157], v209 offset:24576\n"
"v_mfma_f32_16x16x32_bf16 v[28:31], a[220:223], a[108:111], v[28:31]\n"
"ds_read_b64_tr_b16 a[158:159], v209 offset:28672\n"
"v_cvt_pk_bf16_f32 v102, v60, v61\n"
"s_waitcnt lgkmcnt(14)\n"
"v_mfma_f32_16x16x32_bf16 v[32:35], a[128:131], a[80:83], v[32:35]\n"
"s_waitcnt lgkmcnt(12)\n"
"ds_read_b64_tr_b16 a[192:193], v208 offset:512\n"
"ds_read_b64_tr_b16 a[194:195], v208 offset:4608\n"
"v_cvt_pk_bf16_f32 v103, v62, v63\n"
"v_mfma_f32_16x16x32_bf16 v[32:35], a[132:135], a[84:87], v[32:35]\n"
"s_waitcnt lgkmcnt(12)\n"
"ds_read_b64_tr_b16 a[196:197], v208 offset:8704\n"
"ds_read_b64_tr_b16 a[198:199], v208 offset:12800\n"
"buffer_store_dwordx4 v[100:103], v251, s[40:43], s90 offen offset:1024 sc1\n"
"v_mfma_f32_16x16x32_bf16 v[32:35], a[136:139], a[88:91], v[32:35]\n"
"s_waitcnt lgkmcnt(12)\n"
"ds_read_b64_tr_b16 a[200:201], v208 offset:16896\n"
"ds_read_b64_tr_b16 a[202:203], v208 offset:20992\n"
"v_mfma_f32_16x16x32_bf16 v[32:35], a[140:143], a[92:95], v[32:35]\n"
"s_waitcnt lgkmcnt(12)\n"
"ds_read_b64_tr_b16 a[204:205], v208 offset:25088\n"
"ds_read_b64_tr_b16 a[206:207], v208 offset:29184\n"
"v_mfma_f32_16x16x32_bf16 v[36:39], a[144:147], a[80:83], v[36:39]\n"
"s_waitcnt lgkmcnt(12)\n"
"ds_read_b64_tr_b16 a[208:209], v209 offset:512\n"
"ds_read_b64_tr_b16 a[210:211], v209 offset:4608\n"
"v_cvt_pk_bf16_f32 v118, v28, v29\n"
"v_cvt_pk_bf16_f32 v119, v30, v31\n"
"v_mfma_f32_16x16x32_bf16 v[36:39], a[148:151], a[84:87], v[36:39]\n"
"s_waitcnt lgkmcnt(12)\n"
"ds_read_b64_tr_b16 a[212:213], v209 offset:8704\n"
"ds_read_b64_tr_b16 a[214:215], v209 offset:12800\n"
"buffer_store_dwordx4 v[116:119], v251, s[40:43], s88 offen offset:1024 sc1\n"
"v_mfma_f32_16x16x32_bf16 v[36:39], a[152:155], a[88:91], v[36:39]\n"
"s_waitcnt lgkmcnt(12)\n"
"ds_read_b64_tr_b16 a[216:217], v209 offset:16896\n"
"ds_read_b64_tr_b16 a[218:219], v209 offset:20992\n"
"v_mfma_f32_16x16x32_bf16 v[36:39], a[156:159], a[92:95], v[36:39]\n"
"s_waitcnt lgkmcnt(12)\n"
"ds_read_b64_tr_b16 a[220:221], v209 offset:25088\n"
"ds_read_b64_tr_b16 a[222:223], v209 offset:29184\n"
"v_mfma_f32_16x16x32_bf16 v[40:43], a[192:195], a[80:83], v[40:43]\n"
"s_waitcnt lgkmcnt(12)\n"
"ds_read_b64_tr_b16 a[128:129], v216 offset:0\n"
"ds_read_b64_tr_b16 a[130:131], v216 offset:4096\n"
"v_mfma_f32_16x16x32_bf16 v[40:43], a[196:199], a[84:87], v[40:43]\n"
"s_waitcnt lgkmcnt(12)\n"
"ds_read_b64_tr_b16 a[132:133], v216 offset:8192\n"
"ds_read_b64_tr_b16 a[134:135], v216 offset:12288\n"
"v_mfma_f32_16x16x32_bf16 v[40:43], a[200:203], a[88:91], v[40:43]\n"
"s_waitcnt lgkmcnt(12)\n"
"ds_read_b64_tr_b16 a[136:137], v216 offset:16384\n"
"ds_read_b64_tr_b16 a[138:139], v216 offset:20480\n"
"v_mfma_f32_16x16x32_bf16 v[40:43], a[204:207], a[92:95], v[40:43]\n"
"s_waitcnt lgkmcnt(12)\n"
"ds_read_b64_tr_b16 a[140:141], v216 offset:24576\n"
"ds_read_b64_tr_b16 a[142:143], v216 offset:28672\n"
"v_mfma_f32_16x16x32_bf16 v[44:47], a[208:211], a[80:83], v[44:47]\n"
"s_waitcnt lgkmcnt(12)\n"
"ds_read_b64_tr_b16 a[144:145], v217 offset:0\n"
"ds_read_b64_tr_b16 a[146:147], v217 offset:4096\n"
"v_mfma_f32_16x16x32_bf16 v[44:47], a[212:215], a[84:87], v[44:47]\n"
"s_waitcnt lgkmcnt(12)\n"
"ds_read_b64_tr_b16 a[148:149], v217 offset:8192\n"
"ds_read_b64_tr_b16 a[150:151], v217 offset:12288\n"
"v_mfma_f32_16x16x32_bf16 v[44:47], a[216:219], a[88:91], v[44:47]\n"
"s_waitcnt lgkmcnt(12)\n"
"ds_read_b64_tr_b16 a[152:153], v217 offset:16384\n"
"ds_read_b64_tr_b16 a[154:155], v217 offset:20480\n"
"v_mfma_f32_16x16x32_bf16 v[44:47], a[220:223], a[92:95], v[44:47]\n"
"s_waitcnt lgkmcnt(12)\n"
"ds_read_b64_tr_b16 a[156:157], v217 offset:24576\n"
"ds_read_b64_tr_b16 a[158:159], v217 offset:28672\n"
"v_mfma_f32_16x16x32_bf16 v[32:35], a[128:131], a[112:115], v[32:35]\n"
"s_waitcnt lgkmcnt(12)\n"
"ds_read_b64_tr_b16 a[192:193], v216 offset:512\n"
"ds_read_b64_tr_b16 a[194:195], v216 offset:4608\n"
"v_mfma_f32_16x16x32_bf16 v[32:35], a[132:135], a[116:119], v[32:35]\n"
"s_waitcnt lgkmcnt(12)\n"
"ds_read_b64_tr_b16 a[196:197], v216 offset:8704\n"
"ds_read_b64_tr_b16 a[198:199], v216 offset:12800\n"
"v_mfma_f32_16x16x32_bf16 v[32:35], a[136:139], a[120:123], v[32:35]\n"
"s_waitcnt lgkmcnt(12)\n"
"ds_read_b64_tr_b16 a[200:201], v216 offset:16896\n"
"ds_read_b64_tr_b16 a[202:203], v216 offset:20992\n"
"v_mfma_f32_16x16x32_bf16 v[32:35], a[140:143], a[124:127], v[32:35]\n"
"s_waitcnt lgkmcnt(12)\n"
"ds_read_b64_tr_b16 a[204:205], v216 offset:25088\n"
"ds_read_b64_tr_b16 a[206:207], v216 offset:29184\n"
"v_mfma_f32_16x16x32_bf16 v[36:39], a[144:147], a[112:115], v[36:39]\n"
"s_waitcnt lgkmcnt(12)\n"
"ds_read_b64_tr_b16 a[208:209], v217 offset:512\n"
"ds_read_b64_tr_b16 a[210:211], v217 offset:4608\n"
"v_mfma_f32_16x16x32_bf16 v[36:39], a[148:151], a[116:119], v[36:39]\n"
"s_waitcnt lgkmcnt(12)\n"
"ds_read_b64_tr_b16 a[212:213], v217 offset:8704\n"
"ds_read_b64_tr_b16 a[214:215], v217 offset:12800\n"
"v_mfma_f32_16x16x32_bf16 v[36:39], a[152:155], a[120:123], v[36:39]\n"
"s_waitcnt lgkmcnt(12)\n"
"ds_read_b64_tr_b16 a[216:217], v217 offset:16896\n"
"ds_read_b64_tr_b16 a[218:219], v217 offset:20992\n"
"v_mfma_f32_16x16x32_bf16 v[36:39], a[156:159], a[124:127], v[36:39]\n"
"s_waitcnt lgkmcnt(12)\n"
"ds_read_b64_tr_b16 a[220:221], v217 offset:25088\n"
"ds_read_b64_tr_b16 a[222:223], v217 offset:29184\n"
"v_mfma_f32_16x16x32_bf16 v[40:43], a[192:195], a[112:115], v[40:43]\n"
"s_waitcnt lgkmcnt(12)\n"
"ds_read_b64_tr_b16 a[128:129], v208 offset:0\n"
"ds_read_b64_tr_b16 a[130:131], v208 offset:4096\n"
"v_cvt_pk_bf16_f32 v120, v32, v33\n"
"v_cvt_pk_bf16_f32 v121, v34, v35\n"
"v_mfma_f32_16x16x32_bf16 v[40:43], a[196:199], a[116:119], v[40:43]\n"
"s_waitcnt lgkmcnt(12)\n"
"ds_read_b64_tr_b16 a[132:133], v208 offset:8192\n"
"ds_read_b64_tr_b16 a[134:135], v208 offset:12288\n"
"v_mfma_f32_16x16x32_bf16 v[40:43], a[200:203], a[120:123], v[40:43]\n"
"s_waitcnt lgkmcnt(12)\n"
"ds_read_b64_tr_b16 a[136:137], v208 offset:16384\n"
"ds_read_b64_tr_b16 a[138:139], v208 offset:20480\n"
"v_mfma_f32_16x16x32_bf16 v[40:43], a[204:207], a[124:127], v[40:43]\n"
"s_waitcnt lgkmcnt(12)\n"
"ds_read_b64_tr_b16 a[140:141], v208 offset:24576\n"
"ds_read_b64_tr_b16 a[142:143], v208 offset:28672\n"
"v_mfma_f32_16x16x32_bf16 v[44:47], a[208:211], a[112:115], v[44:47]\n"
"s_waitcnt lgkmcnt(12)\n"
"ds_read_b64_tr_b16 a[144:145], v209 offset:0\n"
"ds_read_b64_tr_b16 a[146:147], v209 offset:4096\n"
"v_cvt_pk_bf16_f32 v122, v36, v37\n"
"v_cvt_pk_bf16_f32 v123, v38, v39\n"
"v_mfma_f32_16x16x32_bf16 v[44:47], a[212:215], a[116:119], v[44:47]\n"
"s_waitcnt lgkmcnt(12)\n"
"ds_read_b64_tr_b16 a[148:149], v209 offset:8192\n"
"ds_read_b64_tr_b16 a[150:151], v209 offset:12288\n"
"buffer_store_dwordx4 v[120:123], v251, s[40:43], s89 offen sc1\n"
"v_mfma_f32_16x16x32_bf16 v[44:47], a[216:219], a[120:123], v[44:47]\n"
"s_waitcnt lgkmcnt(12)\n"
"ds_read_b64_tr_b16 a[152:153], v209 offset:16384\n"
"ds_read_b64_tr_b16 a[154:155], v209 offset:20480\n"
"v_mfma_f32_16x16x32_bf16 v[44:47], a[220:223], a[124:127], v[44:47]\n"
"s_waitcnt lgkmcnt(12)\n"
"ds_read_b64_tr_b16 a[156:157], v209 offset:24576\n"
"ds_read_b64_tr_b16 a[158:159], v209 offset:28672\n"
"v_mfma_f32_16x16x32_bf16 v[0:3], a[128:131], a[64:67], v[0:3]\n"
"s_waitcnt lgkmcnt(12)\n"
"ds_read_b64_tr_b16 a[192:193], v208 offset:512\n"
"ds_read_b64_tr_b16 a[194:195], v208 offset:4608\n"
"v_cvt_pk_bf16_f32 v124, v40, v41\n"
"v_cvt_pk_bf16_f32 v125, v42, v43\n"
"v_mfma_f32_16x16x32_bf16 v[0:3], a[132:135], a[68:71], v[0:3]\n"
"s_waitcnt lgkmcnt(12)\n"
"ds_read_b64_tr_b16 a[196:197], v208 offset:8704\n"
"ds_read_b64_tr_b16 a[198:199], v208 offset:12800\n"
"v_mfma_f32_16x16x32_bf16 v[0:3], a[136:139], a[72:75], v[0:3]\n"
"s_waitcnt lgkmcnt(12)\n"
"ds_read_b64_tr_b16 a[200:201], v208 offset:16896\n"
"ds_read_b64_tr_b16 a[202:203], v208 offset:20992\n"
"v_mfma_f32_16x16x32_bf16 v[0:3], a[140:143], a[76:79], v[0:3]\n"
"s_waitcnt lgkmcnt(12)\n"
"ds_read_b64_tr_b16 a[204:205], v208 offset:25088\n"
"ds_read_b64_tr_b16 a[206:207], v208 offset:29184\n"
"v_mfma_f32_16x16x32_bf16 v[4:7], a[144:147], a[64:67], v[4:7]\n"
"s_waitcnt lgkmcnt(12)\n"
"ds_read_b64_tr_b16 a[208:209], v209 offset:512\n"
"ds_read_b64_tr_b16 a[210:211], v209 offset:4608\n"
"v_cvt_pk_bf16_f32 v126, v44, v45\n"
"v_cvt_pk_bf16_f32 v127, v46, v47\n"
"v_mfma_f32_16x16x32_bf16 v[4:7], a[148:151], a[68:71], v[4:7]\n"
"s_waitcnt lgkmcnt(12)\n"
"ds_read_b64_tr_b16 a[212:213], v209 offset:8704\n"
"ds_read_b64_tr_b16 a[214:215], v209 offset:12800\n"
"buffer_store_dwordx4 v[124:127], v251, s[40:43], s89 offen offset:1024 sc1\n"
"v_mfma_f32_16x16x32_bf16 v[4:7], a[152:155], a[72:75], v[4:7]\n"
"s_waitcnt lgkmcnt(12)\n"
"ds_read_b64_tr_b16 a[216:217], v209 offset:16896\n"
"ds_read_b64_tr_b16 a[218:219], v209 offset:20992\n"
"v_mfma_f32_16x16x32_bf16 v[4:7], a[156:159], a[76:79], v[4:7]\n"
"s_waitcnt lgkmcnt(12)\n"
"ds_read_b64_tr_b16 a[220:221], v209 offset:25088\n"
"ds_read_b64_tr_b16 a[222:223], v209 offset:29184\n"
"v_mfma_f32_16x16x32_bf16 v[8:11], a[192:195], a[64:67], v[8:11]\n"
"s_waitcnt lgkmcnt(12)\n"
"ds_read_b64_tr_b16 a[128:129], v216 offset:0\n"
"ds_read_b64_tr_b16 a[130:131], v216 offset:4096\n"
"v_mfma_f32_16x16x32_bf16 v[8:11], a[196:199], a[68:71], v[8:11]\n"
"s_waitcnt lgkmcnt(12)\n"
"ds_read_b64_tr_b16 a[132:133], v216 offset:8192\n"
"ds_read_b64_tr_b16 a[134:135], v216 offset:12288\n"
"v_mfma_f32_16x16x32_bf16 v[8:11], a[200:203], a[72:75], v[8:11]\n"
"s_waitcnt lgkmcnt(12)\n"
"ds_read_b64_tr_b16 a[136:137], v216 offset:16384\n"
"ds_read_b64_tr_b16 a[138:139], v216 offset:20480\n"
"v_mfma_f32_16x16x32_bf16 v[8:11], a[204:207], a[76:79], v[8:11]\n"
"s_waitcnt lgkmcnt(12)\n"
"ds_read_b64_tr_b16 a[140:141], v216 offset:24576\n"
"ds_read_b64_tr_b16 a[142:143], v216 offset:28672\n"
"v_mfma_f32_16x16x32_bf16 v[12:15], a[208:211], a[64:67], v[12:15]\n"
"s_waitcnt lgkmcnt(12)\n"
"ds_read_b64_tr_b16 a[144:145], v217 offset:0\n"
"ds_read_b64_tr_b16 a[146:147], v217 offset:4096\n"
"v_mfma_f32_16x16x32_bf16 v[12:15], a[212:215], a[68:71], v[12:15]\n"
"s_waitcnt lgkmcnt(12)\n"
"ds_read_b64_tr_b16 a[148:149], v217 offset:8192\n"
"ds_read_b64_tr_b16 a[150:151], v217 offset:12288\n"
"v_mfma_f32_16x16x32_bf16 v[12:15], a[216:219], a[72:75], v[12:15]\n"
"s_waitcnt lgkmcnt(12)\n"
"ds_read_b64_tr_b16 a[152:153], v217 offset:16384\n"
"ds_read_b64_tr_b16 a[154:155], v217 offset:20480\n"
"v_mfma_f32_16x16x32_bf16 v[12:15], a[220:223], a[76:79], v[12:15]\n"
"s_waitcnt lgkmcnt(12)\n"
"ds_read_b64_tr_b16 a[156:157], v217 offset:24576\n"
"ds_read_b64_tr_b16 a[158:159], v217 offset:28672\n"
"v_mfma_f32_16x16x32_bf16 v[0:3], a[128:131], a[96:99], v[0:3]\n"
"s_waitcnt lgkmcnt(12)\n"
"ds_read_b64_tr_b16 a[192:193], v216 offset:512\n"
"ds_read_b64_tr_b16 a[194:195], v216 offset:4608\n"
"v_mfma_f32_16x16x32_bf16 v[0:3], a[132:135], a[100:103], v[0:3]\n"
"s_waitcnt lgkmcnt(12)\n"
"ds_read_b64_tr_b16 a[196:197], v216 offset:8704\n"
"ds_read_b64_tr_b16 a[198:199], v216 offset:12800\n"
"v_mfma_f32_16x16x32_bf16 v[0:3], a[136:139], a[104:107], v[0:3]\n"
"s_waitcnt lgkmcnt(12)\n"
"ds_read_b64_tr_b16 a[200:201], v216 offset:16896\n"
"ds_read_b64_tr_b16 a[202:203], v216 offset:20992\n"
"v_mfma_f32_16x16x32_bf16 v[0:3], a[140:143], a[108:111], v[0:3]\n"
"s_waitcnt lgkmcnt(12)\n"
"ds_read_b64_tr_b16 a[204:205], v216 offset:25088\n"
"ds_read_b64_tr_b16 a[206:207], v216 offset:29184\n"
"v_mfma_f32_16x16x32_bf16 v[4:7], a[144:147], a[96:99], v[4:7]\n"
"s_waitcnt lgkmcnt(12)\n"
"ds_read_b64_tr_b16 a[208:209], v217 offset:512\n"
"ds_read_b64_tr_b16 a[210:211], v217 offset:4608\n"
"v_mfma_f32_16x16x32_bf16 v[4:7], a[148:151], a[100:103], v[4:7]\n"
"s_waitcnt lgkmcnt(12)\n"
"ds_read_b64_tr_b16 a[212:213], v217 offset:8704\n"
"ds_read_b64_tr_b16 a[214:215], v217 offset:12800\n"
"v_mfma_f32_16x16x32_bf16 v[4:7], a[152:155], a[104:107], v[4:7]\n"
"s_waitcnt lgkmcnt(12)\n"
"ds_read_b64_tr_b16 a[216:217], v217 offset:16896\n"
"ds_read_b64_tr_b16 a[218:219], v217 offset:20992\n"
"v_mfma_f32_16x16x32_bf16 v[4:7], a[156:159], a[108:111], v[4:7]\n"
"s_waitcnt lgkmcnt(12)\n"
"ds_read_b64_tr_b16 a[220:221], v217 offset:25088\n"
"ds_read_b64_tr_b16 a[222:223], v217 offset:29184\n"
"v_mfma_f32_16x16x32_bf16 v[8:11], a[192:195], a[96:99], v[8:11]\n"
"s_waitcnt lgkmcnt(12)\n"
"v_mfma_f32_16x16x32_bf16 v[8:11], a[196:199], a[100:103], v[8:11]\n"
"s_waitcnt lgkmcnt(10)\n"
"v_mfma_f32_16x16x32_bf16 v[8:11], a[200:203], a[104:107], v[8:11]\n"
"s_waitcnt lgkmcnt(8)\n"
"v_mfma_f32_16x16x32_bf16 v[8:11], a[204:207], a[108:111], v[8:11]\n"
"s_waitcnt lgkmcnt(6)\n"
"v_mfma_f32_16x16x32_bf16 v[12:15], a[208:211], a[96:99], v[12:15]\n"
"s_waitcnt lgkmcnt(4)\n"
"v_mfma_f32_16x16x32_bf16 v[12:15], a[212:215], a[100:103], v[12:15]\n"
"s_waitcnt lgkmcnt(2)\n"
"v_mfma_f32_16x16x32_bf16 v[12:15], a[216:219], a[104:107], v[12:15]\n"
"s_waitcnt lgkmcnt(0)\n"
"v_mfma_f32_16x16x32_bf16 v[12:15], a[220:223], a[108:111], v[12:15]\n"
"v_mov_b32 v210, s34\n"
"v_mov_b32 v211, s35\n"
"s_lshl_b32 s98, s11, 5\n"
"v_lshlrev_b32 v212, 7, v251\n"
"v_add_u32 v212, s98, v212\n"
"s_waitcnt vmcnt(0)\n"
"s_barrier\n"
"s_cmp_lg_u32 s38, 0\n"
"s_cbranch_scc1 SG4_%=\n"
"s_mov_b32 exec_hi, 0\n"
"s_mov_b32 exec_lo, 0xf\n"
"buffer_store_dwordx2 v[210:211], v212, s[40:43], s31 offen sc1\n"
"s_mov_b64 exec, -1\n"
"SG4_%=:\n"
"s_add_u32 s31, s31, 8192\n"
"ds_read_b64_tr_b16 v[64:65], v204 offset:32768\n"
"ds_read_b64_tr_b16 v[66:67], v204 offset:34816\n"
"ds_read_b64_tr_b16 v[68:69], v205 offset:32768\n"
"ds_read_b64_tr_b16 v[70:71], v205 offset:34816\n"
"ds_read_b64_tr_b16 v[72:73], v204 offset:33280\n"
"ds_read_b64_tr_b16 v[74:75], v204 offset:35328\n"
"ds_read_b64_tr_b16 v[76:77], v205 offset:33280\n"
"ds_read_b64_tr_b16 v[78:79], v205 offset:35328\n"
"ds_read_b64_tr_b16 v[80:81], v204 offset:36864\n"
"ds_read_b64_tr_b16 v[82:83], v204 offset:38912\n"
"ds_read_b64_tr_b16 v[84:85], v205 offset:36864\n"
"ds_read_b64_tr_b16 v[86:87], v205 offset:38912\n"
"ds_read_b64_tr_b16 v[88:89], v204 offset:37376\n"
"ds_read_b64_tr_b16 v[90:91], v204 offset:39424\n"
"ds_read_b64_tr_b16 v[92:93], v205 offset:37376\n"
"ds_read_b64_tr_b16 v[94:95], v205 offset:39424\n"
"s_waitcnt lgkmcnt(14)\n"
"v_mfma_f32_16x16x32_bf16 v[164:167], v[64:67], v[224:227], v[164:167]\n"
"s_waitcnt lgkmcnt(12)\n"
"ds_read_b64_tr_b16 v[96:97], v204 offset:40960\n"
"ds_read_b64_tr_b16 v[98:99], v204 offset:43008\n"
"v_mfma_f32_16x16x32_bf16 v[168:171], v[68:71], v[224:227], v[168:171]\n"
"s_waitcnt lgkmcnt(12)\n"
"ds_read_b64_tr_b16 v[100:101], v205 offset:40960\n"
"ds_read_b64_tr_b16 v[102:103], v205 offset:43008\n"
"v_mfma_f32_16x16x32_bf16 v[172:175], v[72:75], v[224:227], v[172:175]\n"
"s_waitcnt lgkmcnt(12)\n"
"ds_read_b64_tr_b16 v[104:105], v204 offset:41472\n"
"ds_read_b64_tr_b16 v[106:107], v204 offset:43520\n"
"v_mfma_f32_16x16x32_bf16 v[176:179], v[76:79], v[224:227], v[176:179]\n"
"s_waitcnt lgkmcnt(12)\n"
"ds_read_b64_tr_b16 v[108:109], v205 offset:41472\n"
"ds_read_b64_tr_b16 v[110:111], v205 offset:43520\n"
"v_mfma_f32_16x16x32_bf16 v[164:167], v[80:83], v[228:231], v[164:167]\n"
"s_waitcnt lgkmcnt(12)\n"
"ds_read_b64_tr_b16 v[64:65], v204 offset:45056\n"
"ds_read_b64_tr_b16 v[66:67], v204 offset:47104\n"
"v_mfma_f32_16x16x32_bf16 v[168:171], v[84:87], v[228:231], v[168:171]\n"
"s_waitcnt lgkmcnt(12)\n"
"ds_read_b64_tr_b16 v[68:69], v205 offset:45056\n"
"ds_read_b64_tr_b16 v[70:71], v205 offset:47104\n"
"v_mfma_f32_16x16x32_bf16 v[172:175], v[88:91], v[228:231], v[172:175]\n"
"s_waitcnt lgkmcnt(12)\n"
"ds_read_b64_tr_b16 v[72:73], v204 offset:45568\n"
"ds_read_b64_tr_b16 v[74:75], v204 offset:47616\n"
"v_mfma_f32_16x16x32_bf16 v[176:179], v[92:95], v[228:231], v[176:179]\n"
"s_waitcnt lgkmcnt(12)\n"
"ds_read_b64_tr_b16 v[76:77], v205 offset:45568\n"
"ds_read_b64_tr_b16 v[78:79], v205 offset:47616\n"
"v_mfma_f32_16x16x32_bf16 v[164:167], v[96:99], v[232:235], v[164:167]\n"
"s_waitcnt lgkmcnt(12)\n"
"ds_read_b64_tr_b16 v[80:81], v204 offset:49152\n"
"ds_read_b64_tr_b16 v[82:83], v204 offset:51200\n"
"v_mfma_f32_16x16x32_bf16 v[168:171], v[100:103], v[232:235], v[168:171]\n"
"s_waitcnt lgkmcnt(12)\n"
"ds_read_b64_tr_b16 v[84:85], v205 offset:49152\n"
"ds_read_b64_tr_b16 v[86:87], v205 offset:51200\n"
"v_mfma_f32_16x16x32_bf16 v[172:175], v[104:107], v[232:235], v[172:175]\n"
"s_waitcnt lgkmcnt(12)\n"
"ds_read_b64_tr_b16 v[88:89], v204 offset:49664\n"
"ds_read_b64_tr_b16 v[90:91], v204 offset:51712\n"
"v_mfma_f32_16x16x32_bf16 v[176:179], v[108:111], v[232:235], v[176:179]\n"
"s_waitcnt lgkmcnt(12)\n"
"ds_read_b64_tr_b16 v[92:93], v205 offset:49664\n"
"ds_read_b64_tr_b16 v[94:95], v205 offset:51712\n"
"v_mfma_f32_16x16x32_bf16 v[164:167], v[64:67], v[236:239], v[164:167]\n"
"s_waitcnt lgkmcnt(12)\n"
"ds_read_b64_tr_b16 v[96:97], v204 offset:53248\n"
"ds_read_b64_tr_b16 v[98:99], v204 offset:55296\n"
"v_mfma_f32_16x16x32_bf16 v[168:171], v[68:71], v[236:239], v[168:171]\n"
"s_waitcnt lgkmcnt(12)\n"
"ds_read_b64_tr_b16 v[100:101], v205 offset:53248\n"
"ds_read_b64_tr_b16 v[102:103], v205 offset:55296\n"
"v_mfma_f32_16x16x32_bf16 v[172:175], v[72:75], v[236:239], v[172:175]\n"
"s_waitcnt lgkmcnt(12)\n"
"ds_read_b64_tr_b16 v[104:105], v204 offset:53760\n"
"ds_read_b64_tr_b16 v[106:107], v204 offset:55808\n"
"v_mfma_f32_16x16x32_bf16 v[176:179], v[76:79], v[236:239], v[176:179]\n"
"s_waitcnt lgkmcnt(12)\n"
"ds_read_b64_tr_b16 v[108:109], v205 offset:53760\n"
"ds_read_b64_tr_b16 v[110:111], v205 offset:55808\n"
"v_mfma_f32_16x16x32_bf16 v[164:167], v[80:83], a[160:163], v[164:167]\n"
"s_waitcnt lgkmcnt(12)\n"
"ds_read_b64_tr_b16 v[64:65], v204 offset:57344\n"
"ds_read_b64_tr_b16 v[66:67], v204 offset:59392\n"
"v_mfma_f32_16x16x32_bf16 v[168:171], v[84:87], a[160:163], v[168:171]\n"
"s_waitcnt lgkmcnt(12)\n"
"ds_read_b64_tr_b16 v[68:69], v205 offset:57344\n"
"ds_read_b64_tr_b16 v[70:71], v205 offset:59392\n"
"v_mfma_f32_16x16x32_bf16 v[172:175], v[88:91], a[160:163], v[172:175]\n"
"s_waitcnt lgkmcnt(12)\n"
"ds_read_b64_tr_b16 v[72:73], v204 offset:57856\n"
"ds_read_b64_tr_b16 v[74:75], v204 offset:59904\n"
"v_mfma_f32_16x16x32_bf16 v[176:179], v[92:95], a[160:163], v[176:179]\n"
"s_waitcnt lgkmcnt(12)\n"
"ds_read_b64_tr_b16 v[76:77], v205 offset:57856\n"
"ds_read_b64_tr_b16 v[78:79], v205 offset:59904\n"
"v_mfma_f32_16x16x32_bf16 v[164:167], v[96:99], a[164:167], v[164:167]\n"
"s_waitcnt lgkmcnt(12)\n"
"ds_read_b64_tr_b16 v[80:81], v204 offset:61440\n"
"ds_read_b64_tr_b16 v[82:83], v204 offset:63488\n"
"v_mfma_f32_16x16x32_bf16 v[168:171], v[100:103], a[164:167], v[168:171]\n"
"s_waitcnt lgkmcnt(12)\n"
"ds_read_b64_tr_b16 v[84:85], v205 offset:61440\n"
"ds_read_b64_tr_b16 v[86:87], v205 offset:63488\n"
"v_mfma_f32_16x16x32_bf16 v[172:175], v[104:107], a[164:167], v[172:175]\n"
"s_waitcnt lgkmcnt(12)\n"
"ds_read_b64_tr_b16 v[88:89], v204 offset:61952\n"
"ds_read_b64_tr_b16 v[90:91], v204 offset:64000\n"
"v_mfma_f32_16x16x32_bf16 v[176:179], v[108:111], a[164:167], v[176:179]\n"
"s_waitcnt lgkmcnt(12)\n"
"ds_read_b64_tr_b16 v[92:93], v205 offset:61952\n"
"ds_read_b64_tr_b16 v[94:95], v205 offset:64000\n"
"v_mfma_f32_16x16x32_bf16 v[164:167], v[64:67], a[168:171], v[164:167]\n"
"s_waitcnt lgkmcnt(12)\n"
"ds_read_b64_tr_b16 v[96:97], v241 offset:0\n"
"ds_read_b64_tr_b16 v[98:99], v241 offset:2048\n"
"v_mfma_f32_16x16x32_bf16 v[168:171], v[68:71], a[168:171], v[168:171]\n"
"s_waitcnt lgkmcnt(12)\n"
"ds_read_b64_tr_b16 v[100:101], v242 offset:0\n"
"ds_read_b64_tr_b16 v[102:103], v242 offset:2048\n"
"v_mfma_f32_16x16x32_bf16 v[172:175], v[72:75], a[168:171], v[172:175]\n"
"s_waitcnt lgkmcnt(12)\n"
"ds_read_b64_tr_b16 v[104:105], v241 offset:512\n"
"ds_read_b64_tr_b16 v[106:107], v241 offset:2560\n"
"v_mfma_f32_16x16x32_bf16 v[176:179], v[76:79], a[168:171], v[176:179]\n"
"s_waitcnt lgkmcnt(12)\n"
"ds_read_b64_tr_b16 v[108:109], v242 offset:512\n"
"ds_read_b64_tr_b16 v[110:111], v242 offset:2560\n"
"v_mfma_f32_16x16x32_bf16 v[164:167], v[80:83], a[172:175], v[164:167]\n"
"s_waitcnt lgkmcnt(12)\n"
"ds_read_b64_tr_b16 v[64:65], v241 offset:4096\n"
"ds_read_b64_tr_b16 v[66:67], v241 offset:6144\n"
"v_mfma_f32_16x16x32_bf16 v[168:171], v[84:87], a[172:175], v[168:171]\n"
"s_waitcnt lgkmcnt(12)\n"
"ds_read_b64_tr_b16 v[68:69], v242 offset:4096\n"
"ds_read_b64_tr_b16 v[70:71], v242 offset:6144\n"
"v_mfma_f32_16x16x32_bf16 v[172:175], v[88:91], a[172:175], v[172:175]\n"
"s_waitcnt lgkmcnt(12)\n"
"ds_read_b64_tr_b16 v[72:73], v241 offset:4608\n"
"ds_read_b64_tr_b16 v[74:75], v241 offset:6656\n"
"v_mfma_f32_16x16x32_bf16 v[176:179], v[92:95], a[172:175], v[176:179]\n"
"s_waitcnt lgkmcnt(12)\n"
"ds_read_b64_tr_b16 v[76:77], v242 offset:4608\n"
"ds_read_b64_tr_b16 v[78:79], v242 offset:6656\n"
"v_mfma_f32_16x16x32_bf16 v[164:167], v[96:99], v[128:131], v[164:167]\n"
"s_waitcnt lgkmcnt(12)\n"
"ds_read_b64_tr_b16 v[80:81], v241 offset:8192\n"
"ds_read_b64_tr_b16 v[82:83], v241 offset:10240\n"
"v_mfma_f32_16x16x32_bf16 v[168:171], v[100:103], v[128:131], v[168:171]\n"
"s_waitcnt lgkmcnt(12)\n"
"ds_read_b64_tr_b16 v[84:85], v242 offset:8192\n"
"ds_read_b64_tr_b16 v[86:87], v242 offset:10240\n"
"v_mfma_f32_16x16x32_bf16 v[172:175], v[104:107], v[128:131], v[172:175]\n"
"s_waitcnt lgkmcnt(12)\n"
"ds_read_b64_tr_b16 v[88:89], v241 offset:8704\n"
"ds_read_b64_tr_b16 v[90:91], v241 offset:10752\n"
"v_mfma_f32_16x16x32_bf16 v[176:179], v[108:111], v[128:131], v[176:179]\n"
"s_waitcnt lgkmcnt(12)\n"
"ds_read_b64_tr_b16 v[92:93], v242 offset:8704\n"
"ds_read_b64_tr_b16 v[94:95], v242 offset:10752\n"
"v_mfma_f32_16x16x32_bf16 v[164:167], v[64:67], v[132:135], v[164:167]\n"
"s_waitcnt lgkmcnt(12)\n"
"ds_read_b64_tr_b16 v[96:97], v241 offset:12288\n"
"ds_read_b64_tr_b16 v[98:99], v241 offset:14336\n"
"v_mfma_f32_16x16x32_bf16 v[168:171], v[68:71], v[132:135], v[168:171]\n"
"s_waitcnt lgkmcnt(12)\n"
"ds_read_b64_tr_b16 v[100:101], v242 offset:12288\n"
"ds_read_b64_tr_b16 v[102:103], v242 offset:14336\n"
"v_mfma_f32_16x16x32_bf16 v[172:175], v[72:75], v[132:135], v[172:175]\n"
"s_waitcnt lgkmcnt(12)\n"
"ds_read_b64_tr_b16 v[104:105], v241 offset:12800\n"
"ds_read_b64_tr_b16 v[106:107], v241 offset:14848\n"
"v_mfma_f32_16x16x32_bf16 v[176:179], v[76:79], v[132:135], v[176:179]\n"
"s_waitcnt lgkmcnt(12)\n"
"ds_read_b64_tr_b16 v[108:109], v242 offset:12800\n"
"ds_read_b64_tr_b16 v[110:111], v242 offset:14848\n"
"v_mfma_f32_16x16x32_bf16 v[164:167], v[80:83], v[136:139], v[164:167]\n"
"s_waitcnt lgkmcnt(12)\n"
"ds_read_b64_tr_b16 v[64:65], v241 offset:16384\n"
"ds_read_b64_tr_b16 v[66:67], v241 offset:18432\n"
"v_mfma_f32_16x16x32_bf16 v[168:171], v[84:87], v[136:139], v[168:171]\n"
"s_waitcnt lgkmcnt(12)\n"
"ds_read_b64_tr_b16 v[68:69], v242 offset:16384\n"
"ds_read_b64_tr_b16 v[70:71], v242 offset:18432\n"
"v_mfma_f32_16x16x32_bf16 v[172:175], v[88:91], v[136:139], v[172:175]\n"
"s_waitcnt lgkmcnt(12)\n"
"ds_read_b64_tr_b16 v[72:73], v241 offset:16896\n"
"ds_read_b64_tr_b16 v[74:75], v241 offset:18944\n"
"v_mfma_f32_16x16x32_bf16 v[176:179], v[92:95], v[136:139], v[176:179]\n"
"s_waitcnt lgkmcnt(12)\n"
"ds_read_b64_tr_b16 v[76:77], v242 offset:16896\n"
"ds_read_b64_tr_b16 v[78:79], v242 offset:18944\n"
"v_mfma_f32_16x16x32_bf16 v[164:167], v[96:99], v[140:143], v[164:167]\n"
"s_waitcnt lgkmcnt(12)\n"
"ds_read_b64_tr_b16 v[80:81], v241 offset:20480\n"
"ds_read_b64_tr_b16 v[82:83], v241 offset:22528\n"
"v_mfma_f32_16x16x32_bf16 v[168:171], v[100:103], v[140:143], v[168:171]\n"
"s_waitcnt lgkmcnt(12)\n"
"ds_read_b64_tr_b16 v[84:85], v242 offset:20480\n"
"ds_read_b64_tr_b16 v[86:87], v242 offset:22528\n"
"v_mfma_f32_16x16x32_bf16 v[172:175], v[104:107], v[140:143], v[172:175]\n"
"s_waitcnt lgkmcnt(12)\n"
"ds_read_b64_tr_b16 v[88:89], v241 offset:20992\n"
"ds_read_b64_tr_b16 v[90:91], v241 offset:23040\n"
"v_mfma_f32_16x16x32_bf16 v[176:179], v[108:111], v[140:143], v[176:179]\n"
"s_waitcnt lgkmcnt(12)\n"
"ds_read_b64_tr_b16 v[92:93], v242 offset:20992\n"
"ds_read_b64_tr_b16 v[94:95], v242 offset:23040\n"
"v_mfma_f32_16x16x32_bf16 v[164:167], v[64:67], v[144:147], v[164:167]\n"
"s_waitcnt lgkmcnt(12)\n"
"ds_read_b64_tr_b16 v[96:97], v241 offset:24576\n"
"ds_read_b64_tr_b16 v[98:99], v241 offset:26624\n"
"v_mfma_f32_16x16x32_bf16 v[168:171], v[68:71], v[144:147], v[168:171]\n"
"s_waitcnt lgkmcnt(12)\n"
"ds_read_b64_tr_b16 v[100:101], v242 offset:24576\n"
"ds_read_b64_tr_b16 v[102:103], v242 offset:26624\n"
"v_mfma_f32_16x16x32_bf16 v[172:175], v[72:75], v[144:147], v[172:175]\n"
"s_waitcnt lgkmcnt(12)\n"
"ds_read_b64_tr_b16 v[104:105], v241 offset:25088\n"
"ds_read_b64_tr_b16 v[106:107], v241 offset:27136\n"
"v_mfma_f32_16x16x32_bf16 v[176:179], v[76:79], v[144:147], v[176:179]\n"
"s_waitcnt lgkmcnt(12)\n"
"ds_read_b64_tr_b16 v[108:109], v242 offset:25088\n"
"ds_read_b64_tr_b16 v[110:111], v242 offset:27136\n"
"v_mfma_f32_16x16x32_bf16 v[164:167], v[80:83], v[148:151], v[164:167]\n"
"s_waitcnt lgkmcnt(12)\n"
"ds_read_b64_tr_b16 v[64:65], v241 offset:28672\n"
"ds_read_b64_tr_b16 v[66:67], v241 offset:30720\n"
"v_mfma_f32_16x16x32_bf16 v[168:171], v[84:87], v[148:151], v[168:171]\n"
"s_waitcnt lgkmcnt(12)\n"
"ds_read_b64_tr_b16 v[68:69], v242 offset:28672\n"
"ds_read_b64_tr_b16 v[70:71], v242 offset:30720\n"
"v_mfma_f32_16x16x32_bf16 v[172:175], v[88:91], v[148:151], v[172:175]\n"
"s_waitcnt lgkmcnt(12)\n"
"ds_read_b64_tr_b16 v[72:73], v241 offset:29184\n"
"ds_read_b64_tr_b16 v[74:75], v241 offset:31232\n"
"v_mfma_f32_16x16x32_bf16 v[176:179], v[92:95], v[148:151], v[176:179]\n"
"s_waitcnt lgkmcnt(12)\n"
"ds_read_b64_tr_b16 v[76:77], v242 offset:29184\n"
"ds_read_b64_tr_b16 v[78:79], v242 offset:31232\n"
"v_mfma_f32_16x16x32_bf16 v[164:167], v[96:99], v[152:155], v[164:167]\n"
"s_waitcnt lgkmcnt(12)\n"
"ds_read_b64_tr_b16 v[80:81], v241 offset:32768\n"
"ds_read_b64_tr_b16 v[82:83], v241 offset:34816\n"
"v_mfma_f32_16x16x32_bf16 v[168:171], v[100:103], v[152:155], v[168:171]\n"
"s_waitcnt lgkmcnt(12)\n"
"ds_read_b64_tr_b16 v[84:85], v242 offset:32768\n"
"ds_read_b64_tr_b16 v[86:87], v242 offset:34816\n"
"v_mfma_f32_16x16x32_bf16 v[172:175], v[104:107], v[152:155], v[172:175]\n"
"s_waitcnt lgkmcnt(12)\n"
"ds_read_b64_tr_b16 v[88:89], v241 offset:33280\n"
"ds_read_b64_tr_b16 v[90:91], v241 offset:35328\n"
"v_mfma_f32_16x16x32_bf16 v[176:179], v[108:111], v[152:155], v[176:179]\n"
"s_waitcnt lgkmcnt(12)\n"
"ds_read_b64_tr_b16 v[92:93], v242 offset:33280\n"
"ds_read_b64_tr_b16 v[94:95], v242 offset:35328\n"
"v_mfma_f32_16x16x32_bf16 v[164:167], v[64:67], v[156:159], v[164:167]\n"
"s_waitcnt lgkmcnt(12)\n"
"ds_read_b64_tr_b16 v[96:97], v241 offset:36864\n"
"ds_read_b64_tr_b16 v[98:99], v241 offset:38912\n"
"v_mfma_f32_16x16x32_bf16 v[168:171], v[68:71], v[156:159], v[168:171]\n"
"s_waitcnt lgkmcnt(12)\n"
"ds_read_b64_tr_b16 v[100:101], v242 offset:36864\n"
"ds_read_b64_tr_b16 v[102:103], v242 offset:38912\n"
"v_mfma_f32_16x16x32_bf16 v[172:175], v[72:75], v[156:159], v[172:175]\n"
"s_waitcnt lgkmcnt(12)\n"
"ds_read_b64_tr_b16 v[104:105], v241 offset:37376\n"
"ds_read_b64_tr_b16 v[106:107], v241 offset:39424\n"
"v_mfma_f32_16x16x32_bf16 v[176:179], v[76:79], v[156:159], v[176:179]\n"
"s_waitcnt lgkmcnt(12)\n"
"ds_read_b64_tr_b16 v[108:109], v242 offset:37376\n"
"ds_read_b64_tr_b16 v[110:111], v242 offset:39424\n"
"v_mfma_f32_16x16x32_bf16 v[164:167], v[80:83], a[176:179], v[164:167]\n"
"s_waitcnt lgkmcnt(12)\n"
"ds_read_b64_tr_b16 v[64:65], v241 offset:40960\n"
"ds_read_b64_tr_b16 v[66:67], v241 offset:43008\n"
"v_mfma_f32_16x16x32_bf16 v[168:171], v[84:87], a[176:179], v[168:171]\n"
"s_waitcnt lgkmcnt(12)\n"
"ds_read_b64_tr_b16 v[68:69], v242 offset:40960\n"
"ds_read_b64_tr_b16 v[70:71], v242 offset:43008\n"
"v_mfma_f32_16x16x32_bf16 v[172:175], v[88:91], a[176:179], v[172:175]\n"
"s_waitcnt lgkmcnt(12)\n"
"ds_read_b64_tr_b16 v[72:73], v241 offset:41472\n"
"ds_read_b64_tr_b16 v[74:75], v241 offset:43520\n"
"v_mfma_f32_16x16x32_bf16 v[176:179], v[92:95], a[176:179], v[176:179]\n"
"s_waitcnt lgkmcnt(12)\n"
"ds_read_b64_tr_b16 v[76:77], v242 offset:41472\n"
"ds_read_b64_tr_b16 v[78:79], v242 offset:43520\n"
"v_mfma_f32_16x16x32_bf16 v[164:167], v[96:99], a[180:183], v[164:167]\n"
"s_waitcnt lgkmcnt(12)\n"
"buffer_load_dwordx2 v[214:215], v207, s[40:43], s30 offen sc1\n"
"v_mfma_f32_16x16x32_bf16 v[168:171], v[100:103], a[180:183], v[168:171]\n"
"s_waitcnt lgkmcnt(10)\n"
"v_mfma_f32_16x16x32_bf16 v[172:175], v[104:107], a[180:183], v[172:175]\n"
"s_waitcnt lgkmcnt(8)\n"
"v_mfma_f32_16x16x32_bf16 v[176:179], v[108:111], a[180:183], v[176:179]\n"
"s_waitcnt lgkmcnt(6)\n"
"v_mfma_f32_16x16x32_bf16 v[164:167], v[64:67], a[184:187], v[164:167]\n"
"s_waitcnt lgkmcnt(4)\n"
"v_mfma_f32_16x16x32_bf16 v[168:171], v[68:71], a[184:187], v[168:171]\n"
"s_waitcnt lgkmcnt(2)\n"
"v_mfma_f32_16x16x32_bf16 v[172:175], v[72:75], a[184:187], v[172:175]\n"
"s_waitcnt lgkmcnt(0)\n"
"v_mfma_f32_16x16x32_bf16 v[176:179], v[76:79], a[184:187], v[176:179]\n"
"v_lshlrev_b32 v64, 16, v180\n"
"v_and_b32 v65, 0xffff0000, v180\n"
"v_lshlrev_b32 v66, 16, v181\n"
"v_and_b32 v67, 0xffff0000, v181\n"
"v_lshlrev_b32 v68, 16, v182\n"
"v_and_b32 v69, 0xffff0000, v182\n"
"v_lshlrev_b32 v70, 16, v183\n"
"v_and_b32 v71, 0xffff0000, v183\n"
"v_lshlrev_b32 v72, 16, v184\n"
"v_and_b32 v73, 0xffff0000, v184\n"
"v_lshlrev_b32 v74, 16, v185\n"
"v_and_b32 v75, 0xffff0000, v185\n"
"v_lshlrev_b32 v76, 16, v186\n"
"v_and_b32 v77, 0xffff0000, v186\n"
"v_lshlrev_b32 v78, 16, v187\n"
"v_and_b32 v79, 0xffff0000, v187\n"
"v_mul_f32 v164, 0x4275fdf0, v164\n"
"v_mul_f32 v165, 0x4275fdf0, v165\n"
"v_mul_f32 v166, 0x4275fdf0, v166\n"
"v_mul_f32 v167, 0x4275fdf0, v167\n"
"v_mul_f32 v168, 0x4275fdf0, v168\n"
"v_mul_f32 v169, 0x4275fdf0, v169\n"
"v_mul_f32 v170, 0x4275fdf0, v170\n"
"v_mul_f32 v171, 0x4275fdf0, v171\n"
"v_mul_f32 v172, 0x4275fdf0, v172\n"
"v_mul_f32 v173, 0x4275fdf0, v173\n"
"v_mul_f32 v174, 0x4275fdf0, v174\n"
"v_mul_f32 v175, 0x4275fdf0, v175\n"
"v_mul_f32 v176, 0x4275fdf0, v176\n"
"v_mul_f32 v177, 0x4275fdf0, v177\n"
"v_mul_f32 v178, 0x4275fdf0, v178\n"
"v_mul_f32 v179, 0x4275fdf0, v179\n"
"v_cvt_pk_bf16_f32 v165, v164, v165\n"
"v_cvt_pk_bf16_f32 v167, v166, v167\n"
"v_cvt_pk_bf16_f32 v169, v168, v169\n"
"v_cvt_pk_bf16_f32 v171, v170, v171\n"
"v_cvt_pk_bf16_f32 v173, v172, v173\n"
"v_cvt_pk_bf16_f32 v175, v174, v175\n"
"v_cvt_pk_bf16_f32 v177, v176, v177\n"
"v_cvt_pk_bf16_f32 v179, v178, v179\n"
"v_lshlrev_b32 v164, 16, v165\n"
"v_lshlrev_b32 v166, 16, v167\n"
"v_lshlrev_b32 v168, 16, v169\n"
"v_lshlrev_b32 v170, 16, v171\n"
"v_lshlrev_b32 v172, 16, v173\n"
"v_lshlrev_b32 v174, 16, v175\n"
"v_lshlrev_b32 v176, 16, v177\n"
"v_lshlrev_b32 v178, 16, v179\n"
"v_and_b32 v165, 0xffff0000, v165\n"
"v_add_f32 v164, v164, v64\n"
"v_and_b32 v167, 0xffff0000, v167\n"
"v_add_f32 v166, v166, v66\n"
"v_and_b32 v169, 0xffff0000, v169\n"
"v_add_f32 v168, v168, v68\n"
"v_and_b32 v171, 0xffff0000, v171\n"
"v_add_f32 v170, v170, v70\n"
"v_and_b32 v173, 0xffff0000, v173\n"
"v_add_f32 v172, v172, v72\n"
"v_and_b32 v175, 0xffff0000, v175\n"
"v_add_f32 v174, v174, v74\n"
"v_and_b32 v177, 0xffff0000, v177\n"
"v_add_f32 v176, v176, v76\n"
"v_and_b32 v179, 0xffff0000, v179\n"
"v_add_f32 v178, v178, v78\n"
"v_add_f32 v165, v165, v65\n"
"v_add_f32 v167, v167, v67\n"
"v_add_f32 v169, v169, v69\n"
"v_add_f32 v171, v171, v71\n"
"v_add_f32 v173, v173, v73\n"
"v_add_f32 v175, v175, v75\n"
"v_add_f32 v177, v177, v77\n"
"v_add_f32 v179, v179, v79\n"
"v_cvt_pk_bf16_f32 v165, v164, v165\n"
"v_cvt_pk_bf16_f32 v167, v166, v167\n"
"v_cvt_pk_bf16_f32 v169, v168, v169\n"
"v_cvt_pk_bf16_f32 v171, v170, v171\n"
"v_cvt_pk_bf16_f32 v173, v172, v173\n"
"v_cvt_pk_bf16_f32 v175, v174, v175\n"
"v_cvt_pk_bf16_f32 v177, v176, v177\n"
"v_cvt_pk_bf16_f32 v179, v178, v179\n"
"v_lshlrev_b32 v164, 16, v165\n"
"v_lshlrev_b32 v166, 16, v167\n"
"v_lshlrev_b32 v168, 16, v169\n"
"v_lshlrev_b32 v170, 16, v171\n"
"v_lshlrev_b32 v172, 16, v173\n"
"v_lshlrev_b32 v174, 16, v175\n"
"v_lshlrev_b32 v176, 16, v177\n"
"v_lshlrev_b32 v178, 16, v179\n"
"v_and_b32 v165, 0xffff0000, v165\n"
"v_mul_f32 v80, 0xbfb8aa3b, v164\n"
"v_and_b32 v167, 0xffff0000, v167\n"
"v_mul_f32 v82, 0xbfb8aa3b, v166\n"
"v_and_b32 v169, 0xffff0000, v169\n"
"v_mul_f32 v84, 0xbfb8aa3b, v168\n"
"v_and_b32 v171, 0xffff0000, v171\n"
"v_mul_f32 v86, 0xbfb8aa3b, v170\n"
"v_and_b32 v173, 0xffff0000, v173\n"
"v_mul_f32 v88, 0xbfb8aa3b, v172\n"
"v_and_b32 v175, 0xffff0000, v175\n"
"v_mul_f32 v90, 0xbfb8aa3b, v174\n"
"v_and_b32 v177, 0xffff0000, v177\n"
"v_mul_f32 v92, 0xbfb8aa3b, v176\n"
"v_and_b32 v179, 0xffff0000, v179\n"
"v_mul_f32 v94, 0xbfb8aa3b, v178\n"
"v_exp_f32 v80, v80\n"
"v_mul_f32 v81, 0xbfb8aa3b, v165\n"
"v_exp_f32 v82, v82\n"
"v_mul_f32 v83, 0xbfb8aa3b, v167\n"
"v_exp_f32 v84, v84\n"
"v_mul_f32 v85, 0xbfb8aa3b, v169\n"
"v_exp_f32 v86, v86\n"
"v_mul_f32 v87, 0xbfb8aa3b, v171\n"
"v_exp_f32 v88, v88\n"
"v_mul_f32 v89, 0xbfb8aa3b, v173\n"
"v_exp_f32 v90, v90\n"
"v_mul_f32 v91, 0xbfb8aa3b, v175\n"
"v_exp_f32 v92, v92\n"
"v_mul_f32 v93, 0xbfb8aa3b, v177\n"
"v_exp_f32 v94, v94\n"
"v_mul_f32 v95, 0xbfb8aa3b, v179\n"
"s_waitcnt vmcnt(0)\n"
"v_xor_b32 v215, v214, v215\n"
"v_xor_b32 v214, 0x9e3779b9, v214\n"
"v_cmp_eq_u32 vcc, 0xe17d05ac, v215\n"
"v_subrev_u32 v214, s36, v214\n"
"v_cmp_gt_u32 s[98:99], 63, v214\n"
"s_nop 0\n"
"s_and_b64 vcc, vcc, s[98:99]\n"
"s_cmp_eq_u64 vcc, -1\n"
"s_cbranch_scc0 RS_%=\n"
"RSr_%=:\n"
"buffer_load_dwordx4 v[112:115], v251, s[40:43], s20 offen sc1\n"
"buffer_load_dwordx4 v[116:119], v251, s[40:43], s20 offen offset:1024 sc1\n"
"buffer_load_dwordx4 v[120:123], v251, s[40:43], s21 offen sc1\n"
"buffer_load_dwordx4 v[124:127], v251, s[40:43], s21 offen offset:1024 sc1\n"
"buffer_load_dwordx4 v[96:99], v251, s[40:43], s22 offen sc1\n"
"buffer_load_dwordx4 v[100:103], v251, s[40:43], s22 offen offset:1024 sc1\n"
"v_add_f32 v80, 1.0, v80\n"
"v_exp_f32 v81, v81\n"
"v_add_f32 v82, 1.0, v82\n"
"v_exp_f32 v83, v83\n"
"v_add_f32 v84, 1.0, v84\n"
"v_exp_f32 v85, v85\n"
"v_add_f32 v86, 1.0, v86\n"
"v_exp_f32 v87, v87\n"
"v_add_f32 v88, 1.0, v88\n"
"v_exp_f32 v89, v89\n"
"v_add_f32 v90, 1.0, v90\n"
"v_exp_f32 v91, v91\n"
"v_add_f32 v92, 1.0, v92\n"
"v_exp_f32 v93, v93\n"
"v_add_f32 v94, 1.0, v94\n"
"v_exp_f32 v95, v95\n"
"v_rcp_f32 v80, v80\n"
"v_add_f32 v81, 1.0, v81\n"
"v_rcp_f32 v82, v82\n"
"v_add_f32 v83, 1.0, v83\n"
"v_rcp_f32 v84, v84\n"
"v_add_f32 v85, 1.0, v85\n"
"v_rcp_f32 v86, v86\n"
"v_add_f32 v87, 1.0, v87\n"
"v_rcp_f32 v88, v88\n"
"v_add_f32 v89, 1.0, v89\n"
"v_rcp_f32 v90, v90\n"
"v_add_f32 v91, 1.0, v91\n"
"v_rcp_f32 v92, v92\n"
"v_add_f32 v93, 1.0, v93\n"
"v_rcp_f32 v94, v94\n"
"v_add_f32 v95, 1.0, v95\n"
"v_mul_f32 v164, v164, v80\n"
"v_rcp_f32 v81, v81\n"
"v_mul_f32 v166, v166, v82\n"
"v_rcp_f32 v83, v83\n"
"v_mul_f32 v168, v168, v84\n"
"v_rcp_f32 v85, v85\n"
"v_mul_f32 v170, v170, v86\n"
"v_rcp_f32 v87, v87\n"
"v_mul_f32 v172, v172, v88\n"
"v_rcp_f32 v89, v89\n"
"v_mul_f32 v174, v174, v90\n"
"v_rcp_f32 v91, v91\n"
"v_mul_f32 v176, v176, v92\n"
"v_rcp_f32 v93, v93\n"
"v_mul_f32 v178, v178, v94\n"
"v_rcp_f32 v95, v95\n"
"v_mul_f32 v165, v165, v81\n"
"v_mul_f32 v167, v167, v83\n"
"v_mul_f32 v169, v169, v85\n"
"v_mul_f32 v171, v171, v87\n"
"v_mul_f32 v173, v173, v89\n"
"v_mul_f32 v175, v175, v91\n"
"v_mul_f32 v177, v177, v93\n"
"v_mul_f32 v179, v179, v95\n"
"v_cvt_pk_bf16_f32 v165, v164, v165\n"
"v_cvt_pk_bf16_f32 v167, v166, v167\n"
"v_cvt_pk_bf16_f32 v169, v168, v169\n"
"v_cvt_pk_bf16_f32 v171, v170, v171\n"
"v_cvt_pk_bf16_f32 v173, v172, v173\n"
"v_cvt_pk_bf16_f32 v175, v174, v175\n"
"v_cvt_pk_bf16_f32 v177, v176, v177\n"
"v_cvt_pk_bf16_f32 v179, v178, v179\n"
"v_lshlrev_b32 v164, 16, v165\n"
"v_lshlrev_b32 v166, 16, v167\n"
"v_lshlrev_b32 v168, 16, v169\n"
"v_lshlrev_b32 v170, 16, v171\n"
"v_lshlrev_b32 v172, 16, v173\n"
"v_lshlrev_b32 v174, 16, v175\n"
"v_lshlrev_b32 v176, 16, v177\n"
"v_lshlrev_b32 v178, 16, v179\n"
"v_and_b32 v165, 0xffff0000, v165\n"
"v_and_b32 v167, 0xffff0000, v167\n"
"v_and_b32 v169, 0xffff0000, v169\n"
"v_and_b32 v171, 0xffff0000, v171\n"
"v_and_b32 v173, 0xffff0000, v173\n"
"v_and_b32 v175, 0xffff0000, v175\n"
"v_and_b32 v177, 0xffff0000, v177\n"
"v_and_b32 v179, 0xffff0000, v179\n"
"v_lshlrev_b32 v128, 16, v188\n"
"v_and_b32 v129, 0xffff0000, v188\n"
"v_lshlrev_b32 v130, 16, v189\n"
"v_and_b32 v131, 0xffff0000, v189\n"
"v_lshlrev_b32 v132, 16, v190\n"
"v_and_b32 v133, 0xffff0000, v190\n"
"v_lshlrev_b32 v134, 16, v191\n"
"v_and_b32 v135, 0xffff0000, v191\n"
"v_lshlrev_b32 v136, 16, v192\n"
"v_and_b32 v137, 0xffff0000, v192\n"
"v_lshlrev_b32 v138, 16, v193\n"
"v_and_b32 v139, 0xffff0000, v193\n"
"v_lshlrev_b32 v140, 16, v194\n"
"v_and_b32 v141, 0xffff0000, v194\n"
"v_lshlrev_b32 v142, 16, v195\n"
"v_and_b32 v143, 0xffff0000, v195\n"
"s_waitcnt vmcnt(5)\n"
"v_lshlrev_b32 v80, 16, v112\n"
"v_and_b32 v81, 0xffff0000, v112\n"
"v_lshlrev_b32 v82, 16, v113\n"
"v_and_b32 v83, 0xffff0000, v113\n"
"v_lshlrev_b32 v84, 16, v114\n"
"v_and_b32 v85, 0xffff0000, v114\n"
"v_lshlrev_b32 v86, 16, v115\n"
"v_and_b32 v87, 0xffff0000, v115\n"
"s_waitcnt vmcnt(4)\n"
"v_lshlrev_b32 v88, 16, v116\n"
"v_and_b32 v89, 0xffff0000, v116\n"
"v_lshlrev_b32 v90, 16, v117\n"
"v_and_b32 v91, 0xffff0000, v117\n"
"v_lshlrev_b32 v92, 16, v118\n"
"v_and_b32 v93, 0xffff0000, v118\n"
"v_lshlrev_b32 v94, 16, v119\n"
"v_and_b32 v95, 0xffff0000, v119\n"
"v_pk_add_f32 v[0:1], v[0:1], v[80:81]\n"
"v_pk_add_f32 v[2:3], v[2:3], v[82:83]\n"
"v_pk_add_f32 v[4:5], v[4:5], v[84:85]\n"
"v_pk_add_f32 v[6:7], v[6:7], v[86:87]\n"
"v_pk_add_f32 v[8:9], v[8:9], v[88:89]\n"
"v_pk_add_f32 v[10:11], v[10:11], v[90:91]\n"
"v_pk_add_f32 v[12:13], v[12:13], v[92:93]\n"
"v_pk_add_f32 v[14:15], v[14:15], v[94:95]\n"
"s_waitcnt vmcnt(3)\n"
"v_lshlrev_b32 v80, 16, v120\n"
"v_and_b32 v81, 0xffff0000, v120\n"
"v_lshlrev_b32 v82, 16, v121\n"
"v_and_b32 v83, 0xffff0000, v121\n"
"v_lshlrev_b32 v84, 16, v122\n"
"v_and_b32 v85, 0xffff0000, v122\n"
"v_lshlrev_b32 v86, 16, v123\n"
"v_and_b32 v87, 0xffff0000, v123\n"
"s_waitcnt vmcnt(2)\n"
"v_lshlrev_b32 v88, 16, v124\n"
"v_and_b32 v89, 0xffff0000, v124\n"
"v_lshlrev_b32 v90, 16, v125\n"
"v_and_b32 v91, 0xffff0000, v125\n"
"v_lshlrev_b32 v92, 16, v126\n"
"v_and_b32 v93, 0xffff0000, v126\n"
"v_lshlrev_b32 v94, 16, v127\n"
"v_and_b32 v95, 0xffff0000, v127\n"
"v_pk_add_f32 v[0:1], v[0:1], v[80:81]\n"
"v_pk_add_f32 v[2:3], v[2:3], v[82:83]\n"
"v_pk_add_f32 v[4:5], v[4:5], v[84:85]\n"
"v_pk_add_f32 v[6:7], v[6:7], v[86:87]\n"
"v_pk_add_f32 v[8:9], v[8:9], v[88:89]\n"
"v_pk_add_f32 v[10:11], v[10:11], v[90:91]\n"
"v_pk_add_f32 v[12:13], v[12:13], v[92:93]\n"
"v_pk_add_f32 v[14:15], v[14:15], v[94:95]\n"
"s_waitcnt vmcnt(1)\n"
"v_lshlrev_b32 v80, 16, v96\n"
"v_and_b32 v81, 0xffff0000, v96\n"
"v_lshlrev_b32 v82, 16, v97\n"
"v_and_b32 v83, 0xffff0000, v97\n"
"v_lshlrev_b32 v84, 16, v98\n"
"v_and_b32 v85, 0xffff0000, v98\n"
"v_lshlrev_b32 v86, 16, v99\n"
"v_and_b32 v87, 0xffff0000, v99\n"
"s_waitcnt vmcnt(0)\n"
"v_lshlrev_b32 v88, 16, v100\n"
"v_and_b32 v89, 0xffff0000, v100\n"
"v_lshlrev_b32 v90, 16, v101\n"
"v_and_b32 v91, 0xffff0000, v101\n"
"v_lshlrev_b32 v92, 16, v102\n"
"v_and_b32 v93, 0xffff0000, v102\n"
"v_lshlrev_b32 v94, 16, v103\n"
"v_and_b32 v95, 0xffff0000, v103\n"
"v_pk_add_f32 v[0:1], v[0:1], v[80:81]\n"
"v_pk_add_f32 v[2:3], v[2:3], v[82:83]\n"
"v_pk_add_f32 v[4:5], v[4:5], v[84:85]\n"
"v_pk_add_f32 v[6:7], v[6:7], v[86:87]\n"
"v_pk_add_f32 v[8:9], v[8:9], v[88:89]\n"
"v_pk_add_f32 v[10:11], v[10:11], v[90:91]\n"
"v_pk_add_f32 v[12:13], v[12:13], v[92:93]\n"
"v_pk_add_f32 v[14:15], v[14:15], v[94:95]\n"
"v_pk_fma_f32 v[0:1], v[0:1], v[164:165], v[128:129]\n"
"v_pk_fma_f32 v[2:3], v[2:3], v[166:167], v[130:131]\n"
"v_pk_fma_f32 v[4:5], v[4:5], v[168:169], v[132:133]\n"
"v_pk_fma_f32 v[6:7], v[6:7], v[170:171], v[134:135]\n"
"v_pk_fma_f32 v[8:9], v[8:9], v[172:173], v[136:137]\n"
"v_pk_fma_f32 v[10:11], v[10:11], v[174:175], v[138:139]\n"
"v_pk_fma_f32 v[12:13], v[12:13], v[176:177], v[140:141]\n"
"v_pk_fma_f32 v[14:15], v[14:15], v[178:179], v[142:143]\n"
"v_cvt_pk_bf16_f32 v32, v0, v1\n"
"v_cvt_pk_bf16_f32 v33, v2, v3\n"
"buffer_store_dwordx2 v[32:33], v206, s[72:75], s10 offen\n"
"v_cvt_pk_bf16_f32 v34, v4, v5\n"
"v_cvt_pk_bf16_f32 v35, v6, v7\n"
"buffer_store_dwordx2 v[34:35], v206, s[72:75], s10 offen offset:32\n"
"v_cvt_pk_bf16_f32 v36, v8, v9\n"
"v_cvt_pk_bf16_f32 v37, v10, v11\n"
"buffer_store_dwordx2 v[36:37], v206, s[72:75], s10 offen offset:64\n"
"v_cvt_pk_bf16_f32 v38, v12, v13\n"
"v_cvt_pk_bf16_f32 v39, v14, v15\n"
"buffer_store_dwordx2 v[38:39], v206, s[72:75], s10 offen offset:96\n"
"s_lshr_b32 s98, s30, 9\n"
"s_and_b32 s98, s98, 896\n"
"s_add_u32 s39, s98, 59768832\n"
"v_mov_b32 v210, 2\n"
"v_mov_b32 v211, 0\n"
"s_cmp_lg_u32 s38, 0\n"
"s_cbranch_scc1 TK3_%=\n"
"s_mov_b64 exec, 1\n"
"buffer_atomic_add v210, v211, s[40:43], s39 offen\n"
"s_mov_b64 exec, -1\n"
"TK3_%=:\n"
"s_branch END_%=\n"
"RH_%=:\n"
"s_waitcnt vmcnt(0)\n"
"s_mov_b32 s39, 0\n"
"SPh_%=:\n"
"buffer_load_dwordx2 v[210:211], v207, s[40:43], s30 offen sc1\n"
"s_waitcnt vmcnt(0)\n"
"v_xor_b32 v211, v210, v211\n"
"v_xor_b32 v210, 0x9e3779b9, v210\n"
"v_cmp_eq_u32 vcc, 0xe17d05ac, v211\n"
"v_subrev_u32 v210, s36, v210\n"
"v_cmp_gt_u32 s[98:99], 63, v210\n"
"s_nop 0\n"
"s_and_b64 vcc, vcc, s[98:99]\n"
"s_cmp_eq_u64 vcc, -1\n"
"s_cbranch_scc1 SDh_%=\n"
"s_sleep 1\n"
"s_add_u32 s39, s39, 1\n"
"s_cmp_lt_u32 s39, 0x20000\n"
"s_cbranch_scc1 SPh_%=\n"
"SDh_%=:\n"
"s_branch RHr_%=\n"
"RS_%=:\n"
"s_waitcnt vmcnt(0)\n"
"s_mov_b32 s39, 0\n"
"SPs_%=:\n"
"buffer_load_dwordx2 v[214:215], v207, s[40:43], s30 offen sc1\n"
"s_waitcnt vmcnt(0)\n"
"v_xor_b32 v215, v214, v215\n"
"v_xor_b32 v214, 0x9e3779b9, v214\n"
"v_cmp_eq_u32 vcc, 0xe17d05ac, v215\n"
"v_subrev_u32 v214, s36, v214\n"
"v_cmp_gt_u32 s[98:99], 63, v214\n"
"s_nop 0\n"
"s_and_b64 vcc, vcc, s[98:99]\n"
"s_cmp_eq_u64 vcc, -1\n"
"s_cbranch_scc1 SDs_%=\n"
"s_sleep 1\n"
"s_add_u32 s39, s39, 1\n"
"s_cmp_lt_u32 s39, 0x20000\n"
"s_cbranch_scc1 SPs_%=\n"
"SDs_%=:\n"
"s_branch RSr_%=\n"
"RP_%=:\n"
"s_waitcnt vmcnt(0)\n"
"s_mov_b32 s39, 0\n"
"SPo_%=:\n"
"buffer_load_dwordx2 v[214:215], v207, s[40:43], s30 offen sc1\n"
"s_waitcnt vmcnt(0)\n"
"v_xor_b32 v215, v214, v215\n"
"v_xor_b32 v214, 0x9e3779b9, v214\n"
"v_cmp_eq_u32 vcc, 0xe17d05ac, v215\n"
"v_subrev_u32 v214, s36, v214\n"
"v_cmp_gt_u32 s[98:99], 63, v214\n"
"s_nop 0\n"
"s_and_b64 vcc, vcc, s[98:99]\n"
"s_cmp_eq_u64 vcc, -1\n"
"s_cbranch_scc1 SDo_%=\n"
"s_sleep 1\n"
"s_add_u32 s39, s39, 1\n"
"s_cmp_lt_u32 s39, 0x20000\n"
"s_cbranch_scc1 SPo_%=\n"
"SDo_%=:\n"
"s_branch RPr_%=\n"
"RO_%=:\n"
"s_waitcnt vmcnt(0)\n"
"s_mov_b32 s39, 0\n"
"SPp_%=:\n"
"buffer_load_dwordx2 v[214:215], v207, s[40:43], s30 offen sc1\n"
"s_waitcnt vmcnt(0)\n"
"v_xor_b32 v215, v214, v215\n"
"v_xor_b32 v214, 0x9e3779b9, v214\n"
"v_cmp_eq_u32 vcc, 0xe17d05ac, v215\n"
"v_subrev_u32 v214, s36, v214\n"
"v_cmp_gt_u32 s[98:99], 63, v214\n"
"s_nop 0\n"
"s_and_b64 vcc, vcc, s[98:99]\n"
"s_cmp_eq_u64 vcc, -1\n"
"s_cbranch_scc1 SDp_%=\n"
"s_sleep 1\n"
"s_add_u32 s39, s39, 1\n"
"s_cmp_lt_u32 s39, 0x20000\n"
"s_cbranch_scc1 SPp_%=\n"
"SDp_%=:\n"
"s_branch ROr_%=\n"
"RA0_%=:\n"
"s_mov_b32 s97, 0\n"
"s_branch RAX0_%=\n"
"RA1_%=:\n"
"s_mov_b32 s97, 1\n"
"s_branch RAX1_%=\n"
"RB1_%=:\n"
"s_mov_b32 s97, 1\n"
"s_branch RBX0_%=\n"
"RA2_%=:\n"
"s_mov_b32 s97, 2\n"
"s_branch RAX0_%=\n"
"RB2_%=:\n"
"s_mov_b32 s97, 2\n"
"s_branch RBX0_%=\n"
"RA3_%=:\n"
"s_mov_b32 s97, 3\n"
"s_branch RAX1_%=\n"
"RB3_%=:\n"
"s_mov_b32 s97, 3\n"
"s_branch RBX0_%=\n"
"RA4_%=:\n"
"s_mov_b32 s97, 4\n"
"s_branch RAX0_%=\n"
"RB4_%=:\n"
"s_mov_b32 s97, 4\n"
"s_branch RBX0_%=\n"
"RA5_%=:\n"
"s_mov_b32 s97, 5\n"
"s_branch RAX1_%=\n"
"RB5_%=:\n"
"s_mov_b32 s97, 5\n"
"s_branch RBX0_%=\n"
"RA6_%=:\n"
"s_mov_b32 s97, 6\n"
"s_branch RAX0_%=\n"
"RB6_%=:\n"
"s_mov_b32 s97, 6\n"
"s_branch RBX0_%=\n"
"RA7_%=:\n"
"s_mov_b32 s97, 7\n"
"s_branch RAX1_%=\n"
"RB7_%=:\n"
"s_mov_b32 s97, 7\n"
"s_branch RBX0_%=\n"
"RAX0_%=:\n"
"s_nop 7\n"
"s_nop 7\n"
"v_mov_b32 v198, v252\n"
"s_nop 1\n"
"v_permlane32_swap_b32 v198, v252\n"
"v_max_f32 v252, v252, v198\n"
"v_mov_b32 v198, v252\n"
"s_nop 1\n"
"v_permlane16_swap_b32 v198, v252\n"
"v_max_f32 v252, v252, v198\n"
"v_mov_b32 v198, v253\n"
"s_nop 1\n"
"v_permlane32_swap_b32 v198, v253\n"
"v_max_f32 v253, v253, v198\n"
"v_mov_b32 v198, v253\n"
"s_nop 1\n"
"v_permlane16_swap_b32 v198, v253\n"
"v_max_f32 v253, v253, v198\n"
"v_mov_b32 v198, v254\n"
"s_nop 1\n"
"v_permlane32_swap_b32 v198, v254\n"
"v_max_f32 v254, v254, v198\n"
"v_mov_b32 v198, v254\n"
"s_nop 1\n"
"v_permlane16_swap_b32 v198, v254\n"
"v_max_f32 v254, v254, v198\n"
"v_mov_b32 v198, v255\n"
"s_nop 1\n"
"v_permlane32_swap_b32 v198, v255\n"
"v_max_f32 v255, v255, v198\n"
"v_mov_b32 v198, v255\n"
"s_nop 1\n"
"v_permlane16_swap_b32 v198, v255\n"
"v_max_f32 v255, v255, v198\n"
"s_cmp_eq_u32 s97, 0\n"
"s_cbranch_scc1 RAY0_%=\n"
"v_max_f32 v252, 0, v252\n"
"v_max_f32 v253, 0, v253\n"
"v_max_f32 v254, 0, v254\n"
"v_max_f32 v255, 0, v255\n"
"s_mov_b32 s95, 1\n"
"RAY0_%=:\n"
"v_exp_f32 v192, -v252\n"
"v_sub_f32 v224, v224, v252\n"
"v_sub_f32 v225, v225, v252\n"
"v_sub_f32 v226, v226, v252\n"
"v_sub_f32 v227, v227, v252\n"
"v_sub_f32 v0, v0, v252\n"
"v_sub_f32 v1, v1, v252\n"
"v_sub_f32 v2, v2, v252\n"
"v_sub_f32 v3, v3, v252\n"
"v_sub_f32 v4, v4, v252\n"
"v_sub_f32 v5, v5, v252\n"
"v_sub_f32 v6, v6, v252\n"
"v_sub_f32 v7, v7, v252\n"
"v_sub_f32 v32, v32, v252\n"
"v_sub_f32 v33, v33, v252\n"
"v_sub_f32 v34, v34, v252\n"
"v_sub_f32 v35, v35, v252\n"
"v_sub_f32 v36, v36, v252\n"
"v_sub_f32 v37, v37, v252\n"
"v_sub_f32 v38, v38, v252\n"
"v_sub_f32 v39, v39, v252\n"
"v_exp_f32 v193, -v253\n"
"v_sub_f32 v228, v228, v253\n"
"v_sub_f32 v229, v229, v253\n"
"v_sub_f32 v230, v230, v253\n"
"v_sub_f32 v231, v231, v253\n"
"v_sub_f32 v8, v8, v253\n"
"v_sub_f32 v9, v9, v253\n"
"v_sub_f32 v10, v10, v253\n"
"v_sub_f32 v11, v11, v253\n"
"v_sub_f32 v12, v12, v253\n"
"v_sub_f32 v13, v13, v253\n"
"v_sub_f32 v14, v14, v253\n"
"v_sub_f32 v15, v15, v253\n"
"v_sub_f32 v40, v40, v253\n"
"v_sub_f32 v41, v41, v253\n"
"v_sub_f32 v42, v42, v253\n"
"v_sub_f32 v43, v43, v253\n"
"v_sub_f32 v44, v44, v253\n"
"v_sub_f32 v45, v45, v253\n"
"v_sub_f32 v46, v46, v253\n"
"v_sub_f32 v47, v47, v253\n"
"v_exp_f32 v194, -v254\n"
"v_sub_f32 v232, v232, v254\n"
"v_sub_f32 v233, v233, v254\n"
"v_sub_f32 v234, v234, v254\n"
"v_sub_f32 v235, v235, v254\n"
"v_sub_f32 v16, v16, v254\n"
"v_sub_f32 v17, v17, v254\n"
"v_sub_f32 v18, v18, v254\n"
"v_sub_f32 v19, v19, v254\n"
"v_sub_f32 v20, v20, v254\n"
"v_sub_f32 v21, v21, v254\n"
"v_sub_f32 v22, v22, v254\n"
"v_sub_f32 v23, v23, v254\n"
"v_sub_f32 v48, v48, v254\n"
"v_sub_f32 v49, v49, v254\n"
"v_sub_f32 v50, v50, v254\n"
"v_sub_f32 v51, v51, v254\n"
"v_sub_f32 v52, v52, v254\n"
"v_sub_f32 v53, v53, v254\n"
"v_sub_f32 v54, v54, v254\n"
"v_sub_f32 v55, v55, v254\n"
"v_exp_f32 v195, -v255\n"
"v_sub_f32 v236, v236, v255\n"
"v_sub_f32 v237, v237, v255\n"
"v_sub_f32 v238, v238, v255\n"
"v_sub_f32 v239, v239, v255\n"
"v_sub_f32 v24, v24, v255\n"
"v_sub_f32 v25, v25, v255\n"
"v_sub_f32 v26, v26, v255\n"
"v_sub_f32 v27, v27, v255\n"
"v_sub_f32 v28, v28, v255\n"
"v_sub_f32 v29, v29, v255\n"
"v_sub_f32 v30, v30, v255\n"
"v_sub_f32 v31, v31, v255\n"
"v_sub_f32 v56, v56, v255\n"
"v_sub_f32 v57, v57, v255\n"
"v_sub_f32 v58, v58, v255\n"
"v_sub_f32 v59, v59, v255\n"
"v_sub_f32 v60, v60, v255\n"
"v_sub_f32 v61, v61, v255\n"
"v_sub_f32 v62, v62, v255\n"
"v_sub_f32 v63, v63, v255\n"
"s_nop 1\n"
"s_cmp_eq_u32 s97, 0\n"
"s_cbranch_scc1 RAr0_%=\n"
"s_cmp_eq_u32 s97, 2\n"
"s_cbranch_scc1 RAr2_%=\n"
"s_cmp_eq_u32 s97, 4\n"
"s_cbranch_scc1 RAr4_%=\n"
"s_cmp_eq_u32 s97, 6\n"
"s_cbranch_scc1 RAr6_%=\n"
"RAX1_%=:\n"
"s_nop 7\n"
"s_nop 7\n"
"v_mov_b32 v198, v252\n"
"s_nop 1\n"
"v_permlane32_swap_b32 v198, v252\n"
"v_max_f32 v252, v252, v198\n"
"v_mov_b32 v198, v252\n"
"s_nop 1\n"
"v_permlane16_swap_b32 v198, v252\n"
"v_max_f32 v252, v252, v198\n"
"v_mov_b32 v198, v253\n"
"s_nop 1\n"
"v_permlane32_swap_b32 v198, v253\n"
"v_max_f32 v253, v253, v198\n"
"v_mov_b32 v198, v253\n"
"s_nop 1\n"
"v_permlane16_swap_b32 v198, v253\n"
"v_max_f32 v253, v253, v198\n"
"v_mov_b32 v198, v254\n"
"s_nop 1\n"
"v_permlane32_swap_b32 v198, v254\n"
"v_max_f32 v254, v254, v198\n"
"v_mov_b32 v198, v254\n"
"s_nop 1\n"
"v_permlane16_swap_b32 v198, v254\n"
"v_max_f32 v254, v254, v198\n"
"v_mov_b32 v198, v255\n"
"s_nop 1\n"
"v_permlane32_swap_b32 v198, v255\n"
"v_max_f32 v255, v255, v198\n"
"v_mov_b32 v198, v255\n"
"s_nop 1\n"
"v_permlane16_swap_b32 v198, v255\n"
"v_max_f32 v255, v255, v198\n"
"s_cmp_eq_u32 s97, 0\n"
"s_cbranch_scc1 RAY1_%=\n"
"v_max_f32 v252, 0, v252\n"
"v_max_f32 v253, 0, v253\n"
"v_max_f32 v254, 0, v254\n"
"v_max_f32 v255, 0, v255\n"
"s_mov_b32 s95, 1\n"
"RAY1_%=:\n"
"v_exp_f32 v192, -v252\n"
"v_sub_f32 v224, v224, v252\n"
"v_sub_f32 v225, v225, v252\n"
"v_sub_f32 v226, v226, v252\n"
"v_sub_f32 v227, v227, v252\n"
"v_sub_f32 v64, v64, v252\n"
"v_sub_f32 v65, v65, v252\n"
"v_sub_f32 v66, v66, v252\n"
"v_sub_f32 v67, v67, v252\n"
"v_sub_f32 v68, v68, v252\n"
"v_sub_f32 v69, v69, v252\n"
"v_sub_f32 v70, v70, v252\n"
"v_sub_f32 v71, v71, v252\n"
"v_sub_f32 v96, v96, v252\n"
"v_sub_f32 v97, v97, v252\n"
"v_sub_f32 v98, v98, v252\n"
"v_sub_f32 v99, v99, v252\n"
"v_sub_f32 v100, v100, v252\n"
"v_sub_f32 v101, v101, v252\n"
"v_sub_f32 v102, v102, v252\n"
"v_sub_f32 v103, v103, v252\n"
"v_exp_f32 v193, -v253\n"
"v_sub_f32 v228, v228, v253\n"
"v_sub_f32 v229, v229, v253\n"
"v_sub_f32 v230, v230, v253\n"
"v_sub_f32 v231, v231, v253\n"
"v_sub_f32 v72, v72, v253\n"
"v_sub_f32 v73, v73, v253\n"
"v_sub_f32 v74, v74, v253\n"
"v_sub_f32 v75, v75, v253\n"
"v_sub_f32 v76, v76, v253\n"
"v_sub_f32 v77, v77, v253\n"
"v_sub_f32 v78, v78, v253\n"
"v_sub_f32 v79, v79, v253\n"
"v_sub_f32 v104, v104, v253\n"
"v_sub_f32 v105, v105, v253\n"
"v_sub_f32 v106, v106, v253\n"
"v_sub_f32 v107, v107, v253\n"
"v_sub_f32 v108, v108, v253\n"
"v_sub_f32 v109, v109, v253\n"
"v_sub_f32 v110, v110, v253\n"
"v_sub_f32 v111, v111, v253\n"
"v_exp_f32 v194, -v254\n"
"v_sub_f32 v232, v232, v254\n"
"v_sub_f32 v233, v233, v254\n"
"v_sub_f32 v234, v234, v254\n"
"v_sub_f32 v235, v235, v254\n"
"v_sub_f32 v80, v80, v254\n"
"v_sub_f32 v81, v81, v254\n"
"v_sub_f32 v82, v82, v254\n"
"v_sub_f32 v83, v83, v254\n"
"v_sub_f32 v84, v84, v254\n"
"v_sub_f32 v85, v85, v254\n"
"v_sub_f32 v86, v86, v254\n"
"v_sub_f32 v87, v87, v254\n"
"v_sub_f32 v112, v112, v254\n"
"v_sub_f32 v113, v113, v254\n"
"v_sub_f32 v114, v114, v254\n"
"v_sub_f32 v115, v115, v254\n"
"v_sub_f32 v116, v116, v254\n"
"v_sub_f32 v117, v117, v254\n"
"v_sub_f32 v118, v118, v254\n"
"v_sub_f32 v119, v119, v254\n"
"v_exp_f32 v195, -v255\n"
"v_sub_f32 v236, v236, v255\n"
"v_sub_f32 v237, v237, v255\n"
"v_sub_f32 v238, v238, v255\n"
"v_sub_f32 v239, v239, v255\n"
"v_sub_f32 v88, v88, v255\n"
"v_sub_f32 v89, v89, v255\n"
"v_sub_f32 v90, v90, v255\n"
"v_sub_f32 v91, v91, v255\n"
"v_sub_f32 v92, v92, v255\n"
"v_sub_f32 v93, v93, v255\n"
"v_sub_f32 v94, v94, v255\n"
"v_sub_f32 v95, v95, v255\n"
"v_sub_f32 v120, v120, v255\n"
"v_sub_f32 v121, v121, v255\n"
"v_sub_f32 v122, v122, v255\n"
"v_sub_f32 v123, v123, v255\n"
"v_sub_f32 v124, v124, v255\n"
"v_sub_f32 v125, v125, v255\n"
"v_sub_f32 v126, v126, v255\n"
"v_sub_f32 v127, v127, v255\n"
"s_nop 1\n"
"s_cmp_eq_u32 s97, 1\n"
"s_cbranch_scc1 RAr1_%=\n"
"s_cmp_eq_u32 s97, 3\n"
"s_cbranch_scc1 RAr3_%=\n"
"s_cmp_eq_u32 s97, 5\n"
"s_cbranch_scc1 RAr5_%=\n"
"s_cmp_eq_u32 s97, 7\n"
"s_cbranch_scc1 RAr7_%=\n"
"RBX0_%=:\n"
"s_nop 7\n"
"s_nop 7\n"
"v_accvgpr_read_b32 v198, a128\n"
"v_mul_f32 v198, v198, v192\n"
"v_accvgpr_write_b32 a128, v198\n"
"v_accvgpr_read_b32 v198, a129\n"
"v_mul_f32 v198, v198, v192\n"
"v_accvgpr_write_b32 a129, v198\n"
"v_accvgpr_read_b32 v198, a130\n"
"v_mul_f32 v198, v198, v192\n"
"v_accvgpr_write_b32 a130, v198\n"
"v_accvgpr_read_b32 v198, a131\n"
"v_mul_f32 v198, v198, v192\n"
"v_accvgpr_write_b32 a131, v198\n"
"v_accvgpr_read_b32 v198, a0\n"
"v_mul_f32 v198, v198, v192\n"
"v_accvgpr_write_b32 a0, v198\n"
"v_accvgpr_read_b32 v198, a1\n"
"v_mul_f32 v198, v198, v192\n"
"v_accvgpr_write_b32 a1, v198\n"
"v_accvgpr_read_b32 v198, a2\n"
"v_mul_f32 v198, v198, v192\n"
"v_accvgpr_write_b32 a2, v198\n"
"v_accvgpr_read_b32 v198, a3\n"
"v_mul_f32 v198, v198, v192\n"
"v_accvgpr_write_b32 a3, v198\n"
"v_accvgpr_read_b32 v198, a16\n"
"v_mul_f32 v198, v198, v192\n"
"v_accvgpr_write_b32 a16, v198\n"
"v_accvgpr_read_b32 v198, a17\n"
"v_mul_f32 v198, v198, v192\n"
"v_accvgpr_write_b32 a17, v198\n"
"v_accvgpr_read_b32 v198, a18\n"
"v_mul_f32 v198, v198, v192\n"
"v_accvgpr_write_b32 a18, v198\n"
"v_accvgpr_read_b32 v198, a19\n"
"v_mul_f32 v198, v198, v192\n"
"v_accvgpr_write_b32 a19, v198\n"
"v_accvgpr_read_b32 v198, a32\n"
"v_mul_f32 v198, v198, v192\n"
"v_accvgpr_write_b32 a32, v198\n"
"v_accvgpr_read_b32 v198, a33\n"
"v_mul_f32 v198, v198, v192\n"
"v_accvgpr_write_b32 a33, v198\n"
"v_accvgpr_read_b32 v198, a34\n"
"v_mul_f32 v198, v198, v192\n"
"v_accvgpr_write_b32 a34, v198\n"
"v_accvgpr_read_b32 v198, a35\n"
"v_mul_f32 v198, v198, v192\n"
"v_accvgpr_write_b32 a35, v198\n"
"v_accvgpr_read_b32 v198, a48\n"
"v_mul_f32 v198, v198, v192\n"
"v_accvgpr_write_b32 a48, v198\n"
"v_accvgpr_read_b32 v198, a49\n"
"v_mul_f32 v198, v198, v192\n"
"v_accvgpr_write_b32 a49, v198\n"
"v_accvgpr_read_b32 v198, a50\n"
"v_mul_f32 v198, v198, v192\n"
"v_accvgpr_write_b32 a50, v198\n"
"v_accvgpr_read_b32 v198, a51\n"
"v_mul_f32 v198, v198, v192\n"
"v_accvgpr_write_b32 a51, v198\n"
"v_accvgpr_read_b32 v198, a64\n"
"v_mul_f32 v198, v198, v192\n"
"v_accvgpr_write_b32 a64, v198\n"
"v_accvgpr_read_b32 v198, a65\n"
"v_mul_f32 v198, v198, v192\n"
"v_accvgpr_write_b32 a65, v198\n"
"v_accvgpr_read_b32 v198, a66\n"
"v_mul_f32 v198, v198, v192\n"
"v_accvgpr_write_b32 a66, v198\n"
"v_accvgpr_read_b32 v198, a67\n"
"v_mul_f32 v198, v198, v192\n"
"v_accvgpr_write_b32 a67, v198\n"
"v_accvgpr_read_b32 v198, a80\n"
"v_mul_f32 v198, v198, v192\n"
"v_accvgpr_write_b32 a80, v198\n"
"v_accvgpr_read_b32 v198, a81\n"
"v_mul_f32 v198, v198, v192\n"
"v_accvgpr_write_b32 a81, v198\n"
"v_accvgpr_read_b32 v198, a82\n"
"v_mul_f32 v198, v198, v192\n"
"v_accvgpr_write_b32 a82, v198\n"
"v_accvgpr_read_b32 v198, a83\n"
"v_mul_f32 v198, v198, v192\n"
"v_accvgpr_write_b32 a83, v198\n"
"v_accvgpr_read_b32 v198, a96\n"
"v_mul_f32 v198, v198, v192\n"
"v_accvgpr_write_b32 a96, v198\n"
"v_accvgpr_read_b32 v198, a97\n"
"v_mul_f32 v198, v198, v192\n"
"v_accvgpr_write_b32 a97, v198\n"
"v_accvgpr_read_b32 v198, a98\n"
"v_mul_f32 v198, v198, v192\n"
"v_accvgpr_write_b32 a98, v198\n"
"v_accvgpr_read_b32 v198, a99\n"
"v_mul_f32 v198, v198, v192\n"
"v_accvgpr_write_b32 a99, v198\n"
"v_accvgpr_read_b32 v198, a112\n"
"v_mul_f32 v198, v198, v192\n"
"v_accvgpr_write_b32 a112, v198\n"
"v_accvgpr_read_b32 v198, a113\n"
"v_mul_f32 v198, v198, v192\n"
"v_accvgpr_write_b32 a113, v198\n"
"v_accvgpr_read_b32 v198, a114\n"
"v_mul_f32 v198, v198, v192\n"
"v_accvgpr_write_b32 a114, v198\n"
"v_accvgpr_read_b32 v198, a115\n"
"v_mul_f32 v198, v198, v192\n"
"v_accvgpr_write_b32 a115, v198\n"
"v_accvgpr_read_b32 v198, a132\n"
"v_mul_f32 v198, v198, v193\n"
"v_accvgpr_write_b32 a132, v198\n"
"v_accvgpr_read_b32 v198, a133\n"
"v_mul_f32 v198, v198, v193\n"
"v_accvgpr_write_b32 a133, v198\n"
"v_accvgpr_read_b32 v198, a134\n"
"v_mul_f32 v198, v198, v193\n"
"v_accvgpr_write_b32 a134, v198\n"
"v_accvgpr_read_b32 v198, a135\n"
"v_mul_f32 v198, v198, v193\n"
"v_accvgpr_write_b32 a135, v198\n"
"v_accvgpr_read_b32 v198, a4\n"
"v_mul_f32 v198, v198, v193\n"
"v_accvgpr_write_b32 a4, v198\n"
"v_accvgpr_read_b32 v198, a5\n"
"v_mul_f32 v198, v198, v193\n"
"v_accvgpr_write_b32 a5, v198\n"
"v_accvgpr_read_b32 v198, a6\n"
"v_mul_f32 v198, v198, v193\n"
"v_accvgpr_write_b32 a6, v198\n"
"v_accvgpr_read_b32 v198, a7\n"
"v_mul_f32 v198, v198, v193\n"
"v_accvgpr_write_b32 a7, v198\n"
"v_accvgpr_read_b32 v198, a20\n"
"v_mul_f32 v198, v198, v193\n"
"v_accvgpr_write_b32 a20, v198\n"
"v_accvgpr_read_b32 v198, a21\n"
"v_mul_f32 v198, v198, v193\n"
"v_accvgpr_write_b32 a21, v198\n"
"v_accvgpr_read_b32 v198, a22\n"
"v_mul_f32 v198, v198, v193\n"
"v_accvgpr_write_b32 a22, v198\n"
"v_accvgpr_read_b32 v198, a23\n"
"v_mul_f32 v198, v198, v193\n"
"v_accvgpr_write_b32 a23, v198\n"
"v_accvgpr_read_b32 v198, a36\n"
"v_mul_f32 v198, v198, v193\n"
"v_accvgpr_write_b32 a36, v198\n"
"v_accvgpr_read_b32 v198, a37\n"
"v_mul_f32 v198, v198, v193\n"
"v_accvgpr_write_b32 a37, v198\n"
"v_accvgpr_read_b32 v198, a38\n"
"v_mul_f32 v198, v198, v193\n"
"v_accvgpr_write_b32 a38, v198\n"
"v_accvgpr_read_b32 v198, a39\n"
"v_mul_f32 v198, v198, v193\n"
"v_accvgpr_write_b32 a39, v198\n"
"v_accvgpr_read_b32 v198, a52\n"
"v_mul_f32 v198, v198, v193\n"
"v_accvgpr_write_b32 a52, v198\n"
"v_accvgpr_read_b32 v198, a53\n"
"v_mul_f32 v198, v198, v193\n"
"v_accvgpr_write_b32 a53, v198\n"
"v_accvgpr_read_b32 v198, a54\n"
"v_mul_f32 v198, v198, v193\n"
"v_accvgpr_write_b32 a54, v198\n"
"v_accvgpr_read_b32 v198, a55\n"
"v_mul_f32 v198, v198, v193\n"
"v_accvgpr_write_b32 a55, v198\n"
"v_accvgpr_read_b32 v198, a68\n"
"v_mul_f32 v198, v198, v193\n"
"v_accvgpr_write_b32 a68, v198\n"
"v_accvgpr_read_b32 v198, a69\n"
"v_mul_f32 v198, v198, v193\n"
"v_accvgpr_write_b32 a69, v198\n"
"v_accvgpr_read_b32 v198, a70\n"
"v_mul_f32 v198, v198, v193\n"
"v_accvgpr_write_b32 a70, v198\n"
"v_accvgpr_read_b32 v198, a71\n"
"v_mul_f32 v198, v198, v193\n"
"v_accvgpr_write_b32 a71, v198\n"
"v_accvgpr_read_b32 v198, a84\n"
"v_mul_f32 v198, v198, v193\n"
"v_accvgpr_write_b32 a84, v198\n"
"v_accvgpr_read_b32 v198, a85\n"
"v_mul_f32 v198, v198, v193\n"
"v_accvgpr_write_b32 a85, v198\n"
"v_accvgpr_read_b32 v198, a86\n"
"v_mul_f32 v198, v198, v193\n"
"v_accvgpr_write_b32 a86, v198\n"
"v_accvgpr_read_b32 v198, a87\n"
"v_mul_f32 v198, v198, v193\n"
"v_accvgpr_write_b32 a87, v198\n"
"v_accvgpr_read_b32 v198, a100\n"
"v_mul_f32 v198, v198, v193\n"
"v_accvgpr_write_b32 a100, v198\n"
"v_accvgpr_read_b32 v198, a101\n"
"v_mul_f32 v198, v198, v193\n"
"v_accvgpr_write_b32 a101, v198\n"
"v_accvgpr_read_b32 v198, a102\n"
"v_mul_f32 v198, v198, v193\n"
"v_accvgpr_write_b32 a102, v198\n"
"v_accvgpr_read_b32 v198, a103\n"
"v_mul_f32 v198, v198, v193\n"
"v_accvgpr_write_b32 a103, v198\n"
"v_accvgpr_read_b32 v198, a116\n"
"v_mul_f32 v198, v198, v193\n"
"v_accvgpr_write_b32 a116, v198\n"
"v_accvgpr_read_b32 v198, a117\n"
"v_mul_f32 v198, v198, v193\n"
"v_accvgpr_write_b32 a117, v198\n"
"v_accvgpr_read_b32 v198, a118\n"
"v_mul_f32 v198, v198, v193\n"
"v_accvgpr_write_b32 a118, v198\n"
"v_accvgpr_read_b32 v198, a119\n"
"v_mul_f32 v198, v198, v193\n"
"v_accvgpr_write_b32 a119, v198\n"
"v_accvgpr_read_b32 v198, a136\n"
"v_mul_f32 v198, v198, v194\n"
"v_accvgpr_write_b32 a136, v198\n"
"v_accvgpr_read_b32 v198, a137\n"
"v_mul_f32 v198, v198, v194\n"
"v_accvgpr_write_b32 a137, v198\n"
"v_accvgpr_read_b32 v198, a138\n"
"v_mul_f32 v198, v198, v194\n"
"v_accvgpr_write_b32 a138, v198\n"
"v_accvgpr_read_b32 v198, a139\n"
"v_mul_f32 v198, v198, v194\n"
"v_accvgpr_write_b32 a139, v198\n"
"v_accvgpr_read_b32 v198, a8\n"
"v_mul_f32 v198, v198, v194\n"
"v_accvgpr_write_b32 a8, v198\n"
"v_accvgpr_read_b32 v198, a9\n"
"v_mul_f32 v198, v198, v194\n"
"v_accvgpr_write_b32 a9, v198\n"
"v_accvgpr_read_b32 v198, a10\n"
"v_mul_f32 v198, v198, v194\n"
"v_accvgpr_write_b32 a10, v198\n"
"v_accvgpr_read_b32 v198, a11\n"
"v_mul_f32 v198, v198, v194\n"
"v_accvgpr_write_b32 a11, v198\n"
"v_accvgpr_read_b32 v198, a24\n"
"v_mul_f32 v198, v198, v194\n"
"v_accvgpr_write_b32 a24, v198\n"
"v_accvgpr_read_b32 v198, a25\n"
"v_mul_f32 v198, v198, v194\n"
"v_accvgpr_write_b32 a25, v198\n"
"v_accvgpr_read_b32 v198, a26\n"
"v_mul_f32 v198, v198, v194\n"
"v_accvgpr_write_b32 a26, v198\n"
"v_accvgpr_read_b32 v198, a27\n"
"v_mul_f32 v198, v198, v194\n"
"v_accvgpr_write_b32 a27, v198\n"
"v_accvgpr_read_b32 v198, a40\n"
"v_mul_f32 v198, v198, v194\n"
"v_accvgpr_write_b32 a40, v198\n"
"v_accvgpr_read_b32 v198, a41\n"
"v_mul_f32 v198, v198, v194\n"
"v_accvgpr_write_b32 a41, v198\n"
"v_accvgpr_read_b32 v198, a42\n"
"v_mul_f32 v198, v198, v194\n"
"v_accvgpr_write_b32 a42, v198\n"
"v_accvgpr_read_b32 v198, a43\n"
"v_mul_f32 v198, v198, v194\n"
"v_accvgpr_write_b32 a43, v198\n"
"v_accvgpr_read_b32 v198, a56\n"
"v_mul_f32 v198, v198, v194\n"
"v_accvgpr_write_b32 a56, v198\n"
"v_accvgpr_read_b32 v198, a57\n"
"v_mul_f32 v198, v198, v194\n"
"v_accvgpr_write_b32 a57, v198\n"
"v_accvgpr_read_b32 v198, a58\n"
"v_mul_f32 v198, v198, v194\n"
"v_accvgpr_write_b32 a58, v198\n"
"v_accvgpr_read_b32 v198, a59\n"
"v_mul_f32 v198, v198, v194\n"
"v_accvgpr_write_b32 a59, v198\n"
"v_accvgpr_read_b32 v198, a72\n"
"v_mul_f32 v198, v198, v194\n"
"v_accvgpr_write_b32 a72, v198\n"
"v_accvgpr_read_b32 v198, a73\n"
"v_mul_f32 v198, v198, v194\n"
"v_accvgpr_write_b32 a73, v198\n"
"v_accvgpr_read_b32 v198, a74\n"
"v_mul_f32 v198, v198, v194\n"
"v_accvgpr_write_b32 a74, v198\n"
"v_accvgpr_read_b32 v198, a75\n"
"v_mul_f32 v198, v198, v194\n"
"v_accvgpr_write_b32 a75, v198\n"
"v_accvgpr_read_b32 v198, a88\n"
"v_mul_f32 v198, v198, v194\n"
"v_accvgpr_write_b32 a88, v198\n"
"v_accvgpr_read_b32 v198, a89\n"
"v_mul_f32 v198, v198, v194\n"
"v_accvgpr_write_b32 a89, v198\n"
"v_accvgpr_read_b32 v198, a90\n"
"v_mul_f32 v198, v198, v194\n"
"v_accvgpr_write_b32 a90, v198\n"
"v_accvgpr_read_b32 v198, a91\n"
"v_mul_f32 v198, v198, v194\n"
"v_accvgpr_write_b32 a91, v198\n"
"v_accvgpr_read_b32 v198, a104\n"
"v_mul_f32 v198, v198, v194\n"
"v_accvgpr_write_b32 a104, v198\n"
"v_accvgpr_read_b32 v198, a105\n"
"v_mul_f32 v198, v198, v194\n"
"v_accvgpr_write_b32 a105, v198\n"
"v_accvgpr_read_b32 v198, a106\n"
"v_mul_f32 v198, v198, v194\n"
"v_accvgpr_write_b32 a106, v198\n"
"v_accvgpr_read_b32 v198, a107\n"
"v_mul_f32 v198, v198, v194\n"
"v_accvgpr_write_b32 a107, v198\n"
"v_accvgpr_read_b32 v198, a120\n"
"v_mul_f32 v198, v198, v194\n"
"v_accvgpr_write_b32 a120, v198\n"
"v_accvgpr_read_b32 v198, a121\n"
"v_mul_f32 v198, v198, v194\n"
"v_accvgpr_write_b32 a121, v198\n"
"v_accvgpr_read_b32 v198, a122\n"
"v_mul_f32 v198, v198, v194\n"
"v_accvgpr_write_b32 a122, v198\n"
"v_accvgpr_read_b32 v198, a123\n"
"v_mul_f32 v198, v198, v194\n"
"v_accvgpr_write_b32 a123, v198\n"
"v_accvgpr_read_b32 v198, a140\n"
"v_mul_f32 v198, v198, v195\n"
"v_accvgpr_write_b32 a140, v198\n"
"v_accvgpr_read_b32 v198, a141\n"
"v_mul_f32 v198, v198, v195\n"
"v_accvgpr_write_b32 a141, v198\n"
"v_accvgpr_read_b32 v198, a142\n"
"v_mul_f32 v198, v198, v195\n"
"v_accvgpr_write_b32 a142, v198\n"
"v_accvgpr_read_b32 v198, a143\n"
"v_mul_f32 v198, v198, v195\n"
"v_accvgpr_write_b32 a143, v198\n"
"v_accvgpr_read_b32 v198, a12\n"
"v_mul_f32 v198, v198, v195\n"
"v_accvgpr_write_b32 a12, v198\n"
"v_accvgpr_read_b32 v198, a13\n"
"v_mul_f32 v198, v198, v195\n"
"v_accvgpr_write_b32 a13, v198\n"
"v_accvgpr_read_b32 v198, a14\n"
"v_mul_f32 v198, v198, v195\n"
"v_accvgpr_write_b32 a14, v198\n"
"v_accvgpr_read_b32 v198, a15\n"
"v_mul_f32 v198, v198, v195\n"
"v_accvgpr_write_b32 a15, v198\n"
"v_accvgpr_read_b32 v198, a28\n"
"v_mul_f32 v198, v198, v195\n"
"v_accvgpr_write_b32 a28, v198\n"
"v_accvgpr_read_b32 v198, a29\n"
"v_mul_f32 v198, v198, v195\n"
"v_accvgpr_write_b32 a29, v198\n"
"v_accvgpr_read_b32 v198, a30\n"
"v_mul_f32 v198, v198, v195\n"
"v_accvgpr_write_b32 a30, v198\n"
"v_accvgpr_read_b32 v198, a31\n"
"v_mul_f32 v198, v198, v195\n"
"v_accvgpr_write_b32 a31, v198\n"
"v_accvgpr_read_b32 v198, a44\n"
"v_mul_f32 v198, v198, v195\n"
"v_accvgpr_write_b32 a44, v198\n"
"v_accvgpr_read_b32 v198, a45\n"
"v_mul_f32 v198, v198, v195\n"
"v_accvgpr_write_b32 a45, v198\n"
"v_accvgpr_read_b32 v198, a46\n"
"v_mul_f32 v198, v198, v195\n"
"v_accvgpr_write_b32 a46, v198\n"
"v_accvgpr_read_b32 v198, a47\n"
"v_mul_f32 v198, v198, v195\n"
"v_accvgpr_write_b32 a47, v198\n"
"v_accvgpr_read_b32 v198, a60\n"
"v_mul_f32 v198, v198, v195\n"
"v_accvgpr_write_b32 a60, v198\n"
"v_accvgpr_read_b32 v198, a61\n"
"v_mul_f32 v198, v198, v195\n"
"v_accvgpr_write_b32 a61, v198\n"
"v_accvgpr_read_b32 v198, a62\n"
"v_mul_f32 v198, v198, v195\n"
"v_accvgpr_write_b32 a62, v198\n"
"v_accvgpr_read_b32 v198, a63\n"
"v_mul_f32 v198, v198, v195\n"
"v_accvgpr_write_b32 a63, v198\n"
"v_accvgpr_read_b32 v198, a76\n"
"v_mul_f32 v198, v198, v195\n"
"v_accvgpr_write_b32 a76, v198\n"
"v_accvgpr_read_b32 v198, a77\n"
"v_mul_f32 v198, v198, v195\n"
"v_accvgpr_write_b32 a77, v198\n"
"v_accvgpr_read_b32 v198, a78\n"
"v_mul_f32 v198, v198, v195\n"
"v_accvgpr_write_b32 a78, v198\n"
"v_accvgpr_read_b32 v198, a79\n"
"v_mul_f32 v198, v198, v195\n"
"v_accvgpr_write_b32 a79, v198\n"
"v_accvgpr_read_b32 v198, a92\n"
"v_mul_f32 v198, v198, v195\n"
"v_accvgpr_write_b32 a92, v198\n"
"v_accvgpr_read_b32 v198, a93\n"
"v_mul_f32 v198, v198, v195\n"
"v_accvgpr_write_b32 a93, v198\n"
"v_accvgpr_read_b32 v198, a94\n"
"v_mul_f32 v198, v198, v195\n"
"v_accvgpr_write_b32 a94, v198\n"
"v_accvgpr_read_b32 v198, a95\n"
"v_mul_f32 v198, v198, v195\n"
"v_accvgpr_write_b32 a95, v198\n"
"v_accvgpr_read_b32 v198, a108\n"
"v_mul_f32 v198, v198, v195\n"
"v_accvgpr_write_b32 a108, v198\n"
"v_accvgpr_read_b32 v198, a109\n"
"v_mul_f32 v198, v198, v195\n"
"v_accvgpr_write_b32 a109, v198\n"
"v_accvgpr_read_b32 v198, a110\n"
"v_mul_f32 v198, v198, v195\n"
"v_accvgpr_write_b32 a110, v198\n"
"v_accvgpr_read_b32 v198, a111\n"
"v_mul_f32 v198, v198, v195\n"
"v_accvgpr_write_b32 a111, v198\n"
"v_accvgpr_read_b32 v198, a124\n"
"v_mul_f32 v198, v198, v195\n"
"v_accvgpr_write_b32 a124, v198\n"
"v_accvgpr_read_b32 v198, a125\n"
"v_mul_f32 v198, v198, v195\n"
"v_accvgpr_write_b32 a125, v198\n"
"v_accvgpr_read_b32 v198, a126\n"
"v_mul_f32 v198, v198, v195\n"
"v_accvgpr_write_b32 a126, v198\n"
"v_accvgpr_read_b32 v198, a127\n"
"v_mul_f32 v198, v198, v195\n"
"v_accvgpr_write_b32 a127, v198\n"
"s_mov_b32 s95, 0\n"
"s_nop 1\n"
"s_cmp_eq_u32 s97, 1\n"
"s_cbranch_scc1 RBr1_%=\n"
"s_cmp_eq_u32 s97, 2\n"
"s_cbranch_scc1 RBr2_%=\n"
"s_cmp_eq_u32 s97, 3\n"
"s_cbranch_scc1 RBr3_%=\n"
"s_cmp_eq_u32 s97, 4\n"
"s_cbranch_scc1 RBr4_%=\n"
"s_cmp_eq_u32 s97, 5\n"
"s_cbranch_scc1 RBr5_%=\n"
"s_cmp_eq_u32 s97, 6\n"
"s_cbranch_scc1 RBr6_%=\n"
"s_cmp_eq_u32 s97, 7\n"
"s_cbranch_scc1 RBr7_%=\n"
"END_%=:\n"
  :: "s"(__builtin_amdgcn_kernarg_segment_ptr()),"s"(blockIdx.x),"v"(threadIdx.x),"s"((unsigned)(size_t)lds) : "v1","v2","v3","v4","v5","v6","v7","v8","v9","v10","v11","v12","v13","v14","v15","v16","v17","v18","v19","v20","v21","v22","v23","v24","v25","v26","v27","v28","v29","v30","v31","v32","v33","v34","v35","v36","v37","v38","v39","v40","v41","v42","v43","v44","v45","v46","v47","v48","v49","v50","v51","v52","v53","v54","v55","v56","v57","v58","v59","v60","v61","v62","v63","v64","v65","v66","v67","v68","v69","v70","v71","v72","v73","v74","v75","v76","v77","v78","v79","v80","v81","v82","v83","v84","v85","v86","v87","v88","v89","v90","v91","v92","v93","v94","v95","v96","v97","v98","v99","v100","v101","v102","v103","v104","v105","v106","v107","v108","v109","v110","v111","v112","v113","v114","v115","v116","v117","v118","v119","v120","v121","v122","v123","v124","v125","v126","v127","v128","v129","v130","v131","v132","v133","v134","v135","v136","v137","v138","v139","v140","v141","v142","v143","v144","v145","v146","v147","v148","v149","v150","v151","v152","v153","v154","v155","v156","v157","v158","v159","v160","v161","v162","v163","v164","v165","v166","v167","v168","v169","v170","v171","v172","v173","v174","v175","v176","v177","v178","v179","v180","v181","v182","v183","v184","v185","v186","v187","v188","v189","v190","v191","v192","v193","v194","v195","v196","v197","v198","v199","v200","v201","v202","v203","v204","v205","v206","v207","v208","v209","v210","v211","v212","v213","v214","v215","v216","v217","v218","v219","v220","v221","v222","v223","v224","v225","v226","v227","v228","v229","v230","v231","v232","v233","v234","v235","v236","v237","v238","v239","v240","v241","v242","v243","v244","v245","v246","v247","v248","v249","v250","v251","v252","v253","v254","v255","a0","a1","a2","a3","a4","a5","a6","a7","a8","a9","a10","a11","a12","a13","a14","a15","a16","a17","a18","a19","a20","a21","a22","a23","a24","a25","a26","a27","a28","a29","a30","a31","a32","a33","a34","a35","a36","a37","a38","a39","a40","a41","a42","a43","a44","a45","a46","a47","a48","a49","a50","a51","a52","a53","a54","a55","a56","a57","a58","a59","a60","a61","a62","a63","a64","a65","a66","a67","a68","a69","a70","a71","a72","a73","a74","a75","a76","a77","a78","a79","a80","a81","a82","a83","a84","a85","a86","a87","a88","a89","a90","a91","a92","a93","a94","a95","a96","a97","a98","a99","a100","a101","a102","a103","a104","a105","a106","a107","a108","a109","a110","a111","a112","a113","a114","a115","a116","a117","a118","a119","a120","a121","a122","a123","a124","a125","a126","a127","a128","a129","a130","a131","a132","a133","a134","a135","a136","a137","a138","a139","a140","a141","a142","a143","a144","a145","a146","a147","a148","a149","a150","a151","a152","a153","a154","a155","a156","a157","a158","a159","a160","a161","a162","a163","a164","a165","a166","a167","a168","a169","a170","a171","a172","a173","a174","a175","a176","a177","a178","a179","a180","a181","a182","a183","a184","a185","a186","a187","a188","a189","a190","a191","a192","a193","a194","a195","a196","a197","a198","a199","a200","a201","a202","a203","a204","a205","a206","a207","a208","a209","a210","a211","a212","a213","a214","a215","a216","a217","a218","a219","a220","a221","a222","a223","a224","a225","a226","a227","a228","a229","a230","a231","a232","a233","a234","a235","a236","a237","a238","a239","a240","a241","a242","a243","a244","a245","a246","a247","a248","a249","a250","a251","a252","a253","a254","a255","s4","s5","s6","s7","s8","s9","s10","s11","s12","s13","s14","s15","s16","s17","s18","s19","s20","s21","s22","s23","s24","s25","s26","s27","s28","s29","s30","s31","s34","s35","s36","s37","s38","s39","s40","s41","s42","s43","s44","s45","s46","s47","s48","s49","s50","s51","s52","s53","s54","s55","s56","s57","s58","s59","s60","s61","s62","s63","s64","s65","s66","s67","s68","s69","s70","s71","s72","s73","s74","s75","s76","s77","s78","s79","s80","s81","s82","s83","s84","s85","s86","s87","s88","s89","s90","s91","s92","s93","s94","s95","s96","s97","s98","s99","vcc","scc","memory");
}
extern "C" void kernel_launch(void* const* d_in, const int* in_sizes, int n_in, void* d_out, int out_size, void* d_ws, size_t ws_size, hipStream_t stream){
  k_fused<<<256,256,0,stream>>>(d_ws,d_in[0],d_in[3],d_in[6],d_in[7],d_in[1],d_in[2],d_in[4],d_in[8],d_in[9],d_out);
}
```
